```python
import torch

def _softmax(x, axis=-1):
    return torch.softmax(x, dim=axis)
def _gelu(x):
    return torch.nn.functional.gelu(x, approximate="tanh")
def _silu(x):
    return torch.nn.functional.silu(x)
def _sigmoid(x):
    return torch.sigmoid(x)
def _layernorm(x, axis=-1, eps=1e-5):
    m = x.mean(dim=axis, keepdim=True)
    v = x.var(dim=axis, keepdim=True, unbiased=False)
    return (x - m) / torch.sqrt(v + eps)
def _scatter_add(tgt, idx, src, axis=0):
    return tgt.clone().index_add_(axis, idx, src)
def _segment_sum(src, seg, n):
    out = torch.zeros((n,) + src.shape[1:], dtype=src.dtype)
    return out.index_add_(0, seg, src)
def _pad(x, pw):
    flat = tuple(v for pair in reversed(pw) for v in pair)
    return torch.nn.functional.pad(x, flat)

kernel_name = 'recurrent_scan__sliding_block_cached__swiglu_ffn__prompt'

def setup_inputs():

    _w = (lambda a: (lambda t: t.to(torch.bfloat16) if t.is_floating_point() else t)(torch.as_tensor(a)))
    def _A_setup():
        B, Sq, D = 1, 2048, 640
        Hq, Hkv, Sk, Dh = 32, 32, 2048, 128
        assert Hq % Hkv == 0, (Hq, Hkv)
        return {
            "x": _w((lambda *s: torch.randn(*s, dtype=torch.bfloat16) * 0.05)(B, Sq, D)),
            "k_cache": _w((lambda *s: torch.randn(*s, dtype=torch.bfloat16) * 0.8)(B, Hkv, Sk, Dh)),
            "v_cache": _w((lambda *s: torch.randn(*s, dtype=torch.bfloat16) * 0.2)(B, Hkv, Sk, Dh)),
            "wq": _w((lambda *s: torch.randn(*s, dtype=torch.bfloat16) * 0.8)(D, Hq * Dh) * (D ** -0.5)),
            "wo": _w((lambda *s: torch.randn(*s, dtype=torch.bfloat16) * 0.2)(Hq * Dh, D) * ((Hq * Dh) ** -0.5)),
        }
    _A_in = _A_setup()

    def _B_setup():
        B, S, D, F = 1, 2048, 640, 1728
        return {
            "x": _w((lambda *s: torch.randn(*s, dtype=torch.bfloat16) * 0.05)(B, S, D)),
            "w_gate": _w((lambda *s: torch.randn(*s, dtype=torch.bfloat16) * 0.1)(D, F)),
            "w_up": _w((lambda *s: torch.randn(*s, dtype=torch.bfloat16) * 0.1)(D, F)),
            "w_down": _w((lambda *s: torch.randn(*s, dtype=torch.bfloat16) * 0.1)(F, D)),
        }
    _B_in = _B_setup()

    return {
        "A_x": _A_in["x"],
        "A_k_cache": _A_in["k_cache"],
        "A_v_cache": _A_in["v_cache"],
        "A_wq": _A_in["wq"],
        "A_wo": _A_in["wo"],
        "B_x": _B_in["x"],
        "B_w_gate": _B_in["w_gate"],
        "B_w_up": _B_in["w_up"],
        "B_w_down": _B_in["w_down"],
    }

def reference(A_x, A_k_cache, A_v_cache, A_wq, A_wo, B_x, B_w_gate, B_w_up, B_w_down):
    def _A_ref(x, k_cache, v_cache, wq, wo):
        B, Sq, D = x.shape
        Hq, Dh = 32, 128
        Hkv, Sk = k_cache.shape[1], k_cache.shape[2]
        W = min(4096, max(256, Sk // 4))
        rep = Hq // Hkv
        q = (lambda x, *d: x.permute(*d))((x @ wq).reshape(B, Sq, Hq, Dh), 0, 2, 1, 3)
        k = (lambda x, n, axis: torch.repeat_interleave(x, n, dim=axis))(k_cache[:, :, Sk - W:, :], rep, axis=1)
        v = (lambda x, n, axis: torch.repeat_interleave(x, n, dim=axis))(v_cache[:, :, Sk - W:, :], rep, axis=1)
        scores = torch.einsum("bhqd,bhkd->bhqk", q, k) * (Dh ** -0.5)
        p = _softmax(scores, axis=-1)
        o = torch.einsum("bhqk,bhkd->bhqd", p, v)
        return (lambda x, *d: x.permute(*d))(o, 0, 2, 1, 3).reshape(B, Sq, Hq * Dh) @ wo

    def _B_ref(x, w_gate, w_up, w_down):
        g = _silu(x @ w_gate)
        u = x @ w_up
        return (g * u) @ w_down

    x = A_x
    xn = _layernorm(x, axis=-1)
    s = _A_ref(xn, A_k_cache, A_v_cache, A_wq, A_wo)
    g = _silu(_B_ref(xn, B_w_gate, B_w_up, B_w_down))
    return x + s * g
```

```cpp
#include <hip/hip_runtime.h>
#include <hip/hip_bf16.h>
#include <stdint.h>
namespace naive {
typedef unsigned short u16;
__device__ __forceinline__ float bf2f(u16 v){ return __uint_as_float(((unsigned)v)<<16); }
__device__ __forceinline__ u16 f2bf(float f){ unsigned u=__float_as_uint(f); u += 0x7fffu + ((u>>16)&1u); return (u16)(u>>16); }
__device__ __forceinline__ float rb(float f){ return bf2f(f2bf(f)); }
constexpr int S=2048, D=640, H=32, DH=128, SK=2048, W=512, F=1728;

__global__ void ln_k(const u16* x, u16* xn){
  int r=blockIdx.x; int t=threadIdx.x;
  float v[10]; float s=0;
  for(int i=0;i<10;i++){ v[i]=bf2f(x[r*D+t+64*i]); s+=v[i]; }
  for(int o=32;o;o>>=1) s+=__shfl_xor(s,o);
  float m=s/D; float q=0;
  for(int i=0;i<10;i++){ float d=v[i]-m; q+=d*d; }
  for(int o=32;o;o>>=1) q+=__shfl_xor(q,o);
  float mb=rb(m), vb=rb(q/D); float sd=rb(sqrtf(rb(vb+1e-5f)));
  for(int i=0;i<10;i++) xn[r*D+t+64*i]=f2bf(rb(v[i]-mb)/sd);
}
__global__ void gemm_k(const u16* A,const u16* B,u16* C,int M,int N,int K){
  __shared__ float As[32][33], Bs[32][33];
  int tx=threadIdx.x, ty=threadIdx.y; int row=blockIdx.y*32+ty, col=blockIdx.x*32+tx; float acc=0;
  for(int k0=0;k0<K;k0+=32){
    As[ty][tx]=bf2f(A[(size_t)row*K+k0+tx]); Bs[ty][tx]=bf2f(B[(size_t)(k0+ty)*N+col]); __syncthreads();
    for(int k=0;k<32;k++) acc+=As[ty][k]*Bs[k][tx]; __syncthreads();
  }
  C[(size_t)row*N+col]=f2bf(acc);
}
__global__ void attn_k(const u16* q,const u16* kc,const u16* vc,u16* o){
  int r=blockIdx.x, h=blockIdx.y, t=threadIdx.x;
  __shared__ float qs[128], p[512], red[128];
  qs[t]=bf2f(q[(size_t)r*4096+h*128+t]); __syncthreads();
  const u16* K=kc+((size_t)h*SK+(SK-W))*DH; const u16* V=vc+((size_t)h*SK+(SK-W))*DH;
  float mx=-1e30f;
  for(int j=t;j<W;j+=128){ float s=0; for(int d=0;d<128;d++) s+=qs[d]*bf2f(K[j*128+d]); s=rb(rb(s)*rb(0.08838834764831845f)); p[j]=s; mx=fmaxf(mx,s);}
  red[t]=mx; __syncthreads(); for(int o2=64;o2;o2>>=1){ if(t<o2) red[t]=fmaxf(red[t],red[t+o2]); __syncthreads(); } mx=red[0]; __syncthreads();
  float sm=0; for(int j=t;j<W;j+=128){ float e=__expf(p[j]-mx); p[j]=e; sm+=e; }
  red[t]=sm; __syncthreads(); for(int o2=64;o2;o2>>=1){ if(t<o2) red[t]+=red[t+o2]; __syncthreads(); } sm=red[0]; __syncthreads();
  for(int j=t;j<W;j+=128) p[j]=rb(p[j]/sm); __syncthreads();
  float acc=0; for(int j=0;j<W;j++) acc+=p[j]*bf2f(V[j*128+t]);
  o[(size_t)r*4096+h*128+t]=f2bf(acc);
}
__global__ void glu_k(const u16* g,const u16* u,u16* h,int n){ int i=blockIdx.x*256+threadIdx.x; if(i<n){ float a=bf2f(g[i]); float s=rb(a/(1.f+__expf(-a))); h[i]=f2bf(s*bf2f(u[i])); } }
__global__ void fin_k(const u16* x,const u16* s,const u16* f,u16* o,int n){ int i=blockIdx.x*256+threadIdx.x; if(i<n){ float a=bf2f(f[i]); float g=rb(a/(1.f+__expf(-a))); o[i]=f2bf(bf2f(x[i])+rb(bf2f(s[i])*g)); } }

static void run(void* const* d_in, void* d_out, void* d_ws, hipStream_t stream){
  const u16* x=(const u16*)d_in[0]; const u16* kc=(const u16*)d_in[1]; const u16* vc=(const u16*)d_in[2];
  const u16* wq=(const u16*)d_in[3]; const u16* wo=(const u16*)d_in[4];
  const u16* wg=(const u16*)d_in[6]; const u16* wu=(const u16*)d_in[7]; const u16* wd=(const u16*)d_in[8];
  u16* ws=(u16*)d_ws; u16* xn=ws; u16* q=xn+S*D; u16* o=q+S*4096; u16* g=o+S*4096; u16* u=g+S*F; u16* hh=u+S*F; u16* s=hh+S*F; u16* f=s+S*D;
  ln_k<<<S,64,0,stream>>>(x,xn);
  gemm_k<<<dim3(4096/32,S/32),dim3(32,32),0,stream>>>(xn,wq,q,S,4096,D);
  attn_k<<<dim3(S,H),128,0,stream>>>(q,kc,vc,o);
  gemm_k<<<dim3(D/32,S/32),dim3(32,32),0,stream>>>(o,wo,s,S,D,4096);
  gemm_k<<<dim3(F/32,S/32),dim3(32,32),0,stream>>>(xn,wg,g,S,F,D);
  gemm_k<<<dim3(F/32,S/32),dim3(32,32),0,stream>>>(xn,wu,u,S,F,D);
  glu_k<<<(S*F+255)/256,256,0,stream>>>(g,u,hh,S*F);
  gemm_k<<<dim3(D/32,S/32),dim3(32,32),0,stream>>>(hh,wd,f,S,D,F);
  fin_k<<<(S*D+255)/256,256,0,stream>>>(x,s,f,(u16*)d_out,S*D);
}
}
extern "C" void kernel_launch(void* const* d_in, const int* in_sizes, int n_in, void* d_out, int out_size, void* d_ws, size_t ws_size, hipStream_t stream){
  naive::run(d_in,d_out,d_ws,stream);
}
```

```cpp
#include <hip/hip_runtime.h>
#include <hip/hip_bf16.h>

__global__ __launch_bounds__(256, 1) __attribute__((aligned(4096))) void fused_kernel(const void* x, const void* kc, const void* vc, const void* wq, const void* wo,
                                                       const void* wg, const void* wu, const void* wd, void* ws, void* out) {
  __shared__ __attribute__((aligned(16))) char smem[163840];
  asm volatile(
"s_load_dwordx16 s[28:43], %0, 0x0\n"
"s_load_dwordx4 s[44:47], %0, 0x40\n"
"s_mov_b32 s73, %1\n"
"v_readfirstlane_b32 s72, %2\n"
"v_mbcnt_lo_u32_b32 v253, -1, 0\n"
"v_mbcnt_hi_u32_b32 v253, -1, v253\n"
"s_lshr_b32 s72, s72, 6\n"
"s_and_b32 s74, s73, 3\n"
"s_lshr_b32 s75, s73, 3\n"
"s_lshl_b32 s92, s75, 2\n"
"s_add_u32 s92, s92, s72\n"
"s_lshl_b32 s92, s92, 7\n"
"v_lshl_add_u32 v252, v253, 14, s92\n"
"s_getpc_b64 s[4:5]\n"
"L_pc0_%=:\n"
"s_mov_b32 s6, L_end_%=-L_pc0_%=\n"
"s_and_b32 s5, s5, 0xffff\n"
"s_mov_b32 s7, 0x20000\n"
"buffer_load_dword v247, v252, s[4:7], 0 offen\n"
"s_and_b32 s11, s75, 7\n"
"s_and_b32 s13, s73, 3\n"
"s_lshl_b32 s13, s13, 3\n"
"s_add_u32 s13, s13, s11\n"
"s_lshl_b32 s77, s72, 11\n"
"v_bfe_u32 v200, v253, 2, 3\n"
"v_lshrrev_b32 v201, 5, v253\n"
"v_lshlrev_b32 v201, 6, v201\n"
"v_and_b32 v202, 3, v253\n"
"v_lshl_add_u32 v201, v202, 4, v201\n"
"s_lshl_b32 s92, s72, 4\n"
"v_add_u32 v202, s92, v200\n"
"v_lshl_add_u32 v224, v202, 13, v201\n"
"s_mov_b32 s0, 0xffff0000\n"
"s_and_b32 s92, s73, 4\n"
"s_lshr_b32 s93, s75, 3\n"
"s_add_u32 s93, s93, s92\n"
"s_lshl_b32 s93, s93, 4\n"
"s_add_u32 s93, s93, s74\n"
"s_lshl_b32 s94, s72, 2\n"
"s_add_u32 s26, s93, s94\n"
"s_xor_b32 s94, s26, 0\n"
"s_mul_i32 s84, s94, 20480\n"
"s_xor_b32 s94, s26, 1\n"
"s_mul_i32 s85, s94, 20480\n"
"s_xor_b32 s94, s26, 2\n"
"s_mul_i32 s86, s94, 20480\n"
"s_xor_b32 s94, s26, 3\n"
"s_mul_i32 s87, s94, 20480\n"
"v_lshrrev_b32 v203, 4, v253\n"
"v_and_b32 v204, 7, v253\n"
"v_xor_b32 v204, v203, v204\n"
"v_lshrrev_b32 v203, 3, v253\n"
"v_mul_u32_u24 v203, 1280, v203\n"
"v_lshl_add_u32 v250, v204, 4, v203\n"
"v_xor_b32 v251, 64, v250\n"
"v_add_u32 v251, 10240, v251\n"
"s_add_u32 s92, s92, s72\n"
"s_lshl_b32 s92, s92, 4\n"
"s_add_u32 s27, s92, s74\n"
"s_bfe_u32 s94, s75, 0x20001\n"
"s_lshl_b32 s94, s94, 2\n"
"s_add_u32 s93, s93, s94\n"
"s_lshl_b32 s93, s93, 1\n"
"s_and_b32 s94, s75, 1\n"
"s_add_u32 s1, s93, s94\n"
"s_waitcnt lgkmcnt(0)\n"
"s_mov_b64 s[64:65], s[46:47]\n"
"s_mov_b64 s[60:61], s[44:45]\n"
"s_mov_b64 s[56:57], s[42:43]\n"
"s_mov_b64 s[52:53], s[36:37]\n"
"s_mov_b64 s[48:49], s[32:33]\n"
"s_mov_b64 s[44:45], s[30:31]\n"
"s_mov_b64 s[32:33], s[34:35]\n"
"s_mov_b64 s[36:37], s[38:39]\n"
"s_lshl_b32 s92, s13, 8\n"
"s_add_u32 s32, s32, s92\n"
"s_addc_u32 s33, s33, 0\n"
"s_and_b32 s33, s33, 0xffff\n"
"s_mov_b32 s34, 5242880\n"
"s_mov_b32 s35, 0x20000\n"
"s_sub_u32 s34, s34, s92\n"
"s_mov_b32 s82, 0\n"
"s_add_u32 m0, s77, 82000\n"
"s_nop 0\n"
"buffer_load_dwordx4 v224, s[32:35], s82 offen lds\n"
"s_addk_i32 m0, 896\n"
"s_nop 0\n"
"buffer_load_dwordx4 v224, s[32:35], s82 offen offset:128 lds\n"
"s_mov_b32 s82, 65536\n"
"s_addk_i32 m0, 7304\n"
"s_nop 0\n"
"buffer_load_dwordx4 v224, s[32:35], s82 offen lds\n"
"s_addk_i32 m0, 896\n"
"s_nop 0\n"
"buffer_load_dwordx4 v224, s[32:35], s82 offen offset:128 lds\n"
"s_and_b32 s29, s29, 0xffff\n"
"s_mov_b32 s30, 2621440\n"
"s_mov_b32 s31, 0x20000\n"
"s_lshl_b32 s97, s1, 2\n"
"s_add_u32 s97, s97, s72\n"
"s_mul_i32 s97, s97, 2560\n"
"v_lshrrev_b32 v172, 5, v253\n"
"v_mul_u32_u24 v172, 1280, v172\n"
"v_and_b32 v173, 31, v253\n"
"v_lshl_add_u32 v172, v173, 4, v172\n"
"v_cmp_gt_u32 vcc, 16, v173\n"
"v_mov_b32 v173, 0x40000000\n"
"s_nop 0\n"
"v_cndmask_b32 v173, v173, v172, vcc\n"
"buffer_load_dwordx4 v[160:163], v172, s[28:31], s97 offen\n"
"buffer_load_dwordx4 v[164:167], v172, s[28:31], s97 offen offset:512\n"
"buffer_load_dwordx4 v[168:171], v173, s[28:31], s97 offen offset:1024\n"
"s_and_b32 s61, s61, 0xffff\n"
"s_mov_b32 s62, 43909120\n"
"s_mov_b32 s63, 0x20000\n"
"v_mov_b32 v231, 0\n"
"buffer_load_dwordx2 v[232:233], v231, s[60:63], 0 offen sc1\n"
"s_add_u32 m0, s77, 16400\n"
"s_nop 0\n"
"buffer_load_dwordx4 v250, s[28:31], s84 offen lds\n"
"s_addk_i32 m0, 1024\n"
"s_nop 0\n"
"buffer_load_dwordx4 v251, s[28:31], s84 offen lds\n"
"s_addk_i32 m0, 7168\n"
"s_nop 0\n"
"buffer_load_dwordx4 v250, s[28:31], s85 offen lds\n"
"s_addk_i32 m0, 1024\n"
"s_nop 0\n"
"buffer_load_dwordx4 v251, s[28:31], s85 offen lds\n"
"s_addk_i32 m0, 7168\n"
"s_nop 0\n"
"buffer_load_dwordx4 v250, s[28:31], s86 offen lds\n"
"s_addk_i32 m0, 1024\n"
"s_nop 0\n"
"buffer_load_dwordx4 v251, s[28:31], s86 offen lds\n"
"s_addk_i32 m0, 7168\n"
"s_nop 0\n"
"buffer_load_dwordx4 v250, s[28:31], s87 offen lds\n"
"s_addk_i32 m0, 1024\n"
"s_nop 0\n"
"buffer_load_dwordx4 v251, s[28:31], s87 offen lds\n"
"s_lshl_b32 s6, s13, 8\n"
"s_sub_u32 s4, s32, s6\n"
"s_subb_u32 s5, s33, 0\n"
"s_mov_b32 s6, 2621440\n"
"s_mov_b32 s7, 0x20000\n"
"s_lshl_b32 s94, s73, 2\n"
"s_add_u32 s94, s94, s72\n"
"s_mul_i32 s94, s94, 1536\n"
"s_add_u32 s94, s94, 1048576\n"
"v_lshlrev_b32 v236, 7, v253\n"
"v_cmp_gt_u32 vcc, 12, v253\n"
"v_mov_b32 v237, 0x40000000\n"
"s_nop 0\n"
"v_cndmask_b32 v236, v237, v236, vcc\n"
"buffer_load_dword v237, v236, s[4:7], s94 offen\n"
"v_and_b32 v205, 15, v253\n"
"v_lshlrev_b32 v205, 4, v205\n"
"v_bfe_u32 v206, v253, 4, 1\n"
"v_mul_u32_u24 v206, 8200, v206\n"
"v_lshrrev_b32 v207, 5, v253\n"
"v_lshlrev_b32 v207, 11, v207\n"
"v_add3_u32 v219, v205, v206, v207\n"
"v_add_u32 v220, 49200, v219\n"
"v_add_u32 v221, 98400, v219\n"
"v_lshlrev_b32 v205, 3, v205\n"
"v_bfe_u32 v206, v253, 1, 3\n"
"v_lshrrev_b32 v207, 4, v253\n"
"v_xor_b32 v206, v206, v207\n"
"v_lshl_add_u32 v205, v206, 4, v205\n"
"v_add_u32 v205, s77, v205\n"
"v_xor_b32 v213, 64, v205\n"
"v_add_u32 v212, 16400, v205\n"
"v_add_u32 v213, 16400, v213\n"
"s_lshl_b32 s96, s72, 9\n"
"v_add_u32 v211, s96, v219\n"
"v_add_u32 v214, s96, v220\n"
"v_add_u32 v215, s96, v221\n"
"v_and_b32 v205, 3, v253\n"
"v_cmp_eq_u32 vcc, 0, v205\n"
"v_mov_b32 v207, 0x3f803f80\n"
"s_nop 0\n"
"v_cndmask_b32 v205, 0, v207, vcc\n"
"v_accvgpr_write_b32 a144, v205\n"
"v_accvgpr_write_b32 a145, v205\n"
"v_accvgpr_write_b32 a146, v205\n"
"v_accvgpr_write_b32 a147, v205\n"
"v_lshlrev_b32 v216, 4, v253\n"
"s_mov_b32 s82, 524288\n"
"s_add_u32 m0, s77, 98400\n"
"s_nop 0\n"
"buffer_load_dwordx4 v224, s[32:35], s82 offen lds\n"
"s_addk_i32 m0, 896\n"
"s_nop 0\n"
"buffer_load_dwordx4 v224, s[32:35], s82 offen offset:128 lds\n"
"s_mov_b32 s82, 589824\n"
"s_addk_i32 m0, 7304\n"
"s_nop 0\n"
"buffer_load_dwordx4 v224, s[32:35], s82 offen lds\n"
"s_addk_i32 m0, 896\n"
"s_nop 0\n"
"buffer_load_dwordx4 v224, s[32:35], s82 offen offset:128 lds\n"
"s_mov_b64 exec, 0\n"
"s_cmp_eq_u32 s72, 0\n"
"s_cbranch_scc1 L_st1_%=\n"
"s_cmp_eq_u32 s72, 1\n"
"s_cbranch_scc1 L_st2_%=\n"
"s_cmp_eq_u32 s72, 2\n"
"s_cbranch_scc1 L_st3_%=\n"
"s_branch L_st4_%=\n"
"L_hd0_%=:\n"
"s_mov_b64 exec, -1\n"
"s_nop 4\n"
"s_waitcnt vmcnt(17)\n"
"s_barrier\n"
"ds_read_b64_tr_b16 a[148:149], v220 offset:32800\n"
"ds_read_b64_tr_b16 a[150:151], v220 offset:33056\n"
"ds_read_b64_tr_b16 a[152:153], v220 offset:36896\n"
"ds_read_b64_tr_b16 a[154:155], v220 offset:37152\n"
"ds_read_b64_tr_b16 a[156:157], v220 offset:32808\n"
"ds_read_b64_tr_b16 a[158:159], v220 offset:33064\n"
"ds_read_b64_tr_b16 a[160:161], v220 offset:36904\n"
"ds_read_b64_tr_b16 a[162:163], v220 offset:37160\n"
"ds_read_b64_tr_b16 a[164:165], v220 offset:33312\n"
"ds_read_b64_tr_b16 a[166:167], v220 offset:33568\n"
"ds_read_b64_tr_b16 a[168:169], v220 offset:37408\n"
"ds_read_b64_tr_b16 a[170:171], v220 offset:37664\n"
"ds_read_b64_tr_b16 a[172:173], v220 offset:33320\n"
"ds_read_b64_tr_b16 a[174:175], v220 offset:33576\n"
"ds_read_b64_tr_b16 a[176:177], v220 offset:37416\n"
"ds_read_b64_tr_b16 a[178:179], v220 offset:37672\n"
"s_waitcnt vmcnt(11)\n"
"ds_read_b128 a[0:3], v212 offset:0\n"
"ds_read_b128 a[16:19], v213 offset:0\n"
"s_waitcnt vmcnt(9)\n"
"ds_read_b128 a[4:7], v212 offset:8192\n"
"ds_read_b128 a[20:23], v213 offset:8192\n"
"s_waitcnt lgkmcnt(0)\n"
"v_mfma_f32_16x16x32_bf16 v[0:3], a[148:151], a[0:3], 0\n"
"s_add_u32 m0, s77, 49072\n"
"ds_read_b64_tr_b16 a[104:105], v214 offset:32800\n"
"ds_read_b64_tr_b16 a[106:107], v214 offset:33056\n"
"ds_read_b64_tr_b16 a[108:109], v214 offset:32808\n"
"ds_read_b64_tr_b16 a[110:111], v214 offset:33064\n"
"v_mfma_f32_16x16x32_bf16 v[0:3], a[152:155], a[16:19], v[0:3]\n"
"s_mov_b32 s82, 1048576\n"
"buffer_load_dwordx4 v250, s[28:31], s84 offen offset:128 lds\n"
"s_addk_i32 m0, 1024\n"
"v_mfma_f32_16x16x32_bf16 v[4:7], a[156:159], a[0:3], 0\n"
"s_waitcnt vmcnt(8)\n"
"buffer_load_dwordx4 v251, s[28:31], s84 offen offset:128 lds\n"
"ds_read_b128 a[8:11], v212 offset:16384\n"
"s_waitcnt vmcnt(7)\n"
"ds_read_b128 a[12:15], v212 offset:24576\n"
"v_mfma_f32_16x16x32_bf16 v[4:7], a[160:163], a[16:19], v[4:7]\n"
"s_addk_i32 m0, 7168\n"
"ds_read_b128 a[24:27], v213 offset:16384\n"
"ds_read_b128 a[28:31], v213 offset:24576\n"
"v_mfma_f32_16x16x32_bf16 v[32:35], a[164:167], a[0:3], 0\n"
"s_lshl_b32 s88, s1, 2\n"
"ds_read_b64_tr_b16 a[180:181], v220 offset:33824\n"
"v_lshlrev_b32 v176, 16, v160\n"
"v_mfma_f32_16x16x32_bf16 v[32:35], a[168:171], a[16:19], v[32:35]\n"
"s_add_u32 s88, s88, s72\n"
"buffer_load_dwordx4 v250, s[28:31], s85 offen offset:128 lds\n"
"v_mfma_f32_16x16x32_bf16 v[36:39], a[172:175], a[0:3], 0\n"
"s_addk_i32 m0, 1024\n"
"ds_read_b64_tr_b16 a[182:183], v220 offset:34080\n"
"v_and_b32 v177, s0, v160\n"
"v_mfma_f32_16x16x32_bf16 v[36:39], a[176:179], a[16:19], v[36:39]\n"
"s_waitcnt lgkmcnt(8)\n"
"ds_read_b64_tr_b16 a[184:185], v220 offset:37920\n"
"v_lshlrev_b32 v178, 16, v161\n"
"v_mfma_f32_16x16x32_bf16 a[96:99], a[104:107], a[144:147], 0\n"
"s_waitcnt lgkmcnt(7)\n"
"buffer_load_dwordx4 v251, s[28:31], s85 offen offset:128 lds\n"
"v_mfma_f32_16x16x32_bf16 a[100:103], a[108:111], a[144:147], 0\n"
"s_addk_i32 m0, 7168\n"
"ds_read_b64_tr_b16 a[186:187], v220 offset:38176\n"
"v_and_b32 v179, s0, v161\n"
"v_mfma_f32_16x16x32_bf16 v[8:11], a[148:151], a[4:7], 0\n"
"s_lshl_b32 s88, s88, 7\n"
"ds_read_b64_tr_b16 a[188:189], v220 offset:33832\n"
"v_lshlrev_b32 v180, 16, v162\n"
"v_mfma_f32_16x16x32_bf16 v[8:11], a[152:155], a[20:23], v[8:11]\n"
"s_add_u32 s88, s88, 262144\n"
"buffer_load_dwordx4 v250, s[28:31], s86 offen offset:128 lds\n"
"v_mfma_f32_16x16x32_bf16 v[12:15], a[156:159], a[4:7], 0\n"
"s_addk_i32 m0, 1024\n"
"ds_read_b64_tr_b16 a[190:191], v220 offset:34088\n"
"v_and_b32 v181, s0, v162\n"
"v_mfma_f32_16x16x32_bf16 v[12:15], a[160:163], a[20:23], v[12:15]\n"
"ds_read_b64_tr_b16 a[192:193], v220 offset:37928\n"
"v_lshlrev_b32 v182, 16, v163\n"
"v_mfma_f32_16x16x32_bf16 v[40:43], a[164:167], a[4:7], 0\n"
"buffer_load_dwordx4 v251, s[28:31], s86 offen offset:128 lds\n"
"v_mfma_f32_16x16x32_bf16 v[40:43], a[168:171], a[20:23], v[40:43]\n"
"s_addk_i32 m0, 7168\n"
"ds_read_b64_tr_b16 a[194:195], v220 offset:38184\n"
"v_and_b32 v183, s0, v163\n"
"v_mfma_f32_16x16x32_bf16 v[44:47], a[172:175], a[4:7], 0\n"
"ds_read_b64_tr_b16 a[196:197], v220 offset:34336\n"
"v_lshlrev_b32 v184, 16, v164\n"
"v_mfma_f32_16x16x32_bf16 v[44:47], a[176:179], a[20:23], v[44:47]\n"
"s_waitcnt lgkmcnt(8)\n"
"buffer_load_dwordx4 v250, s[28:31], s87 offen offset:128 lds\n"
"v_mfma_f32_16x16x32_bf16 v[16:19], a[148:151], a[8:11], 0\n"
"s_addk_i32 m0, 1024\n"
"ds_read_b64_tr_b16 a[198:199], v220 offset:34592\n"
"v_and_b32 v185, s0, v164\n"
"v_lshlrev_b32 v186, 16, v165\n"
"v_mfma_f32_16x16x32_bf16 v[16:19], a[152:155], a[24:27], v[16:19]\n"
"ds_read_b64_tr_b16 a[200:201], v220 offset:38432\n"
"v_and_b32 v187, s0, v165\n"
"v_mfma_f32_16x16x32_bf16 v[20:23], a[156:159], a[8:11], 0\n"
"buffer_load_dwordx4 v251, s[28:31], s87 offen offset:128 lds\n"
"v_lshlrev_b32 v188, 16, v166\n"
"v_mfma_f32_16x16x32_bf16 v[20:23], a[160:163], a[24:27], v[20:23]\n"
"s_add_u32 m0, s77, 114800\n"
"ds_read_b64_tr_b16 a[202:203], v220 offset:38688\n"
"v_and_b32 v189, s0, v166\n"
"v_mfma_f32_16x16x32_bf16 v[48:51], a[164:167], a[8:11], 0\n"
"ds_read_b64_tr_b16 a[204:205], v220 offset:34344\n"
"v_lshlrev_b32 v190, 16, v167\n"
"v_mfma_f32_16x16x32_bf16 v[48:51], a[168:171], a[24:27], v[48:51]\n"
"buffer_load_dwordx4 v224, s[32:35], s82 offen lds\n"
"v_and_b32 v191, s0, v167\n"
"v_mfma_f32_16x16x32_bf16 v[52:55], a[172:175], a[8:11], 0\n"
"s_addk_i32 m0, 896\n"
"ds_read_b64_tr_b16 a[206:207], v220 offset:34600\n"
"v_lshlrev_b32 v192, 16, v168\n"
"v_mfma_f32_16x16x32_bf16 v[52:55], a[176:179], a[24:27], v[52:55]\n"
"ds_read_b64_tr_b16 a[208:209], v220 offset:38440\n"
"v_and_b32 v193, s0, v168\n"
"v_mfma_f32_16x16x32_bf16 v[24:27], a[148:151], a[12:15], 0\n"
"buffer_load_dwordx4 v224, s[32:35], s82 offen offset:128 lds\n"
"v_lshlrev_b32 v194, 16, v169\n"
"s_addk_i32 m0, 7304\n"
"v_mfma_f32_16x16x32_bf16 v[24:27], a[152:155], a[28:31], v[24:27]\n"
"s_mov_b32 s82, 1114112\n"
"buffer_load_dwordx4 v224, s[32:35], s82 offen lds\n"
"v_and_b32 v195, s0, v169\n"
"v_lshlrev_b32 v196, 16, v170\n"
"s_addk_i32 m0, 896\n"
"v_mfma_f32_16x16x32_bf16 v[28:31], a[156:159], a[12:15], 0\n"
"buffer_load_dwordx4 v224, s[32:35], s82 offen offset:128 lds\n"
"v_and_b32 v197, s0, v170\n"
"s_add_u32 m0, s77, 16144\n"
"v_mfma_f32_16x16x32_bf16 v[28:31], a[160:163], a[28:31], v[28:31]\n"
"buffer_load_dwordx4 v250, s[28:31], s84 offen offset:256 lds\n"
"v_lshlrev_b32 v198, 16, v171\n"
"s_addk_i32 m0, 1024\n"
"v_mfma_f32_16x16x32_bf16 v[56:59], a[164:167], a[12:15], 0\n"
"buffer_load_dwordx4 v251, s[28:31], s84 offen offset:256 lds\n"
"v_and_b32 v199, s0, v171\n"
"v_mfma_f32_16x16x32_bf16 v[56:59], a[168:171], a[28:31], v[56:59]\n"
"s_addk_i32 m0, 7168\n"
"ds_read_b64_tr_b16 a[210:211], v220 offset:38696\n"
"v_add_f32 v172, v176, v177\n"
"v_mfma_f32_16x16x32_bf16 v[60:63], a[172:175], a[12:15], 0\n"
"ds_read_b64_tr_b16 a[112:113], v214 offset:36896\n"
"ds_read_b64_tr_b16 a[114:115], v214 offset:37152\n"
"v_add_f32 v173, v178, v179\n"
"v_mfma_f32_16x16x32_bf16 v[60:63], a[176:179], a[28:31], v[60:63]\n"
"s_waitcnt lgkmcnt(6)\n"
"ds_read_b64_tr_b16 a[116:117], v214 offset:36904\n"
"ds_read_b64_tr_b16 a[118:119], v214 offset:37160\n"
"v_add_f32 v172, v172, v180\n"
"v_mfma_f32_16x16x32_bf16 v[64:67], a[180:183], a[0:3], 0\n"
"buffer_load_dwordx4 v250, s[28:31], s85 offen offset:256 lds\n"
"v_add_f32 v173, v173, v181\n"
"v_mfma_f32_16x16x32_bf16 v[64:67], a[184:187], a[16:19], v[64:67]\n"
"s_addk_i32 m0, 1024\n"
"v_add_f32 v172, v172, v182\n"
"v_add_f32 v173, v173, v183\n"
"v_mfma_f32_16x16x32_bf16 v[72:75], a[184:187], a[20:23], 0\n"
"v_add_f32 v172, v172, v184\n"
"v_add_f32 v173, v173, v185\n"
"v_mfma_f32_16x16x32_bf16 v[72:75], a[180:183], a[4:7], v[72:75]\n"
"buffer_load_dwordx4 v251, s[28:31], s85 offen offset:256 lds\n"
"v_mfma_f32_16x16x32_bf16 v[80:83], a[180:183], a[8:11], 0\n"
"s_addk_i32 m0, 7168\n"
"v_add_f32 v172, v172, v186\n"
"v_add_f32 v173, v173, v187\n"
"v_mfma_f32_16x16x32_bf16 v[80:83], a[184:187], a[24:27], v[80:83]\n"
"v_add_f32 v172, v172, v188\n"
"v_add_f32 v173, v173, v189\n"
"v_mfma_f32_16x16x32_bf16 v[88:91], a[184:187], a[28:31], 0\n"
"buffer_load_dwordx4 v250, s[28:31], s86 offen offset:256 lds\n"
"v_mfma_f32_16x16x32_bf16 v[88:91], a[180:183], a[12:15], v[88:91]\n"
"s_waitcnt lgkmcnt(2)\n"
"v_add_f32 v172, v172, v190\n"
"v_add_f32 v173, v173, v191\n"
"v_mfma_f32_16x16x32_bf16 a[96:99], a[112:115], a[144:147], a[96:99]\n"
"s_waitcnt vmcnt(15) lgkmcnt(0)\n"
"ds_read_b128 a[32:35], v212 offset:32800\n"
"ds_read_b128 a[48:51], v213 offset:32800\n"
"v_mfma_f32_16x16x32_bf16 a[100:103], a[116:119], a[144:147], a[100:103]\n"
"s_barrier\n"
"ds_read_b64_tr_b16 a[148:149], v221 offset:0\n"
"v_add_f32 v172, v172, v192\n"
"v_mfma_f32_16x16x32_bf16 v[92:95], a[188:191], a[12:15], 0\n"
"s_addk_i32 m0, 1024\n"
"ds_read_b64_tr_b16 a[150:151], v221 offset:256\n"
"v_add_f32 v173, v173, v193\n"
"v_mfma_f32_16x16x32_bf16 v[92:95], a[192:195], a[28:31], v[92:95]\n"
"s_mov_b32 s82, 1572864\n"
"buffer_load_dwordx4 v251, s[28:31], s86 offen offset:256 lds\n"
"v_add_f32 v172, v172, v194\n"
"v_mfma_f32_16x16x32_bf16 v[84:87], a[192:195], a[24:27], 0\n"
"s_addk_i32 m0, 7168\n"
"ds_read_b64_tr_b16 a[152:153], v221 offset:4096\n"
"v_add_f32 v173, v173, v195\n"
"v_mfma_f32_16x16x32_bf16 v[84:87], a[188:191], a[8:11], v[84:87]\n"
"ds_read_b64_tr_b16 a[154:155], v221 offset:4352\n"
"v_add_f32 v172, v172, v196\n"
"v_mfma_f32_16x16x32_bf16 v[76:79], a[188:191], a[4:7], 0\n"
"s_waitcnt vmcnt(14)\n"
"ds_read_b128 a[36:39], v212 offset:40992\n"
"v_add_f32 v173, v173, v197\n"
"v_mfma_f32_16x16x32_bf16 v[76:79], a[192:195], a[20:23], v[76:79]\n"
"ds_read_b128 a[52:55], v213 offset:40992\n"
"v_add_f32 v172, v172, v198\n"
"v_mfma_f32_16x16x32_bf16 v[68:71], a[192:195], a[16:19], 0\n"
"buffer_load_dwordx4 v250, s[28:31], s87 offen offset:256 lds\n"
"v_add_f32 v173, v173, v199\n"
"v_mfma_f32_16x16x32_bf16 v[68:71], a[188:191], a[0:3], v[68:71]\n"
"s_addk_i32 m0, 1024\n"
"ds_read_b64_tr_b16 a[156:157], v221 offset:8\n"
"v_add_f32 v172, v172, v173\n"
"v_mfma_f32_16x16x32_bf16 v[96:99], a[196:199], a[0:3], 0\n"
"ds_read_b64_tr_b16 a[158:159], v221 offset:264\n"
"v_add_f32_dpp v172, v172, v172 quad_perm:[1,0,3,2] row_mask:0xf bank_mask:0xf\n"
"v_mfma_f32_16x16x32_bf16 v[96:99], a[200:203], a[16:19], v[96:99]\n"
"buffer_load_dwordx4 v251, s[28:31], s87 offen offset:256 lds\n"
"v_add_f32_dpp v172, v172, v172 quad_perm:[2,3,0,1] row_mask:0xf bank_mask:0xf\n"
"v_mfma_f32_16x16x32_bf16 v[104:107], a[200:203], a[20:23], 0\n"
"s_waitcnt vmcnt(14) lgkmcnt(8)\n"
"ds_read_b128 a[40:43], v212 offset:49184\n"
"v_add_f32_dpp v172, v172, v172 row_half_mirror row_mask:0xf bank_mask:0xf\n"
"v_mfma_f32_16x16x32_bf16 v[104:107], a[196:199], a[4:7], v[104:107]\n"
"s_add_u32 m0, s77, 131200\n"
"ds_read_b128 a[56:59], v213 offset:49184\n"
"v_add_f32_dpp v172, v172, v172 row_mirror row_mask:0xf bank_mask:0xf\n"
"v_mov_b32 v173, v172\n"
"v_mfma_f32_16x16x32_bf16 v[112:115], a[196:199], a[8:11], 0\n"
"buffer_load_dwordx4 v224, s[32:35], s82 offen lds\n"
"v_permlane16_swap_b32 v172, v173\n"
"v_mfma_f32_16x16x32_bf16 v[112:115], a[200:203], a[24:27], v[112:115]\n"
"s_addk_i32 m0, 896\n"
"ds_read_b64_tr_b16 a[160:161], v221 offset:4104\n"
"v_add_f32 v172, v172, v173\n"
"v_mfma_f32_16x16x32_bf16 v[120:123], a[200:203], a[28:31], 0\n"
"ds_read_b64_tr_b16 a[162:163], v221 offset:4360\n"
"v_mul_f32 v174, 0x3acccccd, v172\n"
"v_mfma_f32_16x16x32_bf16 v[120:123], a[196:199], a[12:15], v[120:123]\n"
"buffer_load_dwordx4 v224, s[32:35], s82 offen offset:128 lds\n"
"v_and_b32 v173, 16, v253\n"
"v_mfma_f32_16x16x32_bf16 v[124:127], a[204:207], a[12:15], 0\n"
"s_waitcnt vmcnt(14) lgkmcnt(7)\n"
"ds_read_b128 a[44:47], v212 offset:57376\n"
"v_or_b32 v205, s72, v253\n"
"v_cmp_ne_u32 vcc, 0, v205\n"
"v_mov_b32 v208, 0x40000000\n"
"s_nop 0\n"
"v_cndmask_b32 v231, 0, v208, vcc\n"
"v_cmp_eq_u32 vcc, 0, v173\n"
"v_mfma_f32_16x16x32_bf16 v[124:127], a[208:211], a[28:31], v[124:127]\n"
"s_addk_i32 m0, 7304\n"
"ds_read_b128 a[60:63], v213 offset:57376\n"
"v_cndmask_b32 v175, 0, v174, vcc\n"
"v_mfma_f32_16x16x32_bf16 v[116:119], a[208:211], a[24:27], 0\n"
"s_mov_b32 s82, 1638400\n"
"buffer_load_dwordx4 v224, s[32:35], s82 offen lds\n"
"v_sub_f32 v160, v176, v174\n"
"v_mfma_f32_16x16x32_bf16 v[116:119], a[204:207], a[8:11], v[116:119]\n"
"s_addk_i32 m0, 896\n"
"ds_read_b64_tr_b16 a[164:165], v221 offset:512\n"
"v_mul_f32 v172, v160, v160\n"
"v_mfma_f32_16x16x32_bf16 v[108:111], a[204:207], a[4:7], 0\n"
"ds_read_b64_tr_b16 a[166:167], v221 offset:768\n"
"v_sub_f32 v161, v177, v174\n"
"v_mul_f32 v173, v161, v161\n"
"v_mfma_f32_16x16x32_bf16 v[108:111], a[208:211], a[20:23], v[108:111]\n"
"ds_read_b64_tr_b16 a[168:169], v221 offset:4608\n"
"v_sub_f32 v162, v178, v174\n"
"v_mfma_f32_16x16x32_bf16 v[100:103], a[208:211], a[16:19], 0\n"
"ds_read_b64_tr_b16 a[170:171], v221 offset:4864\n"
"v_fmac_f32 v172, v162, v162\n"
"ds_read_b64_tr_b16 a[172:173], v221 offset:520\n"
"ds_read_b64_tr_b16 a[174:175], v221 offset:776\n"
"ds_read_b64_tr_b16 a[176:177], v221 offset:4616\n"
"ds_read_b64_tr_b16 a[178:179], v221 offset:4872\n"
"ds_read_b64_tr_b16 a[120:121], v215 offset:0\n"
"ds_read_b64_tr_b16 a[122:123], v215 offset:256\n"
"v_mfma_f32_16x16x32_bf16 v[100:103], a[204:207], a[0:3], v[100:103]\n"
"ds_read_b64_tr_b16 a[124:125], v215 offset:8\n"
"v_sub_f32 v163, v179, v174\n"
"ds_read_b64_tr_b16 a[126:127], v215 offset:264\n"
"v_mfma_f32_16x16x32_bf16 v[0:3], a[148:151], a[32:35], v[0:3]\n"
"buffer_load_dwordx4 v224, s[32:35], s82 offen offset:128 lds\n"
"v_fmac_f32 v173, v163, v163\n"
"v_mfma_f32_16x16x32_bf16 v[0:3], a[152:155], a[48:51], v[0:3]\n"
"s_waitcnt lgkmcnt(15)\n"
"ds_read_b64_tr_b16 a[180:181], v221 offset:1024\n"
"v_sub_f32 v160, v180, v174\n"
"v_mfma_f32_16x16x32_bf16 v[8:11], a[152:155], a[52:55], v[8:11]\n"
"s_waitcnt vmcnt(10) lgkmcnt(15)\n"
"ds_read_b128 a[64:67], v212 offset:0\n"
"v_fmac_f32 v172, v160, v160\n"
"v_mfma_f32_16x16x32_bf16 v[8:11], a[148:151], a[36:39], v[8:11]\n"
"s_add_u32 m0, s77, 48816\n"
"ds_read_b128 a[80:83], v213 offset:0\n"
"v_sub_f32 v161, v181, v174\n"
"v_mfma_f32_16x16x32_bf16 v[16:19], a[148:151], a[40:43], v[16:19]\n"
"ds_read_b64_tr_b16 a[182:183], v221 offset:1280\n"
"v_fmac_f32 v173, v161, v161\n"
"v_mfma_f32_16x16x32_bf16 v[16:19], a[152:155], a[56:59], v[16:19]\n"
"s_waitcnt lgkmcnt(15)\n"
"buffer_load_dwordx4 v250, s[28:31], s84 offen offset:384 lds\n"
"v_sub_f32 v162, v182, v174\n"
"v_fmac_f32 v172, v162, v162\n"
"v_mfma_f32_16x16x32_bf16 v[24:27], a[152:155], a[60:63], v[24:27]\n"
"s_addk_i32 m0, 1024\n"
"ds_read_b64_tr_b16 a[184:185], v221 offset:5120\n"
"v_sub_f32 v163, v183, v174\n"
"v_mfma_f32_16x16x32_bf16 v[24:27], a[148:151], a[44:47], v[24:27]\n"
"s_waitcnt lgkmcnt(7)\n"
"ds_read_b64_tr_b16 a[186:187], v221 offset:5376\n"
"v_fmac_f32 v173, v163, v163\n"
"v_mfma_f32_16x16x32_bf16 a[96:99], a[120:123], a[144:147], a[96:99]\n"
"s_waitcnt lgkmcnt(6)\n"
"ds_read_b64_tr_b16 a[188:189], v221 offset:1032\n"
"v_sub_f32 v160, v184, v174\n"
"v_mfma_f32_16x16x32_bf16 a[100:103], a[124:127], a[144:147], a[100:103]\n"
"ds_read_b64_tr_b16 a[190:191], v221 offset:1288\n"
"v_fmac_f32 v172, v160, v160\n"
"v_mfma_f32_16x16x32_bf16 v[28:31], a[156:159], a[44:47], v[28:31]\n"
"buffer_load_dwordx4 v251, s[28:31], s84 offen offset:384 lds\n"
"v_sub_f32 v161, v185, v174\n"
"v_mfma_f32_16x16x32_bf16 v[28:31], a[160:163], a[60:63], v[28:31]\n"
"s_addk_i32 m0, 7168\n"
"ds_read_b64_tr_b16 a[192:193], v221 offset:5128\n"
"v_fmac_f32 v173, v161, v161\n"
"v_mfma_f32_16x16x32_bf16 v[20:23], a[160:163], a[56:59], v[20:23]\n"
"ds_read_b64_tr_b16 a[194:195], v221 offset:5384\n"
"v_sub_f32 v162, v186, v174\n"
"v_mfma_f32_16x16x32_bf16 v[20:23], a[156:159], a[40:43], v[20:23]\n"
"ds_read_b64_tr_b16 a[196:197], v221 offset:1536\n"
"v_fmac_f32 v172, v162, v162\n"
"v_mfma_f32_16x16x32_bf16 v[12:15], a[156:159], a[36:39], v[12:15]\n"
"ds_read_b64_tr_b16 a[198:199], v221 offset:1792\n"
"v_sub_f32 v163, v187, v174\n"
"v_fmac_f32 v173, v163, v163\n"
"v_mfma_f32_16x16x32_bf16 v[12:15], a[160:163], a[52:55], v[12:15]\n"
"buffer_load_dwordx4 v250, s[28:31], s85 offen offset:384 lds\n"
"v_sub_f32 v160, v188, v174\n"
"v_mfma_f32_16x16x32_bf16 v[4:7], a[160:163], a[48:51], v[4:7]\n"
"s_addk_i32 m0, 1024\n"
"ds_read_b64_tr_b16 a[200:201], v221 offset:5632\n"
"v_fmac_f32 v172, v160, v160\n"
"v_mfma_f32_16x16x32_bf16 v[4:7], a[156:159], a[32:35], v[4:7]\n"
"s_waitcnt vmcnt(11) lgkmcnt(9)\n"
"ds_read_b128 a[68:71], v212 offset:8192\n"
"v_sub_f32 v161, v189, v174\n"
"v_mfma_f32_16x16x32_bf16 v[32:35], a[164:167], a[32:35], v[32:35]\n"
"s_mov_b32 s82, 2097152\n"
"ds_read_b128 a[84:87], v213 offset:8192\n"
"v_fmac_f32 v173, v161, v161\n"
"v_mfma_f32_16x16x32_bf16 v[32:35], a[168:171], a[48:51], v[32:35]\n"
"ds_read_b64_tr_b16 a[202:203], v221 offset:5888\n"
"v_sub_f32 v162, v190, v174\n"
"v_mfma_f32_16x16x32_bf16 v[40:43], a[168:171], a[52:55], v[40:43]\n"
"buffer_load_dwordx4 v251, s[28:31], s85 offen offset:384 lds\n"
"v_fmac_f32 v172, v162, v162\n"
"v_mfma_f32_16x16x32_bf16 v[40:43], a[164:167], a[36:39], v[40:43]\n"
"s_addk_i32 m0, 7168\n"
"ds_read_b64_tr_b16 a[204:205], v221 offset:1544\n"
"v_sub_f32 v163, v191, v174\n"
"v_mfma_f32_16x16x32_bf16 v[48:51], a[164:167], a[40:43], v[48:51]\n"
"ds_read_b64_tr_b16 a[206:207], v221 offset:1800\n"
"v_fmac_f32 v173, v163, v163\n"
"v_mfma_f32_16x16x32_bf16 v[48:51], a[168:171], a[56:59], v[48:51]\n"
"ds_read_b64_tr_b16 a[208:209], v221 offset:5640\n"
"v_sub_f32 v160, v192, v175\n"
"v_mfma_f32_16x16x32_bf16 v[56:59], a[168:171], a[60:63], v[56:59]\n"
"ds_read_b64_tr_b16 a[210:211], v221 offset:5896\n"
"v_fmac_f32 v172, v160, v160\n"
"v_sub_f32 v161, v193, v175\n"
"v_mfma_f32_16x16x32_bf16 v[56:59], a[164:167], a[44:47], v[56:59]\n"
"buffer_load_dwordx4 v250, s[28:31], s86 offen offset:384 lds\n"
"v_fmac_f32 v173, v161, v161\n"
"v_mfma_f32_16x16x32_bf16 v[60:63], a[172:175], a[44:47], v[60:63]\n"
"s_addk_i32 m0, 1024\n"
"ds_read_b64_tr_b16 a[128:129], v215 offset:4096\n"
"v_sub_f32 v162, v194, v175\n"
"v_mfma_f32_16x16x32_bf16 v[60:63], a[176:179], a[60:63], v[60:63]\n"
"ds_read_b64_tr_b16 a[130:131], v215 offset:4352\n"
"v_fmac_f32 v172, v162, v162\n"
"v_mfma_f32_16x16x32_bf16 v[52:55], a[176:179], a[56:59], v[52:55]\n"
"ds_read_b64_tr_b16 a[132:133], v215 offset:4104\n"
"v_sub_f32 v163, v195, v175\n"
"v_mfma_f32_16x16x32_bf16 v[52:55], a[172:175], a[40:43], v[52:55]\n"
"ds_read_b64_tr_b16 a[134:135], v215 offset:4360\n"
"v_fmac_f32 v173, v163, v163\n"
"v_mfma_f32_16x16x32_bf16 v[44:47], a[172:175], a[36:39], v[44:47]\n"
"buffer_load_dwordx4 v251, s[28:31], s86 offen offset:384 lds\n"
"v_sub_f32 v160, v196, v175\n"
"v_mfma_f32_16x16x32_bf16 v[44:47], a[176:179], a[52:55], v[44:47]\n"
"s_addk_i32 m0, 7168\n"
"v_fmac_f32 v172, v160, v160\n"
"v_sub_f32 v161, v197, v175\n"
"v_mfma_f32_16x16x32_bf16 v[36:39], a[176:179], a[48:51], v[36:39]\n"
"v_fmac_f32 v173, v161, v161\n"
"v_sub_f32 v162, v198, v175\n"
"v_mfma_f32_16x16x32_bf16 v[36:39], a[172:175], a[32:35], v[36:39]\n"
"s_waitcnt vmcnt(12) lgkmcnt(7)\n"
"ds_read_b128 a[72:75], v212 offset:16384\n"
"v_fmac_f32 v172, v162, v162\n"
"v_mfma_f32_16x16x32_bf16 v[64:67], a[180:183], a[32:35], v[64:67]\n"
"s_mov_b32 s46, 131072\n"
"ds_read_b128 a[88:91], v213 offset:16384\n"
"v_sub_f32 v163, v199, v175\n"
"v_mfma_f32_16x16x32_bf16 v[64:67], a[184:187], a[48:51], v[64:67]\n"
"s_mov_b32 s47, 0x20000\n"
"buffer_load_dwordx4 v250, s[28:31], s87 offen offset:384 lds\n"
"v_mfma_f32_16x16x32_bf16 v[72:75], a[184:187], a[52:55], v[72:75]\n"
"s_addk_i32 m0, 1024\n"
"v_fmac_f32 v173, v163, v163\n"
"v_add_f32 v172, v172, v173\n"
"v_mfma_f32_16x16x32_bf16 v[72:75], a[180:183], a[36:39], v[72:75]\n"
"s_mov_b32 s50, 131072\n"
"v_add_f32_dpp v172, v172, v172 quad_perm:[1,0,3,2] row_mask:0xf bank_mask:0xf\n"
"s_nop 1\n"
"v_add_f32_dpp v172, v172, v172 quad_perm:[2,3,0,1] row_mask:0xf bank_mask:0xf\n"
"v_mfma_f32_16x16x32_bf16 v[80:83], a[180:183], a[40:43], v[80:83]\n"
"s_waitcnt lgkmcnt(2)\n"
"v_add_f32_dpp v172, v172, v172 row_half_mirror row_mask:0xf bank_mask:0xf\n"
"s_nop 1\n"
"v_add_f32_dpp v172, v172, v172 row_mirror row_mask:0xf bank_mask:0xf\n"
"v_mfma_f32_16x16x32_bf16 v[80:83], a[184:187], a[56:59], v[80:83]\n"
"s_barrier\n"
"ds_read_b64_tr_b16 a[148:149], v221 offset:16400\n"
"v_mov_b32 v173, v172\n"
"v_mfma_f32_16x16x32_bf16 v[88:91], a[184:187], a[60:63], v[88:91]\n"
"s_mov_b32 s51, 0x20000\n"
"buffer_load_dwordx4 v251, s[28:31], s87 offen offset:384 lds\n"
"v_mfma_f32_16x16x32_bf16 v[88:91], a[180:183], a[44:47], v[88:91]\n"
"s_add_u32 m0, s77, 15888\n"
"ds_read_b64_tr_b16 a[150:151], v221 offset:16656\n"
"v_permlane16_swap_b32 v172, v173\n"
"v_mfma_f32_16x16x32_bf16 a[96:99], a[128:131], a[144:147], a[96:99]\n"
"s_mov_b32 s39, 0x20000\n"
"ds_read_b64_tr_b16 a[152:153], v221 offset:20496\n"
"v_add_f32 v172, v172, v173\n"
"v_mfma_f32_16x16x32_bf16 a[100:103], a[132:135], a[144:147], a[100:103]\n"
"s_mov_b32 s43, 0x20000\n"
"ds_read_b64_tr_b16 a[154:155], v221 offset:20752\n"
"v_mul_f32 v172, 0x3acccccd, v172\n"
"v_mfma_f32_16x16x32_bf16 v[92:95], a[188:191], a[44:47], v[92:95]\n"
"s_mov_b32 s68, s60\n"
"ds_read_b64_tr_b16 a[156:157], v221 offset:16408\n"
"v_cvt_pk_bf16_f32 v172, v172, v172\n"
"v_mfma_f32_16x16x32_bf16 v[92:95], a[192:195], a[60:63], v[92:95]\n"
"s_mov_b32 s69, s61\n"
"buffer_load_dwordx4 v250, s[28:31], s84 offen offset:512 lds\n"
"v_mfma_f32_16x16x32_bf16 v[84:87], a[192:195], a[56:59], v[84:87]\n"
"s_addk_i32 m0, 1024\n"
"ds_read_b64_tr_b16 a[158:159], v221 offset:16664\n"
"v_lshlrev_b32 v172, 16, v172\n"
"v_mfma_f32_16x16x32_bf16 v[84:87], a[188:191], a[40:43], v[84:87]\n"
"L_st1_%=:\n"
"s_cbranch_execz L_st5_%=\n"
"s_mov_b32 s71, s63\n"
"ds_read_b64_tr_b16 a[160:161], v221 offset:20504\n"
"v_add_f32 v172, 0x3727c5ac, v172\n"
"v_mfma_f32_16x16x32_bf16 v[76:79], a[188:191], a[36:39], v[76:79]\n"
"s_and_b32 s65, s65, 0xffff\n"
"ds_read_b64_tr_b16 a[162:163], v221 offset:20760\n"
"v_readfirstlane_b32 s80, v232\n"
"v_readfirstlane_b32 s81, v233\n"
"v_cvt_pk_bf16_f32 v172, v172, v172\n"
"v_mfma_f32_16x16x32_bf16 v[76:79], a[192:195], a[52:55], v[76:79]\n"
"s_mov_b32 s66, 2621440\n"
"ds_read_b64_tr_b16 a[164:165], v221 offset:16912\n"
"v_lshlrev_b32 v172, 16, v172\n"
"v_mfma_f32_16x16x32_bf16 v[68:71], a[192:195], a[48:51], v[68:71]\n"
"s_mov_b32 s67, 0x20000\n"
"v_sqrt_f32 v172, v172\n"
"v_mfma_f32_16x16x32_bf16 v[68:71], a[188:191], a[32:35], v[68:71]\n"
"s_mul_i32 s81, s81, 0x4c957f2d\n"
"buffer_load_dwordx4 v251, s[28:31], s84 offen offset:512 lds\n"
"s_mul_i32 s94, s80, 0x5851f42d\n"
"s_add_u32 s81, s81, s94\n"
"s_mul_hi_u32 s94, s80, 0x4c957f2d\n"
"s_add_u32 s81, s81, s94\n"
"s_mul_i32 s80, s80, 0x4c957f2d\n"
"s_add_u32 s80, s80, 0xf767814f\n"
"s_addc_u32 s81, s81, 0x14057b7e\n"
"v_mov_b32 v232, s80\n"
"v_cvt_pk_bf16_f32 v165, v174, v172\n"
"v_mov_b32 v164, v232\n"
"v_mfma_f32_16x16x32_bf16 v[96:99], a[196:199], a[32:35], v[96:99]\n"
"s_waitcnt vmcnt(14) lgkmcnt(8)\n"
"ds_read_b128 a[76:79], v212 offset:24576\n"
"v_and_b32 v173, 31, v253\n"
"v_mfma_f32_16x16x32_bf16 v[96:99], a[200:203], a[48:51], v[96:99]\n"
"s_add_u32 m0, s77, 82000\n"
"ds_read_b128 a[92:95], v213 offset:24576\n"
"v_cmp_eq_u32 vcc, 0, v173\n"
"v_mfma_f32_16x16x32_bf16 v[104:107], a[200:203], a[52:55], v[104:107]\n"
"s_lshl_b32 s92, s13, 19\n"
"ds_read_b64_tr_b16 a[166:167], v221 offset:17168\n"
"v_lshrrev_b32 v173, 2, v253\n"
"v_mfma_f32_16x16x32_bf16 v[104:107], a[196:199], a[36:39], v[104:107]\n"
"s_add_u32 s92, s92, 393216\n"
"ds_read_b64_tr_b16 a[168:169], v221 offset:21008\n"
"v_mov_b32 v166, 0x40000000\n"
"v_mfma_f32_16x16x32_bf16 v[112:115], a[196:199], a[40:43], v[112:115]\n"
"s_add_u32 s44, s44, s92\n"
"s_addc_u32 s45, s45, 0\n"
"buffer_load_dwordx4 v224, s[32:35], s82 offen lds\n"
"v_cndmask_b32 v166, v166, v173, vcc\n"
"v_mfma_f32_16x16x32_bf16 v[112:115], a[200:203], a[56:59], v[112:115]\n"
"s_add_u32 s48, s48, s92\n"
"s_addc_u32 s49, s49, 0\n"
"ds_read_b64_tr_b16 a[170:171], v221 offset:21264\n"
"v_bfe_u32 v208, v253, 4, 1\n"
"v_mfma_f32_16x16x32_bf16 v[120:123], a[200:203], a[60:63], v[120:123]\n"
"s_addk_i32 m0, 896\n"
"ds_read_b64_tr_b16 a[172:173], v221 offset:16920\n"
"s_lshl_b32 s92, s72, 1\n"
"v_mfma_f32_16x16x32_bf16 v[120:123], a[196:199], a[44:47], v[120:123]\n"
"s_and_b32 s45, s45, 0xffff\n"
"ds_read_b64_tr_b16 a[174:175], v221 offset:17176\n"
"v_add_u32 v208, s92, v208\n"
"v_mfma_f32_16x16x32_bf16 v[124:127], a[204:207], a[44:47], v[124:127]\n"
"s_and_b32 s49, s49, 0xffff\n"
"ds_read_b64_tr_b16 a[176:177], v221 offset:21016\n"
"s_mov_b32 s79, s75\n"
"v_mfma_f32_16x16x32_bf16 v[124:127], a[208:211], a[60:63], v[124:127]\n"
"s_and_b32 s92, s73, 1\n"
"buffer_store_dwordx2 v[164:165], v166, s[60:63], s88 offen sc1\n"
"v_mfma_f32_16x16x32_bf16 v[116:119], a[208:211], a[56:59], v[116:119]\n"
"s_cmp_ge_u32 s75, 27\n"
"s_cselect_b32 s93, 5, 0\n"
"s_cselect_b32 s94, 1, 0\n"
"ds_read_b64_tr_b16 a[178:179], v221 offset:21272\n"
"s_sub_u32 s93, s75, s93\n"
"v_mfma_f32_16x16x32_bf16 v[116:119], a[204:207], a[40:43], v[116:119]\n"
"s_cmp_ge_u32 s75, 22\n"
"s_cselect_b32 s95, 31, s75\n"
"ds_read_b64_tr_b16 a[104:105], v215 offset:16400\n"
"s_cmp_eq_u32 s92, 0\n"
"s_cselect_b32 s79, s93, s95\n"
"s_cselect_b32 s94, s94, 0\n"
"v_mfma_f32_16x16x32_bf16 v[108:111], a[204:207], a[36:39], v[108:111]\n"
"s_mul_i32 s94, s94, 1\n"
"ds_read_b64_tr_b16 a[106:107], v215 offset:16656\n"
"s_xor_b32 s27, s27, s94\n"
"v_mfma_f32_16x16x32_bf16 v[108:111], a[208:211], a[52:55], v[108:111]\n"
"s_lshl_b32 s92, s79, 7\n"
"ds_read_b64_tr_b16 a[108:109], v215 offset:16408\n"
"v_cmp_gt_u32 vcc, 2, v253\n"
"v_mfma_f32_16x16x32_bf16 v[100:103], a[208:211], a[48:51], v[100:103]\n"
"s_add_u32 s36, s36, s92\n"
"s_addc_u32 s37, s37, 0\n"
"ds_read_b64_tr_b16 a[110:111], v215 offset:16664\n"
"v_mov_b32 v210, 0x40000000\n"
"v_mfma_f32_16x16x32_bf16 v[100:103], a[204:207], a[32:35], v[100:103]\n"
"s_waitcnt lgkmcnt(9)\n"
"buffer_load_dwordx4 v224, s[32:35], s82 offen offset:128 lds\n"
"v_mfma_f32_16x16x32_bf16 v[0:3], a[148:151], a[64:67], v[0:3]\n"
"s_waitcnt vmcnt(11)\n"
"ds_read_b128 a[212:215], v212 offset:32800\n"
"v_lshlrev_b32 v172, 16, v165\n"
"v_mfma_f32_16x16x32_bf16 v[0:3], a[152:155], a[80:83], v[0:3]\n"
"s_mov_b32 s7, 0x20000\n"
"ds_read_b128 a[228:231], v213 offset:32800\n"
"v_and_b32 v173, 0xffff0000, v165\n"
"v_mfma_f32_16x16x32_bf16 v[8:11], a[152:155], a[84:87], v[8:11]\n"
"s_addk_i32 m0, 7304\n"
"v_rcp_f32 v173, v173\n"
"v_mfma_f32_16x16x32_bf16 v[8:11], a[148:151], a[68:71], v[8:11]\n"
"s_mov_b32 s82, 2162688\n"
"ds_read_b64_tr_b16 a[180:181], v221 offset:17424\n"
"v_sub_f32 v176, v176, v172\n"
"v_mfma_f32_16x16x32_bf16 v[16:19], a[148:151], a[72:75], v[16:19]\n"
"s_add_u32 s40, s40, s92\n"
"s_addc_u32 s41, s41, 0\n"
"buffer_load_dwordx4 v224, s[32:35], s82 offen lds\n"
"v_mfma_f32_16x16x32_bf16 v[16:19], a[152:155], a[88:91], v[16:19]\n"
"s_addk_i32 m0, 896\n"
"ds_read_b64_tr_b16 a[182:183], v221 offset:17680\n"
"v_sub_f32 v177, v177, v172\n"
"v_mfma_f32_16x16x32_bf16 v[24:27], a[152:155], a[92:95], v[24:27]\n"
"s_and_b32 s37, s37, 0xffff\n"
"ds_read_b64_tr_b16 a[184:185], v221 offset:21520\n"
"v_cndmask_b32 v252, v210, v252, vcc\n"
"v_mfma_f32_16x16x32_bf16 v[24:27], a[148:151], a[76:79], v[24:27]\n"
"s_waitcnt lgkmcnt(7)\n"
"ds_read_b64_tr_b16 a[186:187], v221 offset:21776\n"
"v_cvt_pk_bf16_f32 v177, v176, v177\n"
"v_mfma_f32_16x16x32_bf16 a[96:99], a[104:107], a[144:147], a[96:99]\n"
"s_waitcnt lgkmcnt(6)\n"
"ds_read_b64_tr_b16 a[188:189], v221 offset:17432\n"
"s_lshl_b32 s6, s13, 8\n"
"v_mfma_f32_16x16x32_bf16 a[100:103], a[108:111], a[144:147], a[100:103]\n"
"s_sub_u32 s4, s32, s6\n"
"s_subb_u32 s5, s33, 0\n"
"buffer_load_dwordx4 v224, s[32:35], s82 offen offset:128 lds\n"
"v_mfma_f32_16x16x32_bf16 v[28:31], a[156:159], a[76:79], v[28:31]\n"
"s_mov_b32 s6, 5242880\n"
"ds_read_b64_tr_b16 a[190:191], v221 offset:17688\n"
"s_lshl_b32 s94, s73, 2\n"
"v_mfma_f32_16x16x32_bf16 v[28:31], a[160:163], a[92:95], v[28:31]\n"
"s_add_u32 m0, s77, 24080\n"
"ds_read_b64_tr_b16 a[192:193], v221 offset:21528\n"
"s_add_u32 s94, s94, s72\n"
"v_mfma_f32_16x16x32_bf16 v[20:23], a[160:163], a[88:91], v[20:23]\n"
"s_mov_b32 s82, 2621440\n"
"ds_read_b64_tr_b16 a[194:195], v221 offset:21784\n"
"s_mul_i32 s94, s94, 1536\n"
"v_mfma_f32_16x16x32_bf16 v[20:23], a[156:159], a[72:75], v[20:23]\n"
"s_add_u32 s94, s94, 3670016\n"
"ds_read_b64_tr_b16 a[196:197], v221 offset:17936\n"
"v_lshlrev_b32 v248, 7, v253\n"
"v_mfma_f32_16x16x32_bf16 v[12:15], a[156:159], a[68:71], v[12:15]\n"
"s_and_b32 s41, s41, 0xffff\n"
"buffer_load_dwordx4 v250, s[28:31], s85 offen offset:512 lds\n"
"v_mfma_f32_16x16x32_bf16 v[12:15], a[160:163], a[84:87], v[12:15]\n"
"s_addk_i32 m0, 1024\n"
"ds_read_b64_tr_b16 a[198:199], v221 offset:18192\n"
"v_cmp_gt_u32 vcc, 12, v253\n"
"v_mov_b32 v249, 0x40000000\n"
"s_nop 0\n"
"v_cndmask_b32 v248, v249, v248, vcc\n"
"buffer_load_dword v249, v248, s[4:7], s94 offen\n"
"v_mfma_f32_16x16x32_bf16 v[4:7], a[160:163], a[80:83], v[4:7]\n"
"s_sub_u32 s93, 2211840, s92\n"
"ds_read_b64_tr_b16 a[200:201], v221 offset:22032\n"
"v_lshlrev_b32 v176, 16, v177\n"
"v_mfma_f32_16x16x32_bf16 v[4:7], a[156:159], a[64:67], v[4:7]\n"
"s_waitcnt vmcnt(13) lgkmcnt(9)\n"
"ds_read_b128 a[216:219], v212 offset:40992\n"
"v_and_b32 v177, s0, v177\n"
"v_mfma_f32_16x16x32_bf16 v[32:35], a[164:167], a[64:67], v[32:35]\n"
"s_lshl_b32 s92, s72, 1\n"
"ds_read_b128 a[232:235], v213 offset:40992\n"
"v_mul_f32 v176, v176, v173\n"
"v_mfma_f32_16x16x32_bf16 v[32:35], a[168:171], a[80:83], v[32:35]\n"
"s_lshl_b32 s76, s72, 10\n"
"ds_read_b64_tr_b16 a[202:203], v221 offset:22288\n"
"v_mul_f32 v177, v177, v173\n"
"v_mfma_f32_16x16x32_bf16 v[40:43], a[168:171], a[84:87], v[40:43]\n"
"s_mov_b32 s38, 2211840\n"
"buffer_load_dwordx4 v251, s[28:31], s85 offen offset:512 lds\n"
"v_cvt_pk_bf16_f32 v176, v176, v177\n"
"v_mfma_f32_16x16x32_bf16 v[40:43], a[164:167], a[68:71], v[40:43]\n"
"s_addk_i32 m0, 7168\n"
"ds_read_b64_tr_b16 a[204:205], v221 offset:17944\n"
"v_sub_f32 v178, v178, v172\n"
"v_mfma_f32_16x16x32_bf16 v[48:51], a[164:167], a[72:75], v[48:51]\n"
"s_mov_b32 s42, 2211840\n"
"ds_read_b64_tr_b16 a[206:207], v221 offset:18200\n"
"v_sub_f32 v179, v179, v172\n"
"v_mfma_f32_16x16x32_bf16 v[48:51], a[168:171], a[88:91], v[48:51]\n"
"s_cmp_lt_u32 s79, 27\n"
"s_cselect_b32 s38, s93, 0\n"
"s_cselect_b32 s42, s93, 0\n"
"s_cselect_b32 s70, s62, 0\n"
"ds_read_b64_tr_b16 a[208:209], v221 offset:22040\n"
"v_cvt_pk_bf16_f32 v179, v178, v179\n"
"v_mfma_f32_16x16x32_bf16 v[56:59], a[168:171], a[92:95], v[56:59]\n"
"s_mul_i32 s78, s72, 5120\n"
"ds_read_b64_tr_b16 a[210:211], v221 offset:22296\n"
"v_lshlrev_b32 v178, 16, v179\n"
"v_mfma_f32_16x16x32_bf16 v[56:59], a[164:167], a[76:79], v[56:59]\n"
"s_mov_b32 s12, s11\n"
"buffer_load_dwordx4 v250, s[28:31], s86 offen offset:512 lds\n"
"v_mfma_f32_16x16x32_bf16 v[60:63], a[172:175], a[76:79], v[60:63]\n"
"s_addk_i32 m0, 1024\n"
"ds_read_b64_tr_b16 a[112:113], v215 offset:20496\n"
"v_and_b32 v179, s0, v179\n"
"v_mfma_f32_16x16x32_bf16 v[60:63], a[176:179], a[92:95], v[60:63]\n"
"ds_read_b64_tr_b16 a[114:115], v215 offset:20752\n"
"v_mul_f32 v178, v178, v173\n"
"v_mfma_f32_16x16x32_bf16 v[52:55], a[176:179], a[88:91], v[52:55]\n"
"ds_read_b64_tr_b16 a[116:117], v215 offset:20504\n"
"v_mul_f32 v179, v179, v173\n"
"v_mfma_f32_16x16x32_bf16 v[52:55], a[172:175], a[72:75], v[52:55]\n"
"ds_read_b64_tr_b16 a[118:119], v215 offset:20760\n"
"v_cvt_pk_bf16_f32 v177, v178, v179\n"
"v_mfma_f32_16x16x32_bf16 v[44:47], a[172:175], a[68:71], v[44:47]\n"
"buffer_load_dwordx4 v251, s[28:31], s86 offen offset:512 lds\n"
"v_sub_f32 v180, v180, v172\n"
"v_mfma_f32_16x16x32_bf16 v[44:47], a[176:179], a[84:87], v[44:47]\n"
"s_addk_i32 m0, 7168\n"
"v_sub_f32 v181, v181, v172\n"
"v_cvt_pk_bf16_f32 v181, v180, v181\n"
"v_mfma_f32_16x16x32_bf16 v[36:39], a[176:179], a[80:83], v[36:39]\n"
"v_lshlrev_b32 v180, 16, v181\n"
"v_and_b32 v181, s0, v181\n"
"v_mfma_f32_16x16x32_bf16 v[36:39], a[172:175], a[64:67], v[36:39]\n"
"s_waitcnt vmcnt(14) lgkmcnt(7)\n"
"ds_read_b128 a[220:223], v212 offset:49184\n"
"v_mul_f32 v180, v180, v173\n"
"v_mfma_f32_16x16x32_bf16 v[64:67], a[180:183], a[64:67], v[64:67]\n"
"s_and_b32 s53, s53, 0xffff\n"
"ds_read_b128 a[236:239], v213 offset:49184\n"
"v_mul_f32 v181, v181, v173\n"
"v_mfma_f32_16x16x32_bf16 v[64:67], a[184:187], a[80:83], v[64:67]\n"
"s_mov_b32 s54, 5242880\n"
"buffer_load_dwordx4 v250, s[28:31], s87 offen offset:512 lds\n"
"v_mfma_f32_16x16x32_bf16 v[72:75], a[184:187], a[84:87], v[72:75]\n"
"s_addk_i32 m0, 1024\n"
"v_cvt_pk_bf16_f32 v178, v180, v181\n"
"v_sub_f32 v182, v182, v172\n"
"v_mfma_f32_16x16x32_bf16 v[72:75], a[180:183], a[68:71], v[72:75]\n"
"s_mov_b32 s55, 0x20000\n"
"v_sub_f32 v183, v183, v172\n"
"v_cvt_pk_bf16_f32 v183, v182, v183\n"
"v_mfma_f32_16x16x32_bf16 v[80:83], a[180:183], a[72:75], v[80:83]\n"
"s_waitcnt lgkmcnt(2)\n"
"v_lshlrev_b32 v182, 16, v183\n"
"v_and_b32 v183, s0, v183\n"
"v_mfma_f32_16x16x32_bf16 v[80:83], a[184:187], a[88:91], v[80:83]\n"
"s_barrier\n"
"ds_read_b64_tr_b16 a[148:149], v221 offset:32800\n"
"v_mul_f32 v182, v182, v173\n"
"v_mfma_f32_16x16x32_bf16 v[88:91], a[184:187], a[92:95], v[88:91]\n"
"s_mov_b32 s59, 0x20000\n"
"buffer_load_dwordx4 v251, s[28:31], s87 offen offset:512 lds\n"
"v_mfma_f32_16x16x32_bf16 v[88:91], a[180:183], a[76:79], v[88:91]\n"
"s_add_u32 m0, s77, 98400\n"
"ds_read_b64_tr_b16 a[150:151], v221 offset:33056\n"
"v_mul_f32 v183, v183, v173\n"
"v_mfma_f32_16x16x32_bf16 a[96:99], a[112:115], a[144:147], a[96:99]\n"
"s_lshr_b32 s83, s72, 1\n"
"ds_read_b64_tr_b16 a[152:153], v221 offset:36896\n"
"v_cvt_pk_bf16_f32 v179, v182, v183\n"
"v_mfma_f32_16x16x32_bf16 a[100:103], a[116:119], a[144:147], a[100:103]\n"
"s_lshl_b32 s83, s83, 12\n"
"ds_read_b64_tr_b16 a[154:155], v221 offset:37152\n"
"v_sub_f32 v184, v184, v172\n"
"v_mfma_f32_16x16x32_bf16 v[92:95], a[188:191], a[76:79], v[92:95]\n"
"s_and_b32 s8, s72, 1\n"
"ds_read_b64_tr_b16 a[156:157], v221 offset:32808\n"
"v_sub_f32 v185, v185, v172\n"
"v_mfma_f32_16x16x32_bf16 v[92:95], a[192:195], a[92:95], v[92:95]\n"
"s_lshl_b32 s8, s8, 10\n"
"buffer_load_dwordx4 v224, s[32:35], s82 offen lds\n"
"v_mfma_f32_16x16x32_bf16 v[84:87], a[192:195], a[88:91], v[84:87]\n"
"s_addk_i32 m0, 896\n"
"ds_read_b64_tr_b16 a[158:159], v221 offset:33064\n"
"v_cvt_pk_bf16_f32 v185, v184, v185\n"
"v_mfma_f32_16x16x32_bf16 v[84:87], a[188:191], a[72:75], v[84:87]\n"
"s_add_u32 s98, s83, s8\n"
"ds_read_b64_tr_b16 a[160:161], v221 offset:36904\n"
"v_lshlrev_b32 v184, 16, v185\n"
"v_mfma_f32_16x16x32_bf16 v[76:79], a[188:191], a[68:71], v[76:79]\n"
"s_and_b32 s57, s57, 0xffff\n"
"ds_read_b64_tr_b16 a[162:163], v221 offset:37160\n"
"v_and_b32 v185, s0, v185\n"
"v_mfma_f32_16x16x32_bf16 v[76:79], a[192:195], a[84:87], v[76:79]\n"
"s_mov_b32 s58, 2211840\n"
"ds_read_b64_tr_b16 a[164:165], v221 offset:33312\n"
"v_mul_f32 v184, v184, v173\n"
"v_mfma_f32_16x16x32_bf16 v[68:71], a[192:195], a[80:83], v[68:71]\n"
"s_lshl_b32 s95, s73, 2\n"
"buffer_load_dwordx4 v224, s[32:35], s82 offen offset:128 lds\n"
"v_mfma_f32_16x16x32_bf16 v[68:71], a[188:191], a[64:67], v[68:71]\n"
"s_waitcnt vmcnt(16) lgkmcnt(8)\n"
"ds_read_b128 a[224:227], v212 offset:57376\n"
"v_mul_f32 v185, v185, v173\n"
"v_mfma_f32_16x16x32_bf16 v[96:99], a[196:199], a[64:67], v[96:99]\n"
"s_addk_i32 m0, 7304\n"
"ds_read_b128 a[240:243], v213 offset:57376\n"
"v_cvt_pk_bf16_f32 v180, v184, v185\n"
"v_mfma_f32_16x16x32_bf16 v[96:99], a[200:203], a[80:83], v[96:99]\n"
"s_mov_b32 s82, 2686976\n"
"ds_read_b64_tr_b16 a[166:167], v221 offset:33568\n"
"v_sub_f32 v186, v186, v172\n"
"v_mfma_f32_16x16x32_bf16 v[104:107], a[200:203], a[84:87], v[104:107]\n"
"s_add_u32 s95, s95, s72\n"
"ds_read_b64_tr_b16 a[168:169], v221 offset:37408\n"
"v_sub_f32 v187, v187, v172\n"
"v_mfma_f32_16x16x32_bf16 v[104:107], a[196:199], a[68:71], v[104:107]\n"
"s_mov_b32 s10, 128\n"
"buffer_load_dwordx4 v224, s[32:35], s82 offen lds\n"
"v_mfma_f32_16x16x32_bf16 v[112:115], a[196:199], a[72:75], v[112:115]\n"
"s_addk_i32 m0, 896\n"
"ds_read_b64_tr_b16 a[170:171], v221 offset:37664\n"
"v_cvt_pk_bf16_f32 v187, v186, v187\n"
"v_mfma_f32_16x16x32_bf16 v[112:115], a[200:203], a[88:91], v[112:115]\n"
"s_mov_b32 s2, 28573696\n"
"ds_read_b64_tr_b16 a[172:173], v221 offset:33320\n"
"v_lshlrev_b32 v186, 16, v187\n"
"v_mfma_f32_16x16x32_bf16 v[120:123], a[200:203], a[92:95], v[120:123]\n"
"s_mul_i32 s96, s95, 6554\n"
"ds_read_b64_tr_b16 a[174:175], v221 offset:33576\n"
"v_and_b32 v187, s0, v187\n"
"v_mfma_f32_16x16x32_bf16 v[120:123], a[196:199], a[76:79], v[120:123]\n"
"s_lshr_b32 s96, s96, 16\n"
"ds_read_b64_tr_b16 a[176:177], v221 offset:37416\n"
"v_mul_f32 v186, v186, v173\n"
"v_mfma_f32_16x16x32_bf16 v[124:127], a[204:207], a[76:79], v[124:127]\n"
"s_mul_i32 s83, s96, 10\n"
"buffer_load_dwordx4 v224, s[32:35], s82 offen offset:128 lds\n"
"v_mfma_f32_16x16x32_bf16 v[124:127], a[208:211], a[92:95], v[124:127]\n"
"s_add_u32 m0, s77, 48560\n"
"ds_read_b64_tr_b16 a[178:179], v221 offset:37672\n"
"v_mul_f32 v187, v187, v173\n"
"v_mfma_f32_16x16x32_bf16 v[116:119], a[208:211], a[88:91], v[116:119]\n"
"s_mov_b32 s82, 3145728\n"
"ds_read_b64_tr_b16 a[120:121], v215 offset:32800\n"
"v_cvt_pk_bf16_f32 v181, v186, v187\n"
"v_mfma_f32_16x16x32_bf16 v[116:119], a[204:207], a[72:75], v[116:119]\n"
"s_sub_u32 s97, s95, s83\n"
"ds_read_b64_tr_b16 a[122:123], v215 offset:33056\n"
"v_sub_f32 v188, v188, v172\n"
"v_mfma_f32_16x16x32_bf16 v[108:111], a[204:207], a[68:71], v[108:111]\n"
"s_mul_i32 s83, s96, 40960\n"
"ds_read_b64_tr_b16 a[124:125], v215 offset:32808\n"
"v_sub_f32 v189, v189, v172\n"
"v_mfma_f32_16x16x32_bf16 v[108:111], a[208:211], a[84:87], v[108:111]\n"
"s_lshl_b32 s8, s97, 7\n"
"ds_read_b64_tr_b16 a[126:127], v215 offset:33064\n"
"v_cvt_pk_bf16_f32 v189, v188, v189\n"
"v_mfma_f32_16x16x32_bf16 v[100:103], a[208:211], a[80:83], v[100:103]\n"
"s_add_u32 s24, s83, s8\n"
"buffer_load_dwordx4 v250, s[28:31], s84 offen offset:640 lds\n"
"v_mfma_f32_16x16x32_bf16 v[100:103], a[204:207], a[64:67], v[100:103]\n"
"s_waitcnt vmcnt(17) lgkmcnt(9)\n"
"ds_read_b128 a[0:3], v212 offset:0\n"
"v_lshlrev_b32 v188, 16, v189\n"
"v_mfma_f32_16x16x32_bf16 v[0:3], a[148:151], a[212:215], v[0:3]\n"
"s_addk_i32 m0, 1024\n"
"ds_read_b128 a[16:19], v213 offset:0\n"
"v_and_b32 v189, s0, v189\n"
"v_mfma_f32_16x16x32_bf16 v[0:3], a[152:155], a[228:231], v[0:3]\n"
"s_lshl_b32 s83, s97, 1\n"
"ds_read_b64_tr_b16 a[180:181], v221 offset:33824\n"
"v_mul_f32 v188, v188, v173\n"
"v_mfma_f32_16x16x32_bf16 v[8:11], a[152:155], a[232:235], v[8:11]\n"
"s_add_u32 s83, s83, 0\n"
"ds_read_b64_tr_b16 a[182:183], v221 offset:34080\n"
"v_mul_f32 v189, v189, v173\n"
"v_mfma_f32_16x16x32_bf16 v[8:11], a[148:151], a[216:219], v[8:11]\n"
"s_mul_i32 s8, s83, 52\n"
"buffer_load_dwordx4 v251, s[28:31], s84 offen offset:640 lds\n"
"v_mfma_f32_16x16x32_bf16 v[16:19], a[148:151], a[220:223], v[16:19]\n"
"s_addk_i32 m0, 7168\n"
"ds_read_b64_tr_b16 a[184:185], v221 offset:37920\n"
"v_cvt_pk_bf16_f32 v182, v188, v189\n"
"v_mfma_f32_16x16x32_bf16 v[16:19], a[152:155], a[236:239], v[16:19]\n"
"s_lshr_b32 s8, s8, 8\n"
"ds_read_b64_tr_b16 a[186:187], v221 offset:38176\n"
"v_sub_f32 v190, v190, v172\n"
"v_mfma_f32_16x16x32_bf16 v[24:27], a[152:155], a[240:243], v[24:27]\n"
"s_mul_i32 s9, s8, 5\n"
"ds_read_b64_tr_b16 a[188:189], v221 offset:33832\n"
"v_sub_f32 v191, v191, v172\n"
"v_mfma_f32_16x16x32_bf16 v[24:27], a[148:151], a[224:227], v[24:27]\n"
"s_waitcnt lgkmcnt(9)\n"
"ds_read_b64_tr_b16 a[190:191], v221 offset:34088\n"
"v_cvt_pk_bf16_f32 v191, v190, v191\n"
"v_mfma_f32_16x16x32_bf16 a[96:99], a[120:123], a[144:147], a[96:99]\n"
"s_waitcnt lgkmcnt(8)\n"
"buffer_load_dwordx4 v250, s[28:31], s85 offen offset:640 lds\n"
"v_mfma_f32_16x16x32_bf16 a[100:103], a[124:127], a[144:147], a[100:103]\n"
"s_addk_i32 m0, 1024\n"
"ds_read_b64_tr_b16 a[192:193], v221 offset:37928\n"
"v_lshlrev_b32 v190, 16, v191\n"
"v_mfma_f32_16x16x32_bf16 v[28:31], a[156:159], a[224:227], v[28:31]\n"
"s_sub_u32 s83, s83, s9\n"
"ds_read_b64_tr_b16 a[194:195], v221 offset:38184\n"
"v_and_b32 v191, s0, v191\n"
"v_mfma_f32_16x16x32_bf16 v[28:31], a[160:163], a[240:243], v[28:31]\n"
"s_lshl_b32 s8, s8, 1\n"
"ds_read_b64_tr_b16 a[196:197], v221 offset:34336\n"
"v_mul_f32 v190, v190, v173\n"
"v_mfma_f32_16x16x32_bf16 v[20:23], a[160:163], a[236:239], v[20:23]\n"
"s_lshl_b32 s9, s83, 1\n"
"ds_read_b64_tr_b16 a[198:199], v221 offset:34592\n"
"v_mul_f32 v191, v191, v173\n"
"v_mfma_f32_16x16x32_bf16 v[20:23], a[156:159], a[220:223], v[20:23]\n"
"s_add_u32 s9, s9, 0\n"
"buffer_load_dwordx4 v251, s[28:31], s85 offen offset:640 lds\n"
"v_cvt_pk_bf16_f32 v183, v190, v191\n"
"v_mfma_f32_16x16x32_bf16 v[12:15], a[156:159], a[216:219], v[12:15]\n"
"s_add_u32 m0, s77, 114800\n"
"ds_read_b64_tr_b16 a[200:201], v221 offset:38432\n"
"v_sub_f32 v192, v192, v172\n"
"v_mfma_f32_16x16x32_bf16 v[12:15], a[160:163], a[232:235], v[12:15]\n"
"s_cmp_ge_u32 s9, 5\n"
"s_cselect_b32 s16, 1, 0\n"
"ds_read_b64_tr_b16 a[202:203], v221 offset:38688\n"
"v_sub_f32 v193, v193, v172\n"
"v_mfma_f32_16x16x32_bf16 v[4:7], a[160:163], a[228:231], v[4:7]\n"
"s_add_u32 s16, s16, s8\n"
"ds_read_b64_tr_b16 a[204:205], v221 offset:34344\n"
"v_cvt_pk_bf16_f32 v193, v192, v193\n"
"v_mfma_f32_16x16x32_bf16 v[4:7], a[156:159], a[212:215], v[4:7]\n"
"s_waitcnt vmcnt(12) lgkmcnt(9)\n"
"ds_read_b128 a[4:7], v212 offset:8192\n"
"v_lshlrev_b32 v192, 16, v193\n"
"v_mfma_f32_16x16x32_bf16 v[32:35], a[164:167], a[212:215], v[32:35]\n"
"s_mul_i32 s16, s16, s10\n"
"buffer_load_dwordx4 v224, s[32:35], s82 offen lds\n"
"v_and_b32 v193, s0, v193\n"
"v_mfma_f32_16x16x32_bf16 v[32:35], a[168:171], a[228:231], v[32:35]\n"
"s_addk_i32 m0, 896\n"
"ds_read_b128 a[20:23], v213 offset:8192\n"
"v_mul_f32 v192, v192, v173\n"
"v_mfma_f32_16x16x32_bf16 v[40:43], a[168:171], a[232:235], v[40:43]\n"
"s_add_u32 s16, s16, s96\n"
"ds_read_b64_tr_b16 a[206:207], v221 offset:34600\n"
"v_mul_f32 v193, v193, v173\n"
"v_mfma_f32_16x16x32_bf16 v[40:43], a[164:167], a[216:219], v[40:43]\n"
"s_mul_i32 s16, s16, 5\n"
"ds_read_b64_tr_b16 a[208:209], v221 offset:38440\n"
"v_cvt_pk_bf16_f32 v184, v192, v193\n"
"v_mfma_f32_16x16x32_bf16 v[48:51], a[164:167], a[220:223], v[48:51]\n"
"s_cmp_ge_u32 s9, 6\n"
"s_cselect_b32 s95, 6, 0\n"
"ds_read_b64_tr_b16 a[210:211], v221 offset:38696\n"
"v_sub_f32 v194, v194, v172\n"
"v_mfma_f32_16x16x32_bf16 v[48:51], a[168:171], a[236:239], v[48:51]\n"
"s_sub_u32 s9, s9, s95\n"
"buffer_load_dwordx4 v224, s[32:35], s82 offen offset:128 lds\n"
"v_mfma_f32_16x16x32_bf16 v[56:59], a[168:171], a[240:243], v[56:59]\n"
"s_addk_i32 m0, 7304\n"
"ds_read_b64_tr_b16 a[128:129], v215 offset:36896\n"
"v_sub_f32 v195, v195, v172\n"
"v_mfma_f32_16x16x32_bf16 v[56:59], a[164:167], a[224:227], v[56:59]\n"
"s_mov_b32 s82, 3211264\n"
"ds_read_b64_tr_b16 a[130:131], v215 offset:37152\n"
"v_cvt_pk_bf16_f32 v195, v194, v195\n"
"v_mfma_f32_16x16x32_bf16 v[60:63], a[172:175], a[224:227], v[60:63]\n"
"s_min_u32 s9, s9, 4\n"
"ds_read_b64_tr_b16 a[132:133], v215 offset:36904\n"
"v_lshlrev_b32 v194, 16, v195\n"
"v_mfma_f32_16x16x32_bf16 v[60:63], a[176:179], a[240:243], v[60:63]\n"
"s_add_u32 s16, s16, s9\n"
"ds_read_b64_tr_b16 a[134:135], v215 offset:37160\n"
"v_and_b32 v195, s0, v195\n"
"v_mfma_f32_16x16x32_bf16 v[52:55], a[176:179], a[236:239], v[52:55]\n"
"s_lshl_b32 s16, s16, 10\n"
"buffer_load_dwordx4 v224, s[32:35], s82 offen lds\n"
"v_mul_f32 v194, v194, v173\n"
"v_mfma_f32_16x16x32_bf16 v[52:55], a[172:175], a[220:223], v[52:55]\n"
"s_addk_i32 m0, 896\n"
"v_mul_f32 v195, v195, v173\n"
"v_cvt_pk_bf16_f32 v185, v194, v195\n"
"v_mfma_f32_16x16x32_bf16 v[44:47], a[172:175], a[216:219], v[44:47]\n"
"s_add_u32 s16, s16, s2\n"
"v_sub_f32 v196, v196, v172\n"
"v_sub_f32 v197, v197, v172\n"
"v_mfma_f32_16x16x32_bf16 v[44:47], a[176:179], a[232:235], v[44:47]\n"
"s_lshl_b32 s9, s83, 1\n"
"v_cvt_pk_bf16_f32 v197, v196, v197\n"
"v_lshlrev_b32 v196, 16, v197\n"
"v_mfma_f32_16x16x32_bf16 v[36:39], a[176:179], a[228:231], v[36:39]\n"
"s_add_u32 s9, s9, 1\n"
"v_and_b32 v197, s0, v197\n"
"v_mul_f32 v196, v196, v173\n"
"v_mfma_f32_16x16x32_bf16 v[36:39], a[172:175], a[212:215], v[36:39]\n"
"s_cmp_ge_u32 s9, 5\n"
"s_cselect_b32 s17, 1, 0\n"
"buffer_load_dwordx4 v224, s[32:35], s82 offen offset:128 lds\n"
"v_mfma_f32_16x16x32_bf16 v[64:67], a[180:183], a[212:215], v[64:67]\n"
"s_waitcnt vmcnt(14) lgkmcnt(4)\n"
"ds_read_b128 a[8:11], v212 offset:16384\n"
"v_mul_f32 v197, v197, v173\n"
"v_mfma_f32_16x16x32_bf16 v[64:67], a[184:187], a[228:231], v[64:67]\n"
"s_add_u32 m0, s77, 64944\n"
"ds_read_b128 a[24:27], v213 offset:16384\n"
"v_cvt_pk_bf16_f32 v186, v196, v197\n"
"v_mfma_f32_16x16x32_bf16 v[72:75], a[184:187], a[232:235], v[72:75]\n"
"s_waitcnt lgkmcnt(2)\n"
"v_sub_f32 v198, v198, v172\n"
"v_sub_f32 v199, v199, v172\n"
"v_mfma_f32_16x16x32_bf16 v[72:75], a[180:183], a[216:219], v[72:75]\n"
"s_barrier\n"
"ds_read_b64_tr_b16 a[148:149], v220 offset:32800\n"
"v_cvt_pk_bf16_f32 v199, v198, v199\n"
"v_mfma_f32_16x16x32_bf16 v[80:83], a[180:183], a[220:223], v[80:83]\n"
"s_mov_b32 s82, 3670016\n"
"buffer_load_dwordx4 v250, s[28:31], s86 offen offset:640 lds\n"
"v_mfma_f32_16x16x32_bf16 v[80:83], a[184:187], a[236:239], v[80:83]\n"
"s_addk_i32 m0, 1024\n"
"ds_read_b64_tr_b16 a[150:151], v220 offset:33056\n"
"v_lshlrev_b32 v198, 16, v199\n"
"v_mfma_f32_16x16x32_bf16 v[88:91], a[184:187], a[240:243], v[88:91]\n"
"s_add_u32 s17, s17, s8\n"
"ds_read_b64_tr_b16 a[152:153], v220 offset:36896\n"
"v_and_b32 v199, s0, v199\n"
"v_mfma_f32_16x16x32_bf16 v[88:91], a[180:183], a[224:227], v[88:91]\n"
"s_mul_i32 s17, s17, s10\n"
"ds_read_b64_tr_b16 a[154:155], v220 offset:37152\n"
"v_mul_f32 v198, v198, v173\n"
"v_mfma_f32_16x16x32_bf16 a[96:99], a[128:131], a[144:147], a[96:99]\n"
"s_add_u32 s17, s17, s96\n"
"ds_read_b64_tr_b16 a[156:157], v220 offset:32808\n"
"v_mul_f32 v199, v199, v173\n"
"v_mfma_f32_16x16x32_bf16 a[100:103], a[132:135], a[144:147], a[100:103]\n"
"s_mul_i32 s17, s17, 5\n"
"buffer_load_dwordx4 v251, s[28:31], s86 offen offset:640 lds\n"
"v_mfma_f32_16x16x32_bf16 v[92:95], a[188:191], a[224:227], v[92:95]\n"
"s_addk_i32 m0, 7168\n"
"ds_read_b64_tr_b16 a[158:159], v220 offset:33064\n"
"v_cvt_pk_bf16_f32 v187, v198, v199\n"
"v_mfma_f32_16x16x32_bf16 v[92:95], a[192:195], a[240:243], v[92:95]\n"
"s_cmp_ge_u32 s9, 6\n"
"s_cselect_b32 s95, 6, 0\n"
"ds_read_b64_tr_b16 a[160:161], v220 offset:36904\n"
"v_and_b32 v174, 31, v253\n"
"v_mfma_f32_16x16x32_bf16 v[84:87], a[192:195], a[236:239], v[84:87]\n"
"s_sub_u32 s9, s9, s95\n"
"ds_read_b64_tr_b16 a[162:163], v220 offset:37160\n"
"v_lshlrev_b32 v174, 7, v174\n"
"v_mfma_f32_16x16x32_bf16 v[84:87], a[188:191], a[220:223], v[84:87]\n"
"s_min_u32 s9, s9, 4\n"
"ds_read_b64_tr_b16 a[164:165], v220 offset:33312\n"
"v_lshrrev_b32 v175, 5, v253\n"
"v_mfma_f32_16x16x32_bf16 v[76:79], a[188:191], a[216:219], v[76:79]\n"
"s_add_u32 s17, s17, s9\n"
"buffer_load_dwordx4 v250, s[28:31], s87 offen offset:640 lds\n"
"v_mfma_f32_16x16x32_bf16 v[76:79], a[192:195], a[232:235], v[76:79]\n"
"s_addk_i32 m0, 1024\n"
"ds_read_b64_tr_b16 a[166:167], v220 offset:33568\n"
"v_add_u32 v175, s92, v175\n"
"v_mfma_f32_16x16x32_bf16 v[68:71], a[192:195], a[228:231], v[68:71]\n"
"s_lshl_b32 s17, s17, 10\n"
"ds_read_b64_tr_b16 a[168:169], v220 offset:37408\n"
"v_xor_b32 v175, v175, v253\n"
"v_mfma_f32_16x16x32_bf16 v[68:71], a[188:191], a[212:215], v[68:71]\n"
"s_waitcnt vmcnt(15) lgkmcnt(9)\n"
"ds_read_b128 a[12:15], v212 offset:24576\n"
"v_and_b32 v175, 7, v175\n"
"v_mfma_f32_16x16x32_bf16 v[96:99], a[196:199], a[212:215], v[96:99]\n"
"s_add_u32 s17, s17, s2\n"
"ds_read_b128 a[28:31], v213 offset:24576\n"
"v_lshl_add_u32 v174, v175, 4, v174\n"
"v_mfma_f32_16x16x32_bf16 v[96:99], a[200:203], a[228:231], v[96:99]\n"
"s_lshl_b32 s83, s97, 1\n"
"buffer_load_dwordx4 v251, s[28:31], s87 offen offset:640 lds\n"
"v_mfma_f32_16x16x32_bf16 v[104:107], a[200:203], a[232:235], v[104:107]\n"
"s_add_u32 m0, s77, 15632\n"
"ds_read_b64_tr_b16 a[170:171], v220 offset:37664\n"
"v_add_u32 v174, 147600, v174\n"
"v_mfma_f32_16x16x32_bf16 v[104:107], a[196:199], a[216:219], v[104:107]\n"
"s_add_u32 s83, s83, 1\n"
"ds_write_b128 v174, v[176:179] offset:0\n"
"v_lshlrev_b32 v238, 3, v253\n"
"v_mfma_f32_16x16x32_bf16 v[112:115], a[196:199], a[220:223], v[112:115]\n"
"s_mul_i32 s8, s83, 52\n"
"ds_write_b128 v174, v[180:183] offset:4096\n"
"v_mov_b32 v233, s81\n"
"v_mfma_f32_16x16x32_bf16 v[112:115], a[200:203], a[236:239], v[112:115]\n"
"s_lshr_b32 s8, s8, 8\n"
"ds_write_b128 v174, v[184:187] offset:8192\n"
"v_lshrrev_b32 v205, 3, v253\n"
"v_mfma_f32_16x16x32_bf16 v[120:123], a[200:203], a[240:243], v[120:123]\n"
"s_mul_i32 s9, s8, 5\n"
"buffer_load_dwordx4 v250, s[28:31], s84 offen offset:768 lds\n"
"v_mfma_f32_16x16x32_bf16 v[120:123], a[196:199], a[224:227], v[120:123]\n"
"s_addk_i32 m0, 1024\n"
"ds_read_b64_tr_b16 a[172:173], v220 offset:33320\n"
"v_and_b32 v207, 7, v253\n"
"v_mfma_f32_16x16x32_bf16 v[124:127], a[204:207], a[224:227], v[124:127]\n"
"s_sub_u32 s83, s83, s9\n"
"ds_read_b64_tr_b16 a[174:175], v220 offset:33576\n"
"v_xor_b32 v208, v208, v207\n"
"v_mfma_f32_16x16x32_bf16 v[124:127], a[208:211], a[240:243], v[124:127]\n"
"s_lshl_b32 s8, s8, 1\n"
"ds_read_b64_tr_b16 a[176:177], v220 offset:37416\n"
"v_lshlrev_b32 v208, 4, v208\n"
"v_mfma_f32_16x16x32_bf16 v[116:119], a[208:211], a[236:239], v[116:119]\n"
"s_lshl_b32 s9, s83, 1\n"
"ds_read_b64_tr_b16 a[178:179], v220 offset:37672\n"
"v_lshl_add_u32 v225, v205, 8, v208\n"
"v_mfma_f32_16x16x32_bf16 v[116:119], a[204:207], a[220:223], v[116:119]\n"
"s_add_u32 s9, s9, 0\n"
"buffer_load_dwordx4 v251, s[28:31], s84 offen offset:768 lds\n"
"v_mfma_f32_16x16x32_bf16 v[108:111], a[204:207], a[216:219], v[108:111]\n"
"s_addk_i32 m0, 7168\n"
"ds_read_b64_tr_b16 a[104:105], v214 offset:32800\n"
"v_add_u32 v225, s77, v225\n"
"v_mfma_f32_16x16x32_bf16 v[108:111], a[208:211], a[232:235], v[108:111]\n"
"s_cmp_ge_u32 s9, 5\n"
"s_cselect_b32 s18, 1, 0\n"
"ds_read_b64_tr_b16 a[106:107], v214 offset:33056\n"
"v_bfe_u32 v205, v253, 2, 2\n"
"v_mfma_f32_16x16x32_bf16 v[100:103], a[208:211], a[228:231], v[100:103]\n"
"s_add_u32 s18, s18, s8\n"
"ds_read_b64_tr_b16 a[108:109], v214 offset:32808\n"
"v_lshlrev_b32 v205, 10, v205\n"
"v_mfma_f32_16x16x32_bf16 v[100:103], a[204:207], a[212:215], v[100:103]\n"
"s_mul_i32 s18, s18, s10\n"
"ds_read_b64_tr_b16 a[110:111], v214 offset:33064\n"
"v_and_b32 v208, 3, v253\n"
"v_mfma_f32_16x16x32_bf16 v[0:3], a[148:151], a[0:3], v[0:3]\n"
"s_waitcnt vmcnt(12) lgkmcnt(9)\n"
"ds_read_b128 a[32:35], v212 offset:32800\n"
"v_lshl_add_u32 v205, v208, 7, v205\n"
"v_mfma_f32_16x16x32_bf16 v[0:3], a[152:155], a[16:19], v[0:3]\n"
"s_add_u32 s18, s18, s96\n"
"ds_read_b128 a[48:51], v213 offset:32800\n"
"v_lshl_add_u32 v222, v206, 4, v205\n"
"v_mfma_f32_16x16x32_bf16 v[8:11], a[152:155], a[20:23], v[8:11]\n"
"L_st2_%=:\n"
"s_cbranch_execz L_st6_%=\n"
"s_mul_i32 s18, s18, 5\n"
"buffer_load_dwordx4 v250, s[28:31], s85 offen offset:768 lds\n"
"v_mfma_f32_16x16x32_bf16 v[8:11], a[148:151], a[4:7], v[8:11]\n"
"s_addk_i32 m0, 1024\n"
"ds_read_b64_tr_b16 a[180:181], v220 offset:33824\n"
"v_and_b32 v205, 15, v253\n"
"v_mfma_f32_16x16x32_bf16 v[16:19], a[148:151], a[8:11], v[16:19]\n"
"s_cmp_ge_u32 s9, 6\n"
"s_cselect_b32 s95, 6, 0\n"
"ds_read_b64_tr_b16 a[182:183], v220 offset:34080\n"
"v_lshrrev_b32 v208, 4, v253\n"
"v_mfma_f32_16x16x32_bf16 v[16:19], a[152:155], a[24:27], v[16:19]\n"
"s_sub_u32 s9, s9, s95\n"
"ds_read_b64_tr_b16 a[184:185], v220 offset:37920\n"
"v_mul_u32_u24 v205, 1280, v205\n"
"v_mfma_f32_16x16x32_bf16 v[24:27], a[152:155], a[28:31], v[24:27]\n"
"s_min_u32 s9, s9, 4\n"
"ds_read_b64_tr_b16 a[186:187], v220 offset:38176\n"
"v_lshl_add_u32 v230, v208, 4, v205\n"
"v_mfma_f32_16x16x32_bf16 v[24:27], a[148:151], a[12:15], v[24:27]\n"
"s_waitcnt lgkmcnt(8)\n"
"buffer_load_dwordx4 v251, s[28:31], s85 offen offset:768 lds\n"
"v_mfma_f32_16x16x32_bf16 a[96:99], a[104:107], a[144:147], a[96:99]\n"
"s_waitcnt lgkmcnt(6)\n"
"ds_read_b64_tr_b16 a[188:189], v220 offset:33832\n"
"v_xor_b32 v223, 64, v222\n"
"v_mfma_f32_16x16x32_bf16 a[100:103], a[108:111], a[144:147], a[100:103]\n"
"s_add_u32 m0, s77, 131200\n"
"ds_read_b64_tr_b16 a[190:191], v220 offset:34088\n"
"v_lshl_add_u32 v226, v202, 8, v201\n"
"v_mfma_f32_16x16x32_bf16 v[28:31], a[156:159], a[12:15], v[28:31]\n"
"s_add_u32 s18, s18, s9\n"
"ds_read_b64_tr_b16 a[192:193], v220 offset:37928\n"
"v_mul_u32_u24 v228, 1280, v200\n"
"v_mfma_f32_16x16x32_bf16 v[28:31], a[160:163], a[28:31], v[28:31]\n"
"s_lshl_b32 s18, s18, 10\n"
"ds_read_b64_tr_b16 a[194:195], v220 offset:38184\n"
"v_add_u32 v228, v228, v201\n"
"v_mfma_f32_16x16x32_bf16 v[20:23], a[160:163], a[24:27], v[20:23]\n"
"s_add_u32 s18, s18, s2\n"
"buffer_load_dwordx4 v224, s[32:35], s82 offen lds\n"
"v_mfma_f32_16x16x32_bf16 v[20:23], a[156:159], a[8:11], v[20:23]\n"
"s_addk_i32 m0, 896\n"
"ds_read_b64_tr_b16 a[196:197], v220 offset:34336\n"
"v_add_u32 v229, s98, v220\n"
"v_mfma_f32_16x16x32_bf16 v[12:15], a[156:159], a[4:7], v[12:15]\n"
"s_lshl_b32 s9, s83, 1\n"
"ds_read_b64_tr_b16 a[198:199], v220 offset:34592\n"
"v_mul_u32_u24 v227, 3456, v202\n"
"v_mfma_f32_16x16x32_bf16 v[12:15], a[160:163], a[20:23], v[12:15]\n"
"s_add_u32 s9, s9, 1\n"
"ds_read_b64_tr_b16 a[200:201], v220 offset:38432\n"
"v_add_u32 v227, v227, v201\n"
"v_mfma_f32_16x16x32_bf16 v[4:7], a[160:163], a[16:19], v[4:7]\n"
"s_cmp_ge_u32 s9, 5\n"
"s_cselect_b32 s19, 1, 0\n"
"ds_read_b64_tr_b16 a[202:203], v220 offset:38688\n"
"v_add_u32 v217, 61440, v216\n"
"v_mfma_f32_16x16x32_bf16 v[4:7], a[156:159], a[0:3], v[4:7]\n"
"s_add_u32 s19, s19, s8\n"
"buffer_load_dwordx4 v224, s[32:35], s82 offen offset:128 lds\n"
"v_mfma_f32_16x16x32_bf16 v[32:35], a[164:167], a[0:3], v[32:35]\n"
"s_waitcnt vmcnt(14) lgkmcnt(8)\n"
"ds_read_b128 a[36:39], v212 offset:40992\n"
"s_addk_i32 m0, 7304\n"
"v_mfma_f32_16x16x32_bf16 v[32:35], a[168:171], a[16:19], v[32:35]\n"
"s_mov_b32 s82, 3735552\n"
"ds_read_b128 a[52:55], v213 offset:40992\n"
"v_add_u32 v218, 122880, v216\n"
"v_mfma_f32_16x16x32_bf16 v[40:43], a[168:171], a[20:23], v[40:43]\n"
"s_mul_i32 s19, s19, s10\n"
"ds_read_b64_tr_b16 a[204:205], v220 offset:34344\n"
"s_add_u32 s19, s19, s96\n"
"v_mfma_f32_16x16x32_bf16 v[40:43], a[164:167], a[4:7], v[40:43]\n"
"s_mul_i32 s19, s19, 5\n"
"ds_read_b64_tr_b16 a[206:207], v220 offset:34600\n"
"s_cmp_ge_u32 s9, 6\n"
"s_cselect_b32 s95, 6, 0\n"
"v_mfma_f32_16x16x32_bf16 v[48:51], a[164:167], a[8:11], v[48:51]\n"
"s_sub_u32 s9, s9, s95\n"
"buffer_load_dwordx4 v224, s[32:35], s82 offen lds\n"
"v_mfma_f32_16x16x32_bf16 v[48:51], a[168:171], a[24:27], v[48:51]\n"
"s_addk_i32 m0, 896\n"
"ds_read_b64_tr_b16 a[208:209], v220 offset:38440\n"
"s_min_u32 s9, s9, 4\n"
"v_mfma_f32_16x16x32_bf16 v[56:59], a[168:171], a[28:31], v[56:59]\n"
"s_add_u32 s19, s19, s9\n"
"ds_read_b64_tr_b16 a[210:211], v220 offset:38696\n"
"s_lshl_b32 s19, s19, 10\n"
"v_mfma_f32_16x16x32_bf16 v[56:59], a[164:167], a[12:15], v[56:59]\n"
"s_add_u32 s19, s19, s2\n"
"ds_read_b64_tr_b16 a[112:113], v214 offset:36896\n"
"s_lshl_b32 s95, s73, 2\n"
"v_mfma_f32_16x16x32_bf16 v[60:63], a[172:175], a[12:15], v[60:63]\n"
"s_add_u32 s95, s95, s72\n"
"ds_read_b64_tr_b16 a[114:115], v214 offset:37152\n"
"s_cmp_lt_u32 s95, 256\n"
"s_cselect_b32 s56, s52, s56\n"
"s_cselect_b32 s57, s53, s57\n"
"s_cselect_b32 s58, s54, s58\n"
"s_cselect_b32 s10, 128, 54\n"
"s_mov_b32 s83, 33816576\n"
"s_cselect_b32 s2, 28573696, s83\n"
"s_cselect_b32 s83, 1280, 0\n"
"v_mfma_f32_16x16x32_bf16 v[60:63], a[176:179], a[28:31], v[60:63]\n"
"s_add_u32 s95, s95, s83\n"
"buffer_load_dwordx4 v224, s[32:35], s82 offen offset:128 lds\n"
"v_mfma_f32_16x16x32_bf16 v[52:55], a[176:179], a[24:27], v[52:55]\n"
"s_add_u32 m0, s77, 32016\n"
"ds_read_b64_tr_b16 a[116:117], v214 offset:36904\n"
"s_sub_u32 s95, s95, 256\n"
"v_mfma_f32_16x16x32_bf16 v[52:55], a[172:175], a[8:11], v[52:55]\n"
"s_mul_i32 s83, s10, 10\n"
"ds_read_b64_tr_b16 a[118:119], v214 offset:37160\n"
"s_cmp_lt_u32 s95, s83\n"
"s_cselect_b32 s2, s2, 0x40000000\n"
"v_mfma_f32_16x16x32_bf16 v[44:47], a[172:175], a[4:7], v[44:47]\n"
"s_mul_i32 s96, s95, 6554\n"
"s_lshr_b32 s96, s96, 16\n"
"s_mul_i32 s83, s96, 10\n"
"v_mfma_f32_16x16x32_bf16 v[44:47], a[176:179], a[20:23], v[44:47]\n"
"s_sub_u32 s97, s95, s83\n"
"s_mul_i32 s83, s96, 40960\n"
"s_lshl_b32 s8, s97, 7\n"
"v_mfma_f32_16x16x32_bf16 v[36:39], a[176:179], a[16:19], v[36:39]\n"
"s_add_u32 s25, s83, s8\n"
"buffer_load_dwordx4 v250, s[28:31], s86 offen offset:768 lds\n"
"v_mfma_f32_16x16x32_bf16 v[36:39], a[172:175], a[0:3], v[36:39]\n"
"s_waitcnt vmcnt(11) lgkmcnt(6)\n"
"ds_read_b128 a[40:43], v212 offset:49184\n"
"s_addk_i32 m0, 1024\n"
"v_mfma_f32_16x16x32_bf16 v[64:67], a[180:183], a[0:3], v[64:67]\n"
"s_mov_b32 s82, 4194304\n"
"ds_read_b128 a[56:59], v213 offset:49184\n"
"v_lshrrev_b32 v175, 3, v253\n"
"v_mfma_f32_16x16x32_bf16 v[64:67], a[184:187], a[16:19], v[64:67]\n"
"s_add_u32 s92, s76, 147600\n"
"v_add_u32 v174, s92, v216\n"
"v_xor_b32 v172, v175, v253\n"
"v_mfma_f32_16x16x32_bf16 v[72:75], a[184:187], a[20:23], v[72:75]\n"
"s_lshl_b32 s83, s97, 1\n"
"v_lshlrev_b32 v175, 8, v175\n"
"v_and_b32 v172, 7, v172\n"
"v_mfma_f32_16x16x32_bf16 v[72:75], a[180:183], a[4:7], v[72:75]\n"
"s_add_u32 s83, s83, 0\n"
"buffer_load_dwordx4 v251, s[28:31], s86 offen offset:768 lds\n"
"v_mfma_f32_16x16x32_bf16 v[80:83], a[180:183], a[8:11], v[80:83]\n"
"s_waitcnt lgkmcnt(2)\n"
"s_barrier\n"
"ds_read_b64_tr_b16 a[148:149], v221 offset:0\n"
"v_mfma_f32_16x16x32_bf16 v[80:83], a[184:187], a[24:27], v[80:83]\n"
"s_addk_i32 m0, 7168\n"
"ds_read_b64_tr_b16 a[150:151], v221 offset:256\n"
"v_lshl_add_u32 v175, v172, 4, v175\n"
"v_mfma_f32_16x16x32_bf16 v[88:91], a[184:187], a[28:31], v[88:91]\n"
"s_mul_i32 s8, s83, 52\n"
"ds_read_b64_tr_b16 a[152:153], v221 offset:4096\n"
"s_lshr_b32 s8, s8, 8\n"
"v_mfma_f32_16x16x32_bf16 v[88:91], a[180:183], a[12:15], v[88:91]\n"
"s_mul_i32 s9, s8, 5\n"
"ds_read_b64_tr_b16 a[154:155], v221 offset:4352\n"
"s_sub_u32 s83, s83, s9\n"
"v_mfma_f32_16x16x32_bf16 a[96:99], a[112:115], a[144:147], a[96:99]\n"
"s_lshl_b32 s8, s8, 1\n"
"buffer_load_dwordx4 v250, s[28:31], s87 offen offset:768 lds\n"
"v_mfma_f32_16x16x32_bf16 a[100:103], a[116:119], a[144:147], a[100:103]\n"
"s_addk_i32 m0, 1024\n"
"ds_read_b64_tr_b16 a[156:157], v221 offset:8\n"
"s_lshl_b32 s9, s83, 1\n"
"v_mfma_f32_16x16x32_bf16 v[92:95], a[188:191], a[12:15], v[92:95]\n"
"s_add_u32 s9, s9, 0\n"
"ds_read_b64_tr_b16 a[158:159], v221 offset:264\n"
"s_cmp_ge_u32 s9, 5\n"
"s_cselect_b32 s20, 1, 0\n"
"v_mfma_f32_16x16x32_bf16 v[92:95], a[192:195], a[28:31], v[92:95]\n"
"s_add_u32 s20, s20, s8\n"
"ds_read_b64_tr_b16 a[160:161], v221 offset:4104\n"
"s_mul_i32 s20, s20, s10\n"
"v_mfma_f32_16x16x32_bf16 v[84:87], a[192:195], a[24:27], v[84:87]\n"
"s_add_u32 s20, s20, s96\n"
"ds_read_b64_tr_b16 a[162:163], v221 offset:4360\n"
"s_mul_i32 s20, s20, 5\n"
"v_mfma_f32_16x16x32_bf16 v[84:87], a[188:191], a[8:11], v[84:87]\n"
"s_cmp_ge_u32 s9, 6\n"
"s_cselect_b32 s95, 6, 0\n"
"buffer_load_dwordx4 v251, s[28:31], s87 offen offset:768 lds\n"
"v_mfma_f32_16x16x32_bf16 v[76:79], a[188:191], a[4:7], v[76:79]\n"
"s_addk_i32 m0, 7072\n"
"ds_read_b64_tr_b16 a[164:165], v221 offset:512\n"
"s_sub_u32 s9, s9, s95\n"
"v_mfma_f32_16x16x32_bf16 v[76:79], a[192:195], a[20:23], v[76:79]\n"
"s_min_u32 s9, s9, 4\n"
"ds_read_b64_tr_b16 a[166:167], v221 offset:768\n"
"s_add_u32 s20, s20, s9\n"
"v_mfma_f32_16x16x32_bf16 v[68:71], a[192:195], a[16:19], v[68:71]\n"
"s_lshl_b32 s20, s20, 10\n"
"ds_read_b64_tr_b16 a[168:169], v221 offset:4608\n"
"s_add_u32 s20, s20, s2\n"
"v_mfma_f32_16x16x32_bf16 v[68:71], a[188:191], a[0:3], v[68:71]\n"
"s_waitcnt vmcnt(12) lgkmcnt(9)\n"
"ds_read_b128 a[44:47], v212 offset:57376\n"
"s_lshl_b32 s9, s83, 1\n"
"v_mfma_f32_16x16x32_bf16 v[96:99], a[196:199], a[0:3], v[96:99]\n"
"s_add_u32 s9, s9, 1\n"
"buffer_load_dwordx4 v250, s[28:31], s84 offen offset:896 lds\n"
"v_mfma_f32_16x16x32_bf16 v[96:99], a[200:203], a[16:19], v[96:99]\n"
"s_addk_i32 m0, 1024\n"
"ds_read_b128 a[60:63], v213 offset:57376\n"
"s_cmp_ge_u32 s9, 5\n"
"s_cselect_b32 s21, 1, 0\n"
"v_mfma_f32_16x16x32_bf16 v[104:107], a[200:203], a[20:23], v[104:107]\n"
"s_add_u32 s21, s21, s8\n"
"ds_read_b64_tr_b16 a[170:171], v221 offset:4864\n"
"s_mul_i32 s21, s21, s10\n"
"v_mfma_f32_16x16x32_bf16 v[104:107], a[196:199], a[4:7], v[104:107]\n"
"s_add_u32 s21, s21, s96\n"
"ds_read_b64_tr_b16 a[172:173], v221 offset:520\n"
"s_mul_i32 s21, s21, 5\n"
"v_mfma_f32_16x16x32_bf16 v[112:115], a[196:199], a[8:11], v[112:115]\n"
"s_cmp_ge_u32 s9, 6\n"
"s_cselect_b32 s95, 6, 0\n"
"ds_read_b64_tr_b16 a[174:175], v221 offset:776\n"
"s_sub_u32 s9, s9, s95\n"
"v_mfma_f32_16x16x32_bf16 v[112:115], a[200:203], a[24:27], v[112:115]\n"
"s_min_u32 s9, s9, 4\n"
"buffer_load_dwordx4 v251, s[28:31], s84 offen offset:896 lds\n"
"v_mfma_f32_16x16x32_bf16 v[120:123], a[200:203], a[28:31], v[120:123]\n"
"s_addk_i32 m0, 7168\n"
"ds_read_b64_tr_b16 a[176:177], v221 offset:4616\n"
"s_add_u32 s21, s21, s9\n"
"v_mfma_f32_16x16x32_bf16 v[120:123], a[196:199], a[12:15], v[120:123]\n"
"s_lshl_b32 s21, s21, 10\n"
"ds_read_b64_tr_b16 a[178:179], v221 offset:4872\n"
"s_add_u32 s21, s21, s2\n"
"v_mfma_f32_16x16x32_bf16 v[124:127], a[204:207], a[12:15], v[124:127]\n"
"s_lshl_b32 s83, s97, 1\n"
"ds_read_b64_tr_b16 a[120:121], v215 offset:0\n"
"s_add_u32 s83, s83, 1\n"
"v_mfma_f32_16x16x32_bf16 v[124:127], a[208:211], a[28:31], v[124:127]\n"
"s_mul_i32 s8, s83, 52\n"
"ds_read_b64_tr_b16 a[122:123], v215 offset:256\n"
"s_lshr_b32 s8, s8, 8\n"
"v_mfma_f32_16x16x32_bf16 v[116:119], a[208:211], a[24:27], v[116:119]\n"
"s_mul_i32 s9, s8, 5\n"
"buffer_load_dwordx4 v250, s[28:31], s85 offen offset:896 lds\n"
"v_mfma_f32_16x16x32_bf16 v[116:119], a[204:207], a[8:11], v[116:119]\n"
"s_addk_i32 m0, 1024\n"
"ds_read_b64_tr_b16 a[124:125], v215 offset:8\n"
"s_sub_u32 s83, s83, s9\n"
"v_mfma_f32_16x16x32_bf16 v[108:111], a[204:207], a[4:7], v[108:111]\n"
"s_lshl_b32 s8, s8, 1\n"
"ds_read_b64_tr_b16 a[126:127], v215 offset:264\n"
"s_lshl_b32 s9, s83, 1\n"
"v_mfma_f32_16x16x32_bf16 v[108:111], a[208:211], a[20:23], v[108:111]\n"
"s_add_u32 s9, s9, 0\n"
"ds_read_b128 v[176:179], v174 offset:0\n"
"s_cmp_ge_u32 s9, 5\n"
"s_cselect_b32 s22, 1, 0\n"
"v_mfma_f32_16x16x32_bf16 v[100:103], a[208:211], a[16:19], v[100:103]\n"
"s_add_u32 s22, s22, s8\n"
"ds_read_b128 v[180:183], v174 offset:4096\n"
"s_mul_i32 s22, s22, s10\n"
"v_mfma_f32_16x16x32_bf16 v[100:103], a[204:207], a[0:3], v[100:103]\n"
"s_add_u32 s22, s22, s96\n"
"buffer_load_dwordx4 v251, s[28:31], s85 offen offset:896 lds\n"
"v_mfma_f32_16x16x32_bf16 v[0:3], a[148:151], a[32:35], v[0:3]\n"
"s_waitcnt vmcnt(14) lgkmcnt(8)\n"
"ds_read_b128 a[64:67], v212 offset:0\n"
"s_addk_i32 m0, 24480\n"
"v_mfma_f32_16x16x32_bf16 v[0:3], a[152:155], a[48:51], v[0:3]\n"
"s_mul_i32 s22, s22, 5\n"
"ds_read_b128 a[80:83], v213 offset:0\n"
"s_cmp_ge_u32 s9, 6\n"
"s_cselect_b32 s95, 6, 0\n"
"v_mfma_f32_16x16x32_bf16 v[8:11], a[152:155], a[52:55], v[8:11]\n"
"s_sub_u32 s9, s9, s95\n"
"ds_read_b128 v[184:187], v174 offset:8192\n"
"s_min_u32 s9, s9, 4\n"
"v_mfma_f32_16x16x32_bf16 v[8:11], a[148:151], a[36:39], v[8:11]\n"
"s_add_u32 s22, s22, s9\n"
"ds_read_b64_tr_b16 a[180:181], v221 offset:1024\n"
"s_lshl_b32 s22, s22, 10\n"
"v_mfma_f32_16x16x32_bf16 v[16:19], a[148:151], a[40:43], v[16:19]\n"
"s_add_u32 s22, s22, s2\n"
"buffer_load_dwordx4 v224, s[32:35], s82 offen lds\n"
"v_mfma_f32_16x16x32_bf16 v[16:19], a[152:155], a[56:59], v[16:19]\n"
"s_addk_i32 m0, 896\n"
"ds_read_b64_tr_b16 a[182:183], v221 offset:1280\n"
"s_lshl_b32 s9, s83, 1\n"
"v_mfma_f32_16x16x32_bf16 v[24:27], a[152:155], a[60:63], v[24:27]\n"
"s_add_u32 s9, s9, 1\n"
"ds_read_b64_tr_b16 a[184:185], v221 offset:5120\n"
"s_cmp_ge_u32 s9, 5\n"
"s_cselect_b32 s23, 1, 0\n"
"v_mfma_f32_16x16x32_bf16 v[24:27], a[148:151], a[44:47], v[24:27]\n"
"s_waitcnt lgkmcnt(9)\n"
"ds_read_b64_tr_b16 a[186:187], v221 offset:5376\n"
"s_add_u32 s23, s23, s8\n"
"v_mfma_f32_16x16x32_bf16 a[96:99], a[120:123], a[144:147], a[96:99]\n"
"s_waitcnt lgkmcnt(9)\n"
"ds_read_b64_tr_b16 a[188:189], v221 offset:1032\n"
"s_mul_i32 s23, s23, s10\n"
"v_mfma_f32_16x16x32_bf16 a[100:103], a[124:127], a[144:147], a[100:103]\n"
"s_add_u32 s23, s23, s96\n"
"buffer_load_dwordx4 v224, s[32:35], s82 offen offset:128 lds\n"
"v_mfma_f32_16x16x32_bf16 v[28:31], a[156:159], a[44:47], v[28:31]\n"
"s_addk_i32 m0, 7304\n"
"ds_read_b64_tr_b16 a[190:191], v221 offset:1288\n"
"s_mov_b32 s82, 4259840\n"
"v_mfma_f32_16x16x32_bf16 v[28:31], a[160:163], a[60:63], v[28:31]\n"
"s_mul_i32 s23, s23, 5\n"
"ds_read_b64_tr_b16 a[192:193], v221 offset:5128\n"
"s_cmp_ge_u32 s9, 6\n"
"s_cselect_b32 s95, 6, 0\n"
"v_mfma_f32_16x16x32_bf16 v[20:23], a[160:163], a[56:59], v[20:23]\n"
"s_sub_u32 s9, s9, s95\n"
"ds_read_b64_tr_b16 a[194:195], v221 offset:5384\n"
"s_min_u32 s9, s9, 4\n"
"v_mfma_f32_16x16x32_bf16 v[20:23], a[156:159], a[40:43], v[20:23]\n"
"s_add_u32 s23, s23, s9\n"
"ds_read_b64_tr_b16 a[196:197], v221 offset:1536\n"
"s_lshl_b32 s23, s23, 10\n"
"v_mfma_f32_16x16x32_bf16 v[12:15], a[156:159], a[36:39], v[12:15]\n"
"s_add_u32 s23, s23, s2\n"
"buffer_load_dwordx4 v224, s[32:35], s82 offen lds\n"
"v_mfma_f32_16x16x32_bf16 v[12:15], a[160:163], a[52:55], v[12:15]\n"
"s_addk_i32 m0, 896\n"
"ds_read_b64_tr_b16 a[198:199], v221 offset:1792\n"
"s_lshr_b32 s92, s1, 1\n"
"v_mfma_f32_16x16x32_bf16 v[4:7], a[160:163], a[48:51], v[4:7]\n"
"s_mul_i32 s92, s92, 21504\n"
"ds_read_b64_tr_b16 a[200:201], v221 offset:5632\n"
"s_and_b32 s93, s1, 1\n"
"v_mfma_f32_16x16x32_bf16 v[4:7], a[156:159], a[32:35], v[4:7]\n"
"s_waitcnt vmcnt(15) lgkmcnt(9)\n"
"ds_read_b128 a[68:71], v212 offset:8192\n"
"s_lshl_b32 s93, s93, 7\n"
"v_mfma_f32_16x16x32_bf16 v[32:35], a[164:167], a[32:35], v[32:35]\n"
"s_add_u32 s92, s92, s93\n"
"ds_read_b128 a[84:87], v213 offset:8192\n"
"s_add_u32 s92, s92, s77\n"
"v_mfma_f32_16x16x32_bf16 v[32:35], a[168:171], a[48:51], v[32:35]\n"
"s_add_u32 s89, s92, 1179648\n"
"buffer_load_dwordx4 v224, s[32:35], s82 offen offset:128 lds\n"
"v_mfma_f32_16x16x32_bf16 v[40:43], a[168:171], a[52:55], v[40:43]\n"
"s_addk_i32 m0, 39128\n"
"ds_read_b64_tr_b16 a[202:203], v221 offset:5888\n"
"s_add_u32 s90, s92, 1187840\n"
"v_mfma_f32_16x16x32_bf16 v[40:43], a[164:167], a[36:39], v[40:43]\n"
"s_add_u32 s91, s92, 1196032\n"
"ds_read_b64_tr_b16 a[204:205], v221 offset:1544\n"
"s_cmp_lt_u32 s72, 2\n"
"s_cselect_b32 s91, s91, 0x40000000\n"
"v_mfma_f32_16x16x32_bf16 v[48:51], a[164:167], a[40:43], v[48:51]\n"
"ds_read_b64_tr_b16 a[206:207], v221 offset:1800\n"
"v_mfma_f32_16x16x32_bf16 v[48:51], a[168:171], a[56:59], v[48:51]\n"
"ds_read_b64_tr_b16 a[208:209], v221 offset:5640\n"
"v_mfma_f32_16x16x32_bf16 v[56:59], a[168:171], a[60:63], v[56:59]\n"
"buffer_load_dwordx4 v250, s[28:31], s86 offen offset:896 lds\n"
"v_mfma_f32_16x16x32_bf16 v[56:59], a[164:167], a[44:47], v[56:59]\n"
"s_addk_i32 m0, 1024\n"
"ds_read_b64_tr_b16 a[210:211], v221 offset:5896\n"
"v_mfma_f32_16x16x32_bf16 v[60:63], a[172:175], a[44:47], v[60:63]\n"
"ds_read_b64_tr_b16 a[128:129], v215 offset:4096\n"
"v_mfma_f32_16x16x32_bf16 v[60:63], a[176:179], a[60:63], v[60:63]\n"
"ds_read_b64_tr_b16 a[130:131], v215 offset:4352\n"
"v_mfma_f32_16x16x32_bf16 v[52:55], a[176:179], a[56:59], v[52:55]\n"
"ds_read_b64_tr_b16 a[132:133], v215 offset:4104\n"
"v_mfma_f32_16x16x32_bf16 v[52:55], a[172:175], a[40:43], v[52:55]\n"
"buffer_load_dwordx4 v251, s[28:31], s86 offen offset:896 lds\n"
"v_mfma_f32_16x16x32_bf16 v[44:47], a[172:175], a[36:39], v[44:47]\n"
"s_addk_i32 m0, 7168\n"
"ds_read_b64_tr_b16 a[134:135], v215 offset:4360\n"
"v_mfma_f32_16x16x32_bf16 v[44:47], a[176:179], a[52:55], v[44:47]\n"
"v_mfma_f32_16x16x32_bf16 v[36:39], a[176:179], a[48:51], v[36:39]\n"
"v_mfma_f32_16x16x32_bf16 v[36:39], a[172:175], a[32:35], v[36:39]\n"
"s_waitcnt vmcnt(12) lgkmcnt(7)\n"
"ds_read_b128 a[72:75], v212 offset:16384\n"
"v_bfe_u32 v239, v253, 1, 3\n"
"v_mfma_f32_16x16x32_bf16 v[64:67], a[180:183], a[32:35], v[64:67]\n"
"s_mov_b32 s82, 4718592\n"
"buffer_load_dwordx4 v250, s[28:31], s87 offen offset:896 lds\n"
"v_mfma_f32_16x16x32_bf16 v[64:67], a[184:187], a[48:51], v[64:67]\n"
"s_addk_i32 m0, 1024\n"
"ds_read_b128 a[88:91], v213 offset:16384\n"
"v_and_b32 v240, 8, v238\n"
"v_mfma_f32_16x16x32_bf16 v[72:75], a[184:187], a[52:55], v[72:75]\n"
"s_xor_b32 s4, s26, 0\n"
"v_lshl_add_u32 v239, v239, 7, v240\n"
"s_lshl_b32 s4, s4, 10\n"
"v_mfma_f32_16x16x32_bf16 v[72:75], a[180:183], a[36:39], v[72:75]\n"
"s_add_u32 s4, s4, 262144\n"
"s_xor_b32 s5, s26, 1\n"
"s_lshl_b32 s5, s5, 10\n"
"v_mfma_f32_16x16x32_bf16 v[80:83], a[180:183], a[40:43], v[80:83]\n"
"s_add_u32 s5, s5, 262144\n"
"s_xor_b32 s6, s26, 2\n"
"s_lshl_b32 s6, s6, 10\n"
"v_mfma_f32_16x16x32_bf16 v[80:83], a[184:187], a[56:59], v[80:83]\n"
"s_add_u32 s6, s6, 262144\n"
"buffer_load_dwordx2 v[240:241], v239, s[60:63], s4 offen sc1\n"
"v_mfma_f32_16x16x32_bf16 v[88:91], a[184:187], a[60:63], v[88:91]\n"
"s_waitcnt lgkmcnt(2)\n"
"s_xor_b32 s7, s26, 3\n"
"s_lshl_b32 s7, s7, 10\n"
"v_mfma_f32_16x16x32_bf16 v[88:91], a[180:183], a[44:47], v[88:91]\n"
"s_add_u32 s7, s7, 262144\n"
"s_barrier\n"
"ds_read_b64_tr_b16 a[148:149], v221 offset:16400\n"
"v_mfma_f32_16x16x32_bf16 a[96:99], a[128:131], a[144:147], a[96:99]\n"
"ds_read_b64_tr_b16 a[150:151], v221 offset:16656\n"
"v_mfma_f32_16x16x32_bf16 a[100:103], a[132:135], a[144:147], a[100:103]\n"
"ds_read_b64_tr_b16 a[152:153], v221 offset:20496\n"
"v_mfma_f32_16x16x32_bf16 v[92:95], a[188:191], a[44:47], v[92:95]\n"
"buffer_load_dwordx2 v[242:243], v239, s[60:63], s5 offen sc1\n"
"v_mfma_f32_16x16x32_bf16 v[92:95], a[192:195], a[60:63], v[92:95]\n"
"ds_read_b64_tr_b16 a[154:155], v221 offset:20752\n"
"v_mfma_f32_16x16x32_bf16 v[84:87], a[192:195], a[56:59], v[84:87]\n"
"ds_read_b64_tr_b16 a[156:157], v221 offset:16408\n"
"v_mfma_f32_16x16x32_bf16 v[84:87], a[188:191], a[40:43], v[84:87]\n"
"ds_read_b64_tr_b16 a[158:159], v221 offset:16664\n"
"v_mfma_f32_16x16x32_bf16 v[76:79], a[188:191], a[36:39], v[76:79]\n"
"ds_read_b64_tr_b16 a[160:161], v221 offset:20504\n"
"v_mfma_f32_16x16x32_bf16 v[76:79], a[192:195], a[52:55], v[76:79]\n"
"buffer_load_dwordx2 v[244:245], v239, s[60:63], s6 offen sc1\n"
"v_mfma_f32_16x16x32_bf16 v[68:71], a[192:195], a[48:51], v[68:71]\n"
"ds_read_b64_tr_b16 a[162:163], v221 offset:20760\n"
"v_mfma_f32_16x16x32_bf16 v[68:71], a[188:191], a[32:35], v[68:71]\n"
"s_waitcnt vmcnt(14) lgkmcnt(8)\n"
"ds_read_b128 a[76:79], v212 offset:24576\n"
"v_mfma_f32_16x16x32_bf16 v[96:99], a[196:199], a[32:35], v[96:99]\n"
"ds_read_b128 a[92:95], v213 offset:24576\n"
"v_mfma_f32_16x16x32_bf16 v[96:99], a[200:203], a[48:51], v[96:99]\n"
"ds_read_b64_tr_b16 a[164:165], v221 offset:16912\n"
"v_mfma_f32_16x16x32_bf16 v[104:107], a[200:203], a[52:55], v[104:107]\n"
"buffer_load_dwordx2 v[246:247], v239, s[60:63], s7 offen sc1\n"
"v_mfma_f32_16x16x32_bf16 v[104:107], a[196:199], a[36:39], v[104:107]\n"
"ds_read_b64_tr_b16 a[166:167], v221 offset:17168\n"
"v_mfma_f32_16x16x32_bf16 v[112:115], a[196:199], a[40:43], v[112:115]\n"
"ds_read_b64_tr_b16 a[168:169], v221 offset:21008\n"
"v_mfma_f32_16x16x32_bf16 v[112:115], a[200:203], a[56:59], v[112:115]\n"
"ds_read_b64_tr_b16 a[170:171], v221 offset:21264\n"
"v_mfma_f32_16x16x32_bf16 v[120:123], a[200:203], a[60:63], v[120:123]\n"
"ds_read_b64_tr_b16 a[172:173], v221 offset:16920\n"
"v_mfma_f32_16x16x32_bf16 v[120:123], a[196:199], a[44:47], v[120:123]\n"
"buffer_load_dwordx4 v251, s[28:31], s87 offen offset:896 lds\n"
"v_mfma_f32_16x16x32_bf16 v[124:127], a[204:207], a[44:47], v[124:127]\n"
"s_add_u32 m0, s77, 15376\n"
"ds_read_b64_tr_b16 a[174:175], v221 offset:17176\n"
"v_mfma_f32_16x16x32_bf16 v[124:127], a[208:211], a[60:63], v[124:127]\n"
"ds_read_b64_tr_b16 a[176:177], v221 offset:21016\n"
"v_mfma_f32_16x16x32_bf16 v[116:119], a[208:211], a[56:59], v[116:119]\n"
"ds_read_b64_tr_b16 a[178:179], v221 offset:21272\n"
"v_mfma_f32_16x16x32_bf16 v[116:119], a[204:207], a[40:43], v[116:119]\n"
"ds_read_b64_tr_b16 a[104:105], v215 offset:16400\n"
"v_mfma_f32_16x16x32_bf16 v[108:111], a[204:207], a[36:39], v[108:111]\n"
"ds_read_b64_tr_b16 a[106:107], v215 offset:16656\n"
"v_mfma_f32_16x16x32_bf16 v[108:111], a[208:211], a[52:55], v[108:111]\n"
"ds_read_b64_tr_b16 a[108:109], v215 offset:16408\n"
"v_mfma_f32_16x16x32_bf16 v[100:103], a[208:211], a[48:51], v[100:103]\n"
"ds_read_b64_tr_b16 a[110:111], v215 offset:16664\n"
"v_mfma_f32_16x16x32_bf16 v[100:103], a[204:207], a[32:35], v[100:103]\n"
"s_waitcnt lgkmcnt(10)\n"
"ds_read_b64_tr_b16 a[112:113], v215 offset:20496\n"
"v_mfma_f32_16x16x32_bf16 v[0:3], a[148:151], a[64:67], v[0:3]\n"
"ds_read_b64_tr_b16 a[114:115], v215 offset:20752\n"
"v_mfma_f32_16x16x32_bf16 v[0:3], a[152:155], a[80:83], v[0:3]\n"
"ds_read_b64_tr_b16 a[116:117], v215 offset:20504\n"
"ds_read_b64_tr_b16 a[118:119], v215 offset:20760\n"
"v_mfma_f32_16x16x32_bf16 v[8:11], a[152:155], a[84:87], v[8:11]\n"
"s_waitcnt vmcnt(14) lgkmcnt(11)\n"
"ds_read_b128 a[212:215], v212 offset:32800\n"
"v_mfma_f32_16x16x32_bf16 v[8:11], a[148:151], a[68:71], v[8:11]\n"
"ds_read_b128 a[228:231], v213 offset:32800\n"
"v_mfma_f32_16x16x32_bf16 v[16:19], a[148:151], a[72:75], v[16:19]\n"
"buffer_load_dwordx4 v250, s[28:31], s84 offen offset:1024 lds\n"
"v_mfma_f32_16x16x32_bf16 v[16:19], a[152:155], a[88:91], v[16:19]\n"
"s_addk_i32 m0, 1024\n"
"ds_read_b64_tr_b16 a[180:181], v221 offset:17424\n"
"v_mfma_f32_16x16x32_bf16 v[24:27], a[152:155], a[92:95], v[24:27]\n"
"ds_read_b64_tr_b16 a[182:183], v221 offset:17680\n"
"v_mfma_f32_16x16x32_bf16 v[24:27], a[148:151], a[76:79], v[24:27]\n"
"s_waitcnt lgkmcnt(10)\n"
"ds_read_b64_tr_b16 a[184:185], v221 offset:21520\n"
"v_mfma_f32_16x16x32_bf16 a[96:99], a[104:107], a[144:147], a[96:99]\n"
"s_waitcnt lgkmcnt(9)\n"
"ds_read_b64_tr_b16 a[186:187], v221 offset:21776\n"
"v_mfma_f32_16x16x32_bf16 a[100:103], a[108:111], a[144:147], a[100:103]\n"
"s_waitcnt lgkmcnt(8)\n"
"buffer_load_dwordx4 v251, s[28:31], s84 offen offset:1024 lds\n"
"v_mfma_f32_16x16x32_bf16 a[96:99], a[112:115], a[144:147], a[96:99]\n"
"s_waitcnt lgkmcnt(6)\n"
"ds_read_b64_tr_b16 a[188:189], v221 offset:17432\n"
"s_addk_i32 m0, 7168\n"
"v_mfma_f32_16x16x32_bf16 a[100:103], a[116:119], a[144:147], a[100:103]\n"
"ds_read_b64_tr_b16 a[190:191], v221 offset:17688\n"
"v_mfma_f32_16x16x32_bf16 v[28:31], a[156:159], a[76:79], v[28:31]\n"
"ds_read_b64_tr_b16 a[192:193], v221 offset:21528\n"
"v_mfma_f32_16x16x32_bf16 v[28:31], a[160:163], a[92:95], v[28:31]\n"
"ds_read_b64_tr_b16 a[194:195], v221 offset:21784\n"
"v_mfma_f32_16x16x32_bf16 v[20:23], a[160:163], a[88:91], v[20:23]\n"
"buffer_load_dwordx4 v250, s[28:31], s85 offen offset:1024 lds\n"
"v_mfma_f32_16x16x32_bf16 v[20:23], a[156:159], a[72:75], v[20:23]\n"
"s_addk_i32 m0, 1024\n"
"ds_read_b64_tr_b16 a[196:197], v221 offset:17936\n"
"v_mfma_f32_16x16x32_bf16 v[12:15], a[156:159], a[68:71], v[12:15]\n"
"ds_read_b64_tr_b16 a[198:199], v221 offset:18192\n"
"v_mfma_f32_16x16x32_bf16 v[12:15], a[160:163], a[84:87], v[12:15]\n"
"ds_read_b64_tr_b16 a[200:201], v221 offset:22032\n"
"v_mfma_f32_16x16x32_bf16 v[4:7], a[160:163], a[80:83], v[4:7]\n"
"s_waitcnt vmcnt(15) lgkmcnt(9)\n"
"ds_read_b128 a[216:219], v212 offset:40992\n"
"v_mfma_f32_16x16x32_bf16 v[4:7], a[156:159], a[64:67], v[4:7]\n"
"buffer_load_dwordx4 v251, s[28:31], s85 offen offset:1024 lds\n"
"v_mfma_f32_16x16x32_bf16 v[32:35], a[164:167], a[64:67], v[32:35]\n"
"s_add_u32 m0, s77, 98400\n"
"ds_read_b128 a[232:235], v213 offset:40992\n"
"v_mfma_f32_16x16x32_bf16 v[32:35], a[168:171], a[80:83], v[32:35]\n"
"ds_read_b64_tr_b16 a[202:203], v221 offset:22288\n"
"v_mfma_f32_16x16x32_bf16 v[40:43], a[168:171], a[84:87], v[40:43]\n"
"ds_read_b64_tr_b16 a[204:205], v221 offset:17944\n"
"v_mfma_f32_16x16x32_bf16 v[40:43], a[164:167], a[68:71], v[40:43]\n"
"ds_read_b64_tr_b16 a[206:207], v221 offset:18200\n"
"v_mfma_f32_16x16x32_bf16 v[48:51], a[164:167], a[72:75], v[48:51]\n"
"buffer_load_dwordx4 v224, s[32:35], s82 offen lds\n"
"v_mfma_f32_16x16x32_bf16 v[48:51], a[168:171], a[88:91], v[48:51]\n"
"s_addk_i32 m0, 896\n"
"ds_read_b64_tr_b16 a[208:209], v221 offset:22040\n"
"v_mfma_f32_16x16x32_bf16 v[56:59], a[168:171], a[92:95], v[56:59]\n"
"ds_read_b64_tr_b16 a[210:211], v221 offset:22296\n"
"v_mfma_f32_16x16x32_bf16 v[56:59], a[164:167], a[76:79], v[56:59]\n"
"ds_read_b64_tr_b16 a[120:121], v215 offset:32800\n"
"v_mfma_f32_16x16x32_bf16 v[60:63], a[172:175], a[76:79], v[60:63]\n"
"ds_read_b64_tr_b16 a[122:123], v215 offset:33056\n"
"v_mfma_f32_16x16x32_bf16 v[60:63], a[176:179], a[92:95], v[60:63]\n"
"buffer_load_dwordx4 v224, s[32:35], s82 offen offset:128 lds\n"
"v_mfma_f32_16x16x32_bf16 v[52:55], a[176:179], a[88:91], v[52:55]\n"
"s_addk_i32 m0, 7304\n"
"ds_read_b64_tr_b16 a[124:125], v215 offset:32808\n"
"s_mov_b32 s82, 4784128\n"
"v_mfma_f32_16x16x32_bf16 v[52:55], a[172:175], a[72:75], v[52:55]\n"
"ds_read_b64_tr_b16 a[126:127], v215 offset:33064\n"
"v_mfma_f32_16x16x32_bf16 v[44:47], a[172:175], a[68:71], v[44:47]\n"
"ds_read_b64_tr_b16 a[128:129], v215 offset:36896\n"
"v_mfma_f32_16x16x32_bf16 v[44:47], a[176:179], a[84:87], v[44:47]\n"
"ds_read_b64_tr_b16 a[130:131], v215 offset:37152\n"
"v_mfma_f32_16x16x32_bf16 v[36:39], a[176:179], a[80:83], v[36:39]\n"
"s_waitcnt vmcnt(12) lgkmcnt(9)\n"
"ds_read_b128 a[220:223], v212 offset:49184\n"
"v_mfma_f32_16x16x32_bf16 v[36:39], a[172:175], a[64:67], v[36:39]\n"
"ds_read_b64_tr_b16 a[132:133], v215 offset:36904\n"
"v_mfma_f32_16x16x32_bf16 v[64:67], a[180:183], a[64:67], v[64:67]\n"
"ds_read_b64_tr_b16 a[134:135], v215 offset:37160\n"
"v_mfma_f32_16x16x32_bf16 v[64:67], a[184:187], a[80:83], v[64:67]\n"
"ds_read_b128 a[236:239], v213 offset:49184\n"
"v_mfma_f32_16x16x32_bf16 v[72:75], a[184:187], a[84:87], v[72:75]\n"
"buffer_load_dwordx4 v224, s[32:35], s82 offen lds\n"
"v_mfma_f32_16x16x32_bf16 v[72:75], a[180:183], a[68:71], v[72:75]\n"
"s_addk_i32 m0, 896\n"
"ds_read_b64_tr_b16 a[148:149], v221 offset:32800\n"
"v_mfma_f32_16x16x32_bf16 v[80:83], a[180:183], a[72:75], v[80:83]\n"
"ds_read_b64_tr_b16 a[150:151], v221 offset:33056\n"
"v_mfma_f32_16x16x32_bf16 v[80:83], a[184:187], a[88:91], v[80:83]\n"
"ds_read_b64_tr_b16 a[152:153], v221 offset:36896\n"
"v_mfma_f32_16x16x32_bf16 v[88:91], a[184:187], a[92:95], v[88:91]\n"
"ds_read_b64_tr_b16 a[154:155], v221 offset:37152\n"
"v_mfma_f32_16x16x32_bf16 v[88:91], a[180:183], a[76:79], v[88:91]\n"
"s_waitcnt lgkmcnt(10)\n"
"buffer_load_dwordx4 v224, s[32:35], s82 offen offset:128 lds\n"
"v_mfma_f32_16x16x32_bf16 a[96:99], a[120:123], a[144:147], a[96:99]\n"
"s_add_u32 m0, s77, 31760\n"
"ds_read_b64_tr_b16 a[156:157], v221 offset:32808\n"
"v_mfma_f32_16x16x32_bf16 a[100:103], a[124:127], a[144:147], a[100:103]\n"
"s_waitcnt lgkmcnt(9)\n"
"ds_read_b64_tr_b16 a[158:159], v221 offset:33064\n"
"v_mfma_f32_16x16x32_bf16 a[96:99], a[128:131], a[144:147], a[96:99]\n"
"s_waitcnt lgkmcnt(6)\n"
"s_barrier\n"
"ds_read_b64_tr_b16 a[160:161], v221 offset:36904\n"
"v_mfma_f32_16x16x32_bf16 a[100:103], a[132:135], a[144:147], a[100:103]\n"
"ds_read_b64_tr_b16 a[162:163], v221 offset:37160\n"
"v_mfma_f32_16x16x32_bf16 v[92:95], a[188:191], a[76:79], v[92:95]\n"
"buffer_load_dwordx4 v250, s[28:31], s86 offen offset:1024 lds\n"
"v_mfma_f32_16x16x32_bf16 v[92:95], a[192:195], a[92:95], v[92:95]\n"
"s_addk_i32 m0, 1024\n"
"ds_read_b64_tr_b16 a[164:165], v221 offset:33312\n"
"v_mfma_f32_16x16x32_bf16 v[84:87], a[192:195], a[88:91], v[84:87]\n"
"ds_read_b64_tr_b16 a[166:167], v221 offset:33568\n"
"v_mfma_f32_16x16x32_bf16 v[84:87], a[188:191], a[72:75], v[84:87]\n"
"ds_read_b64_tr_b16 a[168:169], v221 offset:37408\n"
"v_mfma_f32_16x16x32_bf16 v[76:79], a[188:191], a[68:71], v[76:79]\n"
"s_waitcnt vmcnt(9) lgkmcnt(9)\n"
"ds_read_b128 a[224:227], v212 offset:57376\n"
"v_mfma_f32_16x16x32_bf16 v[76:79], a[192:195], a[84:87], v[76:79]\n"
"buffer_load_dwordx4 v251, s[28:31], s86 offen offset:1024 lds\n"
"v_mfma_f32_16x16x32_bf16 v[68:71], a[192:195], a[80:83], v[68:71]\n"
"s_addk_i32 m0, 7168\n"
"ds_read_b128 a[240:243], v213 offset:57376\n"
"v_mfma_f32_16x16x32_bf16 v[68:71], a[188:191], a[64:67], v[68:71]\n"
"ds_read_b64_tr_b16 a[170:171], v221 offset:37664\n"
"v_mfma_f32_16x16x32_bf16 v[96:99], a[196:199], a[64:67], v[96:99]\n"
"ds_read_b64_tr_b16 a[172:173], v221 offset:33320\n"
"v_mfma_f32_16x16x32_bf16 v[96:99], a[200:203], a[80:83], v[96:99]\n"
"L_st3_%=:\n"
"s_cbranch_execz L_st7_%=\n"
"ds_read_b64_tr_b16 a[174:175], v221 offset:33576\n"
"v_mfma_f32_16x16x32_bf16 v[104:107], a[200:203], a[84:87], v[104:107]\n"
"buffer_load_dwordx4 v250, s[28:31], s87 offen offset:1024 lds\n"
"v_mfma_f32_16x16x32_bf16 v[104:107], a[196:199], a[68:71], v[104:107]\n"
"s_addk_i32 m0, 1024\n"
"ds_read_b64_tr_b16 a[176:177], v221 offset:37416\n"
"v_mfma_f32_16x16x32_bf16 v[112:115], a[196:199], a[72:75], v[112:115]\n"
"ds_read_b64_tr_b16 a[178:179], v221 offset:37672\n"
"v_mfma_f32_16x16x32_bf16 v[112:115], a[200:203], a[88:91], v[112:115]\n"
"ds_read_b64_tr_b16 a[104:105], v214 offset:32800\n"
"v_mfma_f32_16x16x32_bf16 v[120:123], a[200:203], a[92:95], v[120:123]\n"
"ds_read_b64_tr_b16 a[106:107], v214 offset:33056\n"
"v_mfma_f32_16x16x32_bf16 v[120:123], a[196:199], a[76:79], v[120:123]\n"
"buffer_load_dwordx4 v251, s[28:31], s87 offen offset:1024 lds\n"
"v_mfma_f32_16x16x32_bf16 v[124:127], a[204:207], a[76:79], v[124:127]\n"
"s_addk_i32 m0, 7072\n"
"ds_read_b64_tr_b16 a[108:109], v214 offset:32808\n"
"v_mfma_f32_16x16x32_bf16 v[124:127], a[208:211], a[92:95], v[124:127]\n"
"ds_read_b64_tr_b16 a[110:111], v214 offset:33064\n"
"v_mfma_f32_16x16x32_bf16 v[116:119], a[208:211], a[88:91], v[116:119]\n"
"ds_read_b64_tr_b16 a[112:113], v214 offset:36896\n"
"v_mfma_f32_16x16x32_bf16 v[116:119], a[204:207], a[72:75], v[116:119]\n"
"s_waitcnt vmcnt(10) lgkmcnt(9)\n"
"ds_read_b128 a[0:3], v212 offset:0\n"
"v_mfma_f32_16x16x32_bf16 v[108:111], a[204:207], a[68:71], v[108:111]\n"
"buffer_load_dwordx4 v250, s[28:31], s84 offen offset:1152 lds\n"
"v_mfma_f32_16x16x32_bf16 v[108:111], a[208:211], a[84:87], v[108:111]\n"
"s_addk_i32 m0, 1024\n"
"ds_read_b128 a[16:19], v213 offset:0\n"
"v_mfma_f32_16x16x32_bf16 v[100:103], a[208:211], a[80:83], v[100:103]\n"
"ds_read_b64_tr_b16 a[114:115], v214 offset:37152\n"
"v_mfma_f32_16x16x32_bf16 v[100:103], a[204:207], a[64:67], v[100:103]\n"
"ds_read_b64_tr_b16 a[116:117], v214 offset:36904\n"
"v_mfma_f32_16x16x32_bf16 v[0:3], a[148:151], a[212:215], v[0:3]\n"
"ds_read_b64_tr_b16 a[118:119], v214 offset:37160\n"
"v_mfma_f32_16x16x32_bf16 v[0:3], a[152:155], a[228:231], v[0:3]\n"
"buffer_load_dwordx4 v251, s[28:31], s84 offen offset:1152 lds\n"
"v_mfma_f32_16x16x32_bf16 v[8:11], a[152:155], a[232:235], v[8:11]\n"
"s_addk_i32 m0, 7168\n"
"ds_read_b64_tr_b16 a[180:181], v221 offset:33824\n"
"v_mfma_f32_16x16x32_bf16 v[8:11], a[148:151], a[216:219], v[8:11]\n"
"ds_read_b64_tr_b16 a[182:183], v221 offset:34080\n"
"v_mfma_f32_16x16x32_bf16 v[16:19], a[148:151], a[220:223], v[16:19]\n"
"ds_read_b64_tr_b16 a[184:185], v221 offset:37920\n"
"v_mfma_f32_16x16x32_bf16 v[16:19], a[152:155], a[236:239], v[16:19]\n"
"ds_read_b64_tr_b16 a[186:187], v221 offset:38176\n"
"v_mfma_f32_16x16x32_bf16 v[24:27], a[152:155], a[240:243], v[24:27]\n"
"buffer_load_dwordx4 v250, s[28:31], s85 offen offset:1152 lds\n"
"v_mfma_f32_16x16x32_bf16 v[24:27], a[148:151], a[224:227], v[24:27]\n"
"s_waitcnt lgkmcnt(8)\n"
"ds_read_b64_tr_b16 a[188:189], v221 offset:33832\n"
"s_addk_i32 m0, 1024\n"
"v_mfma_f32_16x16x32_bf16 a[96:99], a[104:107], a[144:147], a[96:99]\n"
"ds_read_b64_tr_b16 a[190:191], v221 offset:34088\n"
"v_mfma_f32_16x16x32_bf16 a[100:103], a[108:111], a[144:147], a[100:103]\n"
"s_waitcnt lgkmcnt(8)\n"
"ds_read_b64_tr_b16 a[192:193], v221 offset:37928\n"
"v_mfma_f32_16x16x32_bf16 a[96:99], a[112:115], a[144:147], a[96:99]\n"
"s_waitcnt lgkmcnt(7)\n"
"ds_read_b64_tr_b16 a[194:195], v221 offset:38184\n"
"v_mfma_f32_16x16x32_bf16 a[100:103], a[116:119], a[144:147], a[100:103]\n"
"s_waitcnt vmcnt(7)\n"
"s_barrier\n"
"ds_read_b64_tr_b16 a[196:197], v221 offset:34336\n"
"v_mfma_f32_16x16x32_bf16 v[28:31], a[156:159], a[224:227], v[28:31]\n"
"buffer_load_dwordx4 v251, s[28:31], s85 offen offset:1152 lds\n"
"v_mfma_f32_16x16x32_bf16 v[28:31], a[160:163], a[240:243], v[28:31]\n"
"s_addk_i32 m0, 7168\n"
"ds_read_b64_tr_b16 a[198:199], v221 offset:34592\n"
"v_mfma_f32_16x16x32_bf16 v[20:23], a[160:163], a[236:239], v[20:23]\n"
"ds_read_b128 a[4:7], v212 offset:8192\n"
"v_mfma_f32_16x16x32_bf16 v[20:23], a[156:159], a[220:223], v[20:23]\n"
"ds_read_b128 a[20:23], v213 offset:8192\n"
"v_mfma_f32_16x16x32_bf16 v[12:15], a[156:159], a[216:219], v[12:15]\n"
"ds_read_b64_tr_b16 a[200:201], v221 offset:38432\n"
"v_mfma_f32_16x16x32_bf16 v[12:15], a[160:163], a[232:235], v[12:15]\n"
"buffer_load_dwordx4 v250, s[28:31], s86 offen offset:1152 lds\n"
"v_mfma_f32_16x16x32_bf16 v[4:7], a[160:163], a[228:231], v[4:7]\n"
"s_addk_i32 m0, 1024\n"
"ds_read_b64_tr_b16 a[202:203], v221 offset:38688\n"
"v_mfma_f32_16x16x32_bf16 v[4:7], a[156:159], a[212:215], v[4:7]\n"
"ds_read_b64_tr_b16 a[204:205], v221 offset:34344\n"
"v_mfma_f32_16x16x32_bf16 v[32:35], a[164:167], a[212:215], v[32:35]\n"
"ds_read_b64_tr_b16 a[206:207], v221 offset:34600\n"
"v_mfma_f32_16x16x32_bf16 v[32:35], a[168:171], a[228:231], v[32:35]\n"
"ds_read_b64_tr_b16 a[208:209], v221 offset:38440\n"
"v_mfma_f32_16x16x32_bf16 v[40:43], a[168:171], a[232:235], v[40:43]\n"
"buffer_load_dwordx4 v251, s[28:31], s86 offen offset:1152 lds\n"
"v_mfma_f32_16x16x32_bf16 v[40:43], a[164:167], a[216:219], v[40:43]\n"
"s_addk_i32 m0, 7168\n"
"ds_read_b64_tr_b16 a[210:211], v221 offset:38696\n"
"v_mfma_f32_16x16x32_bf16 v[48:51], a[164:167], a[220:223], v[48:51]\n"
"ds_read_b64_tr_b16 a[120:121], v215 offset:0\n"
"v_mfma_f32_16x16x32_bf16 v[48:51], a[168:171], a[236:239], v[48:51]\n"
"ds_read_b64_tr_b16 a[122:123], v215 offset:256\n"
"v_mfma_f32_16x16x32_bf16 v[56:59], a[168:171], a[240:243], v[56:59]\n"
"ds_read_b64_tr_b16 a[124:125], v215 offset:8\n"
"v_mfma_f32_16x16x32_bf16 v[56:59], a[164:167], a[224:227], v[56:59]\n"
"buffer_store_dwordx4 v[176:179], v175, s[60:63], s89 offen sc1\n"
"v_mfma_f32_16x16x32_bf16 v[60:63], a[172:175], a[224:227], v[60:63]\n"
"ds_read_b64_tr_b16 a[126:127], v215 offset:264\n"
"v_mfma_f32_16x16x32_bf16 v[60:63], a[176:179], a[240:243], v[60:63]\n"
"s_waitcnt vmcnt(9) lgkmcnt(9)\n"
"ds_read_b128 a[8:11], v212 offset:16384\n"
"v_mfma_f32_16x16x32_bf16 v[52:55], a[176:179], a[236:239], v[52:55]\n"
"ds_read_b128 a[24:27], v213 offset:16384\n"
"v_mfma_f32_16x16x32_bf16 v[52:55], a[172:175], a[220:223], v[52:55]\n"
"ds_read_b64_tr_b16 a[128:129], v215 offset:4096\n"
"v_mfma_f32_16x16x32_bf16 v[44:47], a[172:175], a[216:219], v[44:47]\n"
"buffer_store_dwordx4 v[180:183], v175, s[60:63], s90 offen sc1\n"
"v_mfma_f32_16x16x32_bf16 v[44:47], a[176:179], a[232:235], v[44:47]\n"
"ds_read_b64_tr_b16 a[130:131], v215 offset:4352\n"
"v_mfma_f32_16x16x32_bf16 v[36:39], a[176:179], a[228:231], v[36:39]\n"
"ds_read_b64_tr_b16 a[132:133], v215 offset:4104\n"
"v_mfma_f32_16x16x32_bf16 v[36:39], a[172:175], a[212:215], v[36:39]\n"
"v_cmp_eq_u32_e64 s[4:5], v240, s80\n"
"v_cmp_eq_u32_e64 s[6:7], v242, s80\n"
"s_nop 1\n"
"s_and_b64 s[4:5], s[4:5], s[6:7]\n"
"v_cmp_eq_u32_e64 s[6:7], v244, s80\n"
"s_nop 1\n"
"s_and_b64 s[4:5], s[4:5], s[6:7]\n"
"v_cmp_eq_u32_e64 s[6:7], v246, s80\n"
"s_nop 1\n"
"s_and_b64 s[4:5], s[4:5], s[6:7]\n"
"s_cmp_eq_u64 s[4:5], -1\n"
"s_cbranch_scc0 L_spinG_%=\n"
"L_retG_%=:\n"
"ds_read_b64_tr_b16 a[134:135], v215 offset:4360\n"
"v_lshlrev_b32 v160, 16, v241\n"
"v_mfma_f32_16x16x32_bf16 v[64:67], a[180:183], a[212:215], v[64:67]\n"
"s_mov_b32 s82, 0\n"
"ds_read_b64_tr_b16 a[148:149], v220 offset:32800\n"
"v_sub_f32 v160, 0, v160\n"
"v_mfma_f32_16x16x32_bf16 v[64:67], a[184:187], a[228:231], v[64:67]\n"
"s_add_u32 s92, s77, 147600\n"
"buffer_store_dwordx4 v[184:187], v175, s[60:63], s91 offen sc1\n"
"v_mfma_f32_16x16x32_bf16 v[72:75], a[184:187], a[232:235], v[72:75]\n"
"ds_read_b64_tr_b16 a[150:151], v220 offset:33056\n"
"v_add_u32 v174, s92, v216\n"
"v_mfma_f32_16x16x32_bf16 v[72:75], a[180:183], a[216:219], v[72:75]\n"
"s_waitcnt lgkmcnt(6)\n"
"ds_read_b64_tr_b16 a[152:153], v220 offset:36896\n"
"s_barrier\n"
"v_mfma_f32_16x16x32_bf16 v[80:83], a[180:183], a[220:223], v[80:83]\n"
"ds_read_b64_tr_b16 a[154:155], v220 offset:37152\n"
"v_lshrrev_b32 v175, 4, v253\n"
"v_mfma_f32_16x16x32_bf16 v[80:83], a[184:187], a[236:239], v[80:83]\n"
"ds_read_b64_tr_b16 a[156:157], v220 offset:32808\n"
"v_lshlrev_b32 v175, 8, v175\n"
"v_mfma_f32_16x16x32_bf16 v[88:91], a[184:187], a[240:243], v[88:91]\n"
"buffer_load_dwordx4 v250, s[28:31], s87 offen offset:1152 lds\n"
"v_mfma_f32_16x16x32_bf16 v[88:91], a[180:183], a[224:227], v[88:91]\n"
"s_addk_i32 m0, 1024\n"
"ds_read_b64_tr_b16 a[158:159], v220 offset:33064\n"
"v_add_u32 v175, 147600, v175\n"
"v_mfma_f32_16x16x32_bf16 a[96:99], a[120:123], a[144:147], a[96:99]\n"
"ds_read_b64_tr_b16 a[160:161], v220 offset:36904\n"
"v_lshlrev_b32 v161, 16, v243\n"
"v_mfma_f32_16x16x32_bf16 a[100:103], a[124:127], a[144:147], a[100:103]\n"
"s_waitcnt lgkmcnt(9)\n"
"ds_read_b64_tr_b16 a[162:163], v220 offset:37160\n"
"v_sub_f32 v161, 0, v161\n"
"v_mfma_f32_16x16x32_bf16 a[96:99], a[128:131], a[144:147], a[96:99]\n"
"s_waitcnt lgkmcnt(8)\n"
"ds_read_b64_tr_b16 a[164:165], v220 offset:33312\n"
"v_lshlrev_b32 v162, 16, v245\n"
"v_mfma_f32_16x16x32_bf16 a[100:103], a[132:135], a[144:147], a[100:103]\n"
"buffer_load_dwordx4 v251, s[28:31], s87 offen offset:1152 lds\n"
"v_mfma_f32_16x16x32_bf16 v[92:95], a[188:191], a[224:227], v[92:95]\n"
"s_waitcnt vmcnt(11) lgkmcnt(8)\n"
"ds_read_b128 a[12:15], v212 offset:24576\n"
"v_sub_f32 v162, 0, v162\n"
"v_mfma_f32_16x16x32_bf16 v[92:95], a[192:195], a[240:243], v[92:95]\n"
"s_add_u32 m0, s76, 0\n"
"ds_read_b128 a[28:31], v213 offset:24576\n"
"v_lshlrev_b32 v163, 16, v247\n"
"v_mfma_f32_16x16x32_bf16 v[84:87], a[192:195], a[236:239], v[84:87]\n"
"ds_read_b64_tr_b16 a[166:167], v220 offset:33568\n"
"v_sub_f32 v163, 0, v163\n"
"v_mfma_f32_16x16x32_bf16 v[84:87], a[188:191], a[220:223], v[84:87]\n"
"ds_read_b64_tr_b16 a[168:169], v220 offset:37408\n"
"v_and_b32 v234, 0xffff0000, v241\n"
"v_mfma_f32_16x16x32_bf16 v[76:79], a[188:191], a[216:219], v[76:79]\n"
"buffer_load_dwordx4 v225, s[44:47], s82 offen lds\n"
"v_mfma_f32_16x16x32_bf16 v[76:79], a[192:195], a[232:235], v[76:79]\n"
"s_addk_i32 m0, 8064\n"
"ds_read_b64_tr_b16 a[170:171], v220 offset:37664\n"
"v_and_b32 v235, 0xffff0000, v243\n"
"v_mfma_f32_16x16x32_bf16 v[68:71], a[192:195], a[228:231], v[68:71]\n"
"ds_write_b128 v174, a[96:99] offset:0\n"
"v_and_b32 v236, 0xffff0000, v245\n"
"v_mfma_f32_16x16x32_bf16 v[68:71], a[188:191], a[212:215], v[68:71]\n"
"ds_write_b128 v174, a[100:103] offset:1024\n"
"v_and_b32 v237, 0xffff0000, v247\n"
"v_mfma_f32_16x16x32_bf16 v[96:99], a[196:199], a[212:215], v[96:99]\n"
"ds_read_b64_tr_b16 a[172:173], v220 offset:33320\n"
"v_mfma_f32_16x16x32_bf16 v[96:99], a[200:203], a[228:231], v[96:99]\n"
"buffer_load_dwordx4 v225, s[44:47], s82 offen offset:128 lds\n"
"v_mfma_f32_16x16x32_bf16 v[104:107], a[200:203], a[232:235], v[104:107]\n"
"s_addk_i32 m0, 61568\n"
"ds_read_b64_tr_b16 a[174:175], v220 offset:33576\n"
"s_movk_i32 s82, 8192\n"
"v_mfma_f32_16x16x32_bf16 v[104:107], a[196:199], a[216:219], v[104:107]\n"
"ds_read_b64_tr_b16 a[176:177], v220 offset:37416\n"
"v_mfma_f32_16x16x32_bf16 v[112:115], a[196:199], a[220:223], v[112:115]\n"
"ds_read_b64_tr_b16 a[178:179], v220 offset:37672\n"
"v_mfma_f32_16x16x32_bf16 v[112:115], a[200:203], a[236:239], v[112:115]\n"
"v_rcp_f32 v234, v234\n"
"v_mfma_f32_16x16x32_bf16 v[120:123], a[200:203], a[240:243], v[120:123]\n"
"buffer_load_dwordx4 v225, s[44:47], s82 offen lds\n"
"v_mfma_f32_16x16x32_bf16 v[120:123], a[196:199], a[224:227], v[120:123]\n"
"s_waitcnt vmcnt(12) lgkmcnt(7)\n"
"ds_read_b128 a[32:35], v212 offset:32800\n"
"v_mul_f32 v234, 0x3e0293ee, v234\n"
"v_mfma_f32_16x16x32_bf16 v[124:127], a[204:207], a[224:227], v[124:127]\n"
"s_addk_i32 m0, 8064\n"
"ds_read_b128 a[48:51], v213 offset:32800\n"
"v_mfma_f32_16x16x32_bf16 v[124:127], a[208:211], a[240:243], v[124:127]\n"
"v_rcp_f32 v235, v235\n"
"v_mfma_f32_16x16x32_bf16 v[116:119], a[208:211], a[236:239], v[116:119]\n"
"v_mul_f32 v235, 0x3e0293ee, v235\n"
"v_mfma_f32_16x16x32_bf16 v[116:119], a[204:207], a[220:223], v[116:119]\n"
"buffer_load_dwordx4 v225, s[44:47], s82 offen offset:128 lds\n"
"v_mfma_f32_16x16x32_bf16 v[108:111], a[204:207], a[216:219], v[108:111]\n"
"s_add_u32 m0, s77, 114800\n"
"v_rcp_f32 v236, v236\n"
"v_mfma_f32_16x16x32_bf16 v[108:111], a[208:211], a[232:235], v[108:111]\n"
"s_mov_b32 s82, 0\n"
"v_mul_f32 v236, 0x3e0293ee, v236\n"
"v_mfma_f32_16x16x32_bf16 v[100:103], a[208:211], a[228:231], v[100:103]\n"
"v_rcp_f32 v237, v237\n"
"v_mfma_f32_16x16x32_bf16 v[100:103], a[204:207], a[212:215], v[100:103]\n"
"v_mul_f32 v237, 0x3e0293ee, v237\n"
"v_mfma_f32_16x16x32_bf16 v[0:3], a[148:151], a[0:3], v[0:3]\n"
"buffer_load_dwordx4 v226, s[48:51], s82 offen lds\n"
"v_mfma_f32_16x16x32_bf16 v[0:3], a[152:155], a[16:19], v[0:3]\n"
"s_waitcnt lgkmcnt(0)\n"
"s_barrier\n"
"ds_read_b128 v[128:131], v175 offset:0\n"
"v_mfma_f32_16x16x32_bf16 v[8:11], a[152:155], a[20:23], v[8:11]\n"
"s_addk_i32 m0, 896\n"
"ds_read_b128 v[132:135], v175 offset:1024\n"
"v_mfma_f32_16x16x32_bf16 v[8:11], a[148:151], a[4:7], v[8:11]\n"
"ds_read_b128 v[136:139], v175 offset:2048\n"
"v_mfma_f32_16x16x32_bf16 v[16:19], a[148:151], a[8:11], v[16:19]\n"
"ds_read_b128 v[140:143], v175 offset:3072\n"
"v_mfma_f32_16x16x32_bf16 v[16:19], a[152:155], a[24:27], v[16:19]\n"
"buffer_load_dwordx4 v226, s[48:51], s82 offen offset:128 lds\n"
"v_mfma_f32_16x16x32_bf16 v[24:27], a[152:155], a[28:31], v[24:27]\n"
"s_waitcnt vmcnt(13)\n"
"ds_read_b128 a[36:39], v212 offset:40992\n"
"s_addk_i32 m0, 7304\n"
"v_mfma_f32_16x16x32_bf16 v[24:27], a[148:151], a[12:15], v[24:27]\n"
"s_movk_i32 s82, 2048\n"
"ds_read_b128 a[52:55], v213 offset:40992\n"
"v_mfma_f32_16x16x32_bf16 v[28:31], a[156:159], a[12:15], v[28:31]\n"
"ds_read_b128 v[144:147], v175 offset:4096\n"
"v_mfma_f32_16x16x32_bf16 v[28:31], a[160:163], a[28:31], v[28:31]\n"
"ds_read_b128 v[148:151], v175 offset:5120\n"
"v_mfma_f32_16x16x32_bf16 v[20:23], a[160:163], a[24:27], v[20:23]\n"
"buffer_load_dwordx4 v226, s[48:51], s82 offen lds\n"
"v_mfma_f32_16x16x32_bf16 v[20:23], a[156:159], a[8:11], v[20:23]\n"
"s_waitcnt lgkmcnt(7)\n"
"ds_read_b128 v[152:155], v175 offset:6144\n"
"v_fmac_f32 v0, v160, v128\n"
"v_mfma_f32_16x16x32_bf16 v[12:15], a[156:159], a[4:7], v[12:15]\n"
"s_addk_i32 m0, 896\n"
"ds_read_b128 v[156:159], v175 offset:7168\n"
"v_fmac_f32 v1, v160, v129\n"
"v_mfma_f32_16x16x32_bf16 v[12:15], a[160:163], a[20:23], v[12:15]\n"
"ds_read_b64_tr_b16 a[180:181], v220 offset:33824\n"
"v_fmac_f32 v2, v160, v130\n"
"v_mfma_f32_16x16x32_bf16 v[4:7], a[160:163], a[16:19], v[4:7]\n"
"ds_read_b64_tr_b16 a[182:183], v220 offset:34080\n"
"v_fmac_f32 v3, v160, v131\n"
"v_mfma_f32_16x16x32_bf16 v[4:7], a[156:159], a[0:3], v[4:7]\n"
"buffer_load_dwordx4 v226, s[48:51], s82 offen offset:128 lds\n"
"v_mfma_f32_16x16x32_bf16 v[32:35], a[164:167], a[0:3], v[32:35]\n"
"ds_read_b64_tr_b16 a[184:185], v220 offset:37920\n"
"v_fmac_f32 v8, v161, v128\n"
"v_mfma_f32_16x16x32_bf16 v[32:35], a[168:171], a[16:19], v[32:35]\n"
"ds_read_b64_tr_b16 a[186:187], v220 offset:38176\n"
"v_fmac_f32 v9, v161, v129\n"
"v_mfma_f32_16x16x32_bf16 v[40:43], a[168:171], a[20:23], v[40:43]\n"
"ds_read_b64_tr_b16 a[188:189], v220 offset:33832\n"
"v_fmac_f32 v10, v161, v130\n"
"v_mfma_f32_16x16x32_bf16 v[40:43], a[164:167], a[4:7], v[40:43]\n"
"s_waitcnt lgkmcnt(9)\n"
"ds_read_b64_tr_b16 a[190:191], v220 offset:34088\n"
"v_fmac_f32 v4, v160, v132\n"
"v_mfma_f32_16x16x32_bf16 v[48:51], a[164:167], a[8:11], v[48:51]\n"
"ds_read_b64_tr_b16 a[192:193], v220 offset:37928\n"
"v_fmac_f32 v5, v160, v133\n"
"v_mfma_f32_16x16x32_bf16 v[48:51], a[168:171], a[24:27], v[48:51]\n"
"s_waitcnt vmcnt(13) lgkmcnt(9)\n"
"ds_read_b128 a[40:43], v212 offset:49184\n"
"v_fmac_f32 v6, v160, v134\n"
"v_mfma_f32_16x16x32_bf16 v[56:59], a[168:171], a[28:31], v[56:59]\n"
"ds_read_b64_tr_b16 a[194:195], v220 offset:38184\n"
"v_fmac_f32 v7, v160, v135\n"
"v_mfma_f32_16x16x32_bf16 v[56:59], a[164:167], a[12:15], v[56:59]\n"
"ds_read_b64_tr_b16 a[196:197], v220 offset:34336\n"
"v_fmac_f32 v32, v160, v136\n"
"v_mfma_f32_16x16x32_bf16 v[60:63], a[172:175], a[12:15], v[60:63]\n"
"ds_read_b64_tr_b16 a[198:199], v220 offset:34592\n"
"v_fmac_f32 v33, v160, v137\n"
"v_mfma_f32_16x16x32_bf16 v[60:63], a[176:179], a[28:31], v[60:63]\n"
"ds_read_b64_tr_b16 a[200:201], v220 offset:38432\n"
"ds_read_b64_tr_b16 a[202:203], v220 offset:38688\n"
"ds_read_b64_tr_b16 a[204:205], v220 offset:34344\n"
"ds_read_b64_tr_b16 a[206:207], v220 offset:34600\n"
"ds_read_b64_tr_b16 a[208:209], v220 offset:38440\n"
"ds_read_b64_tr_b16 a[210:211], v220 offset:38696\n"
"ds_read_b128 a[56:59], v213 offset:49184\n"
"v_mfma_f32_16x16x32_bf16 v[52:55], a[176:179], a[24:27], v[52:55]\n"
"v_fmac_f32 v34, v160, v138\n"
"v_fmac_f32 v35, v160, v139\n"
"v_mfma_f32_16x16x32_bf16 v[52:55], a[172:175], a[8:11], v[52:55]\n"
"v_fmac_f32 v11, v161, v131\n"
"v_fmac_f32 v12, v161, v132\n"
"v_mfma_f32_16x16x32_bf16 v[44:47], a[172:175], a[4:7], v[44:47]\n"
"v_fmac_f32 v13, v161, v133\n"
"v_fmac_f32 v14, v161, v134\n"
"v_mfma_f32_16x16x32_bf16 v[44:47], a[176:179], a[20:23], v[44:47]\n"
"v_fmac_f32 v15, v161, v135\n"
"v_fmac_f32 v40, v161, v136\n"
"v_mfma_f32_16x16x32_bf16 v[36:39], a[176:179], a[16:19], v[36:39]\n"
"v_fmac_f32 v41, v161, v137\n"
"v_fmac_f32 v42, v161, v138\n"
"v_mfma_f32_16x16x32_bf16 v[36:39], a[172:175], a[0:3], v[36:39]\n"
"s_waitcnt lgkmcnt(12)\n"
"ds_read_b64_tr_b16 a[148:149], v221 offset:0\n"
"s_and_b32 s99, s73, 7\n"
"v_mfma_f32_16x16x32_bf16 v[64:67], a[180:183], a[0:3], v[64:67]\n"
"s_add_u32 m0, s76, 16400\n"
"ds_read_b64_tr_b16 a[150:151], v221 offset:256\n"
"s_lshl_b32 s99, s99, 5\n"
"v_mfma_f32_16x16x32_bf16 v[64:67], a[184:187], a[16:19], v[64:67]\n"
"s_movk_i32 s82, 16384\n"
"buffer_load_dwordx4 v225, s[44:47], s82 offen lds\n"
"v_mfma_f32_16x16x32_bf16 v[72:75], a[184:187], a[20:23], v[72:75]\n"
"s_waitcnt vmcnt(11) lgkmcnt(2)\n"
"s_barrier\n"
"s_add_u32 s99, s99, s75\n"
"s_lshl_b32 s99, s99, 7\n"
"s_add_u32 s99, s99, 65536\n"
"buffer_store_dwordx2 v[232:233], v231, s[60:63], s99 offen sc1\n"
"v_mfma_f32_16x16x32_bf16 v[72:75], a[180:183], a[4:7], v[72:75]\n"
"s_addk_i32 m0, 8064\n"
"ds_read_b64_tr_b16 a[152:153], v221 offset:4096\n"
"v_fmac_f32 v36, v160, v140\n"
"v_mfma_f32_16x16x32_bf16 v[80:83], a[180:183], a[8:11], v[80:83]\n"
"s_waitcnt vmcnt(10)\n"
"ds_read_b128 a[44:47], v212 offset:57376\n"
"v_fmac_f32 v37, v160, v141\n"
"v_mfma_f32_16x16x32_bf16 v[80:83], a[184:187], a[24:27], v[80:83]\n"
"ds_read_b128 a[60:63], v213 offset:57376\n"
"v_fmac_f32 v38, v160, v142\n"
"v_mfma_f32_16x16x32_bf16 v[88:91], a[184:187], a[28:31], v[88:91]\n"
"ds_read_b64_tr_b16 a[154:155], v221 offset:4352\n"
"v_fmac_f32 v39, v160, v143\n"
"v_mfma_f32_16x16x32_bf16 v[88:91], a[180:183], a[12:15], v[88:91]\n"
"buffer_load_dwordx4 v225, s[44:47], s82 offen offset:128 lds\n"
"v_mfma_f32_16x16x32_bf16 v[92:95], a[188:191], a[12:15], v[92:95]\n"
"s_addk_i32 m0, 61568\n"
"ds_read_b64_tr_b16 a[156:157], v221 offset:8\n"
"v_fmac_f32 v43, v161, v139\n"
"v_mfma_f32_16x16x32_bf16 v[92:95], a[192:195], a[28:31], v[92:95]\n"
"s_movk_i32 s82, 24576\n"
"ds_read_b64_tr_b16 a[158:159], v221 offset:264\n"
"v_fmac_f32 v44, v161, v140\n"
"v_mfma_f32_16x16x32_bf16 v[84:87], a[192:195], a[24:27], v[84:87]\n"
"ds_read_b64_tr_b16 a[160:161], v221 offset:4104\n"
"v_fmac_f32 v45, v161, v141\n"
"v_mfma_f32_16x16x32_bf16 v[84:87], a[188:191], a[8:11], v[84:87]\n"
"ds_read_b64_tr_b16 a[162:163], v221 offset:4360\n"
"v_fmac_f32 v46, v161, v142\n"
"v_mfma_f32_16x16x32_bf16 v[76:79], a[188:191], a[4:7], v[76:79]\n"
"buffer_load_dwordx4 v225, s[44:47], s82 offen lds\n"
"v_mfma_f32_16x16x32_bf16 v[76:79], a[192:195], a[20:23], v[76:79]\n"
"s_addk_i32 m0, 8064\n"
"ds_read_b64_tr_b16 a[164:165], v221 offset:512\n"
"v_fmac_f32 v47, v161, v143\n"
"v_mfma_f32_16x16x32_bf16 v[68:71], a[192:195], a[16:19], v[68:71]\n"
"ds_read_b64_tr_b16 a[166:167], v221 offset:768\n"
"v_fmac_f32 v16, v162, v128\n"
"v_mfma_f32_16x16x32_bf16 v[68:71], a[188:191], a[0:3], v[68:71]\n"
"ds_read_b64_tr_b16 a[168:169], v221 offset:4608\n"
"v_fmac_f32 v17, v162, v129\n"
"v_mfma_f32_16x16x32_bf16 v[96:99], a[196:199], a[0:3], v[96:99]\n"
"ds_read_b64_tr_b16 a[170:171], v221 offset:4864\n"
"v_fmac_f32 v18, v162, v130\n"
"v_mfma_f32_16x16x32_bf16 v[96:99], a[200:203], a[16:19], v[96:99]\n"
"buffer_load_dwordx4 v225, s[44:47], s82 offen offset:128 lds\n"
"v_mfma_f32_16x16x32_bf16 v[104:107], a[200:203], a[20:23], v[104:107]\n"
"s_add_u32 m0, s77, 131200\n"
"ds_read_b64_tr_b16 a[172:173], v221 offset:520\n"
"v_fmac_f32 v19, v162, v131\n"
"v_mfma_f32_16x16x32_bf16 v[104:107], a[196:199], a[4:7], v[104:107]\n"
"s_movk_i32 s82, 16384\n"
"ds_read_b64_tr_b16 a[174:175], v221 offset:776\n"
"v_fmac_f32 v20, v162, v132\n"
"v_mfma_f32_16x16x32_bf16 v[112:115], a[196:199], a[8:11], v[112:115]\n"
"ds_read_b64_tr_b16 a[176:177], v221 offset:4616\n"
"v_fmac_f32 v21, v162, v133\n"
"v_mfma_f32_16x16x32_bf16 v[112:115], a[200:203], a[24:27], v[112:115]\n"
"ds_read_b64_tr_b16 a[178:179], v221 offset:4872\n"
"v_fmac_f32 v22, v162, v134\n"
"v_mfma_f32_16x16x32_bf16 v[120:123], a[200:203], a[28:31], v[120:123]\n"
"buffer_load_dwordx4 v226, s[48:51], s82 offen lds\n"
"v_mfma_f32_16x16x32_bf16 v[120:123], a[196:199], a[12:15], v[120:123]\n"
"s_addk_i32 m0, 896\n"
"v_fmac_f32 v23, v162, v135\n"
"v_fmac_f32 v48, v162, v136\n"
"v_mfma_f32_16x16x32_bf16 v[124:127], a[204:207], a[12:15], v[124:127]\n"
"v_fmac_f32 v49, v162, v137\n"
"v_fmac_f32 v50, v162, v138\n"
"v_mfma_f32_16x16x32_bf16 v[124:127], a[208:211], a[28:31], v[124:127]\n"
"v_fmac_f32 v51, v162, v139\n"
"v_fmac_f32 v52, v162, v140\n"
"v_mfma_f32_16x16x32_bf16 v[116:119], a[208:211], a[24:27], v[116:119]\n"
"v_fmac_f32 v53, v162, v141\n"
"v_fmac_f32 v54, v162, v142\n"
"v_mfma_f32_16x16x32_bf16 v[116:119], a[204:207], a[8:11], v[116:119]\n"
"buffer_load_dwordx4 v226, s[48:51], s82 offen offset:128 lds\n"
"v_mfma_f32_16x16x32_bf16 v[108:111], a[204:207], a[4:7], v[108:111]\n"
"s_addk_i32 m0, 7304\n"
"v_fmac_f32 v55, v162, v143\n"
"v_fmac_f32 v24, v163, v128\n"
"v_mfma_f32_16x16x32_bf16 v[108:111], a[208:211], a[20:23], v[108:111]\n"
"s_movk_i32 s82, 18432\n"
"v_fmac_f32 v25, v163, v129\n"
"v_fmac_f32 v26, v163, v130\n"
"v_mfma_f32_16x16x32_bf16 v[100:103], a[208:211], a[16:19], v[100:103]\n"
"v_fmac_f32 v27, v163, v131\n"
"v_fmac_f32 v28, v163, v132\n"
"v_mfma_f32_16x16x32_bf16 v[100:103], a[204:207], a[0:3], v[100:103]\n"
"s_waitcnt lgkmcnt(2)\n"
"ds_read_b64_tr_b16 a[180:181], v221 offset:1024\n"
"v_fmac_f32 v29, v163, v133\n"
"v_mfma_f32_16x16x32_bf16 v[0:3], a[148:151], a[32:35], v[0:3]\n"
"buffer_load_dwordx4 v226, s[48:51], s82 offen lds\n"
"v_mfma_f32_16x16x32_bf16 v[0:3], a[152:155], a[48:51], v[0:3]\n"
"s_addk_i32 m0, 896\n"
"ds_read_b64_tr_b16 a[182:183], v221 offset:1280\n"
"v_fmac_f32 v30, v163, v134\n"
"v_mfma_f32_16x16x32_bf16 v[4:7], a[156:159], a[32:35], v[4:7]\n"
"ds_read_b64_tr_b16 a[184:185], v221 offset:5120\n"
"v_fmac_f32 v31, v163, v135\n"
"v_mfma_f32_16x16x32_bf16 v[4:7], a[160:163], a[48:51], v[4:7]\n"
"ds_read_b64_tr_b16 a[186:187], v221 offset:5376\n"
"v_fmac_f32 v56, v163, v136\n"
"v_mfma_f32_16x16x32_bf16 v[32:35], a[164:167], a[32:35], v[32:35]\n"
"ds_read_b64_tr_b16 a[188:189], v221 offset:1032\n"
"v_fmac_f32 v57, v163, v137\n"
"v_mfma_f32_16x16x32_bf16 v[32:35], a[168:171], a[48:51], v[32:35]\n"
"buffer_load_dwordx4 v226, s[48:51], s82 offen offset:128 lds\n"
"v_mfma_f32_16x16x32_bf16 v[36:39], a[172:175], a[32:35], v[36:39]\n"
"s_waitcnt lgkmcnt(5)\n"
"ds_read_b64_tr_b16 a[190:191], v221 offset:1288\n"
"v_fmac_f32 v58, v163, v138\n"
"v_mfma_f32_16x16x32_bf16 v[36:39], a[176:179], a[48:51], v[36:39]\n"
"ds_read_b64_tr_b16 a[192:193], v221 offset:5128\n"
"v_fmac_f32 v59, v163, v139\n"
"v_mfma_f32_16x16x32_bf16 v[8:11], a[148:151], a[36:39], v[8:11]\n"
"ds_read_b64_tr_b16 a[194:195], v221 offset:5384\n"
"v_fmac_f32 v60, v163, v140\n"
"v_mfma_f32_16x16x32_bf16 v[8:11], a[152:155], a[52:55], v[8:11]\n"
"ds_read_b64_tr_b16 a[196:197], v221 offset:1536\n"
"v_fmac_f32 v61, v163, v141\n"
"v_mfma_f32_16x16x32_bf16 v[12:15], a[156:159], a[36:39], v[12:15]\n"
"ds_read_b64_tr_b16 a[198:199], v221 offset:1792\n"
"v_fmac_f32 v62, v163, v142\n"
"v_mfma_f32_16x16x32_bf16 v[12:15], a[160:163], a[52:55], v[12:15]\n"
"ds_read_b64_tr_b16 a[200:201], v221 offset:5632\n"
"v_fmac_f32 v63, v163, v143\n"
"v_mfma_f32_16x16x32_bf16 v[40:43], a[164:167], a[36:39], v[40:43]\n"
"ds_read_b64_tr_b16 a[202:203], v221 offset:5888\n"
"v_fmac_f32 v64, v160, v144\n"
"v_mfma_f32_16x16x32_bf16 v[40:43], a[168:171], a[52:55], v[40:43]\n"
"ds_read_b64_tr_b16 a[204:205], v221 offset:1544\n"
"v_fmac_f32 v65, v160, v145\n"
"v_mfma_f32_16x16x32_bf16 v[44:47], a[172:175], a[36:39], v[44:47]\n"
"ds_read_b64_tr_b16 a[206:207], v221 offset:1800\n"
"v_fmac_f32 v66, v160, v146\n"
"v_mfma_f32_16x16x32_bf16 v[44:47], a[176:179], a[52:55], v[44:47]\n"
"ds_read_b64_tr_b16 a[208:209], v221 offset:5640\n"
"v_fmac_f32 v67, v160, v147\n"
"v_mfma_f32_16x16x32_bf16 v[16:19], a[148:151], a[40:43], v[16:19]\n"
"ds_read_b64_tr_b16 a[210:211], v221 offset:5896\n"
"v_fmac_f32 v68, v160, v148\n"
"v_mfma_f32_16x16x32_bf16 v[16:19], a[152:155], a[56:59], v[16:19]\n"
"v_fmac_f32 v69, v160, v149\n"
"v_fmac_f32 v70, v160, v150\n"
"v_mfma_f32_16x16x32_bf16 v[20:23], a[156:159], a[40:43], v[20:23]\n"
"v_fmac_f32 v71, v160, v151\n"
"v_fmac_f32 v96, v160, v152\n"
"v_mfma_f32_16x16x32_bf16 v[20:23], a[160:163], a[56:59], v[20:23]\n"
"v_fmac_f32 v97, v160, v153\n"
"v_fmac_f32 v98, v160, v154\n"
"v_mfma_f32_16x16x32_bf16 v[48:51], a[164:167], a[40:43], v[48:51]\n"
"v_fmac_f32 v99, v160, v155\n"
"v_fmac_f32 v100, v160, v156\n"
"v_mfma_f32_16x16x32_bf16 v[48:51], a[168:171], a[56:59], v[48:51]\n"
"v_fmac_f32 v101, v160, v157\n"
"v_fmac_f32 v102, v160, v158\n"
"v_mfma_f32_16x16x32_bf16 v[52:55], a[172:175], a[40:43], v[52:55]\n"
"v_fmac_f32 v103, v160, v159\n"
"v_fmac_f32 v72, v161, v144\n"
"v_mfma_f32_16x16x32_bf16 v[52:55], a[176:179], a[56:59], v[52:55]\n"
"v_fmac_f32 v73, v161, v145\n"
"v_fmac_f32 v74, v161, v146\n"
"v_mfma_f32_16x16x32_bf16 v[24:27], a[148:151], a[44:47], v[24:27]\n"
"v_fmac_f32 v75, v161, v147\n"
"v_fmac_f32 v76, v161, v148\n"
"v_mfma_f32_16x16x32_bf16 v[24:27], a[152:155], a[60:63], v[24:27]\n"
"v_fmac_f32 v77, v161, v149\n"
"v_fmac_f32 v78, v161, v150\n"
"v_mfma_f32_16x16x32_bf16 v[28:31], a[156:159], a[44:47], v[28:31]\n"
"v_fmac_f32 v79, v161, v151\n"
"v_fmac_f32 v104, v161, v152\n"
"v_mfma_f32_16x16x32_bf16 v[28:31], a[160:163], a[60:63], v[28:31]\n"
"v_fmac_f32 v105, v161, v153\n"
"v_fmac_f32 v106, v161, v154\n"
"v_mfma_f32_16x16x32_bf16 v[56:59], a[164:167], a[44:47], v[56:59]\n"
"v_fmac_f32 v107, v161, v155\n"
"v_fmac_f32 v108, v161, v156\n"
"v_mfma_f32_16x16x32_bf16 v[56:59], a[168:171], a[60:63], v[56:59]\n"
"v_fmac_f32 v109, v161, v157\n"
"v_fmac_f32 v110, v161, v158\n"
"v_mfma_f32_16x16x32_bf16 v[60:63], a[172:175], a[44:47], v[60:63]\n"
"v_fmac_f32 v111, v161, v159\n"
"v_fmac_f32 v80, v162, v144\n"
"v_mfma_f32_16x16x32_bf16 v[60:63], a[176:179], a[60:63], v[60:63]\n"
"s_waitcnt lgkmcnt(0)\n"
"v_fmac_f32 v81, v162, v145\n"
"v_fmac_f32 v82, v162, v146\n"
"v_mfma_f32_16x16x32_bf16 v[64:67], a[180:183], a[32:35], v[64:67]\n"
"s_mov_b32 s7, 0x20000\n"
"v_fmac_f32 v83, v162, v147\n"
"v_fmac_f32 v84, v162, v148\n"
"v_mfma_f32_16x16x32_bf16 v[64:67], a[184:187], a[48:51], v[64:67]\n"
"s_waitcnt vmcnt(13)\n"
"v_fmac_f32 v85, v162, v149\n"
"v_fmac_f32 v86, v162, v150\n"
"v_mfma_f32_16x16x32_bf16 v[68:71], a[188:191], a[32:35], v[68:71]\n"
"s_barrier\n"
"ds_read_b128 a[148:151], v222 offset:0\n"
"v_fmac_f32 v87, v162, v151\n"
"v_mfma_f32_16x16x32_bf16 v[68:71], a[192:195], a[48:51], v[68:71]\n"
"s_add_u32 m0, s76, 32800\n"
"ds_read_b128 a[152:155], v223 offset:0\n"
"s_lshl_b32 s6, s79, 7\n"
"v_mfma_f32_16x16x32_bf16 v[96:99], a[196:199], a[32:35], v[96:99]\n"
"s_sub_u32 s4, s36, s6\n"
"s_subb_u32 s5, s37, 0\n"
"ds_read_b128 a[156:159], v222 offset:8192\n"
"s_mov_b32 s6, 2211840\n"
"v_mfma_f32_16x16x32_bf16 v[96:99], a[200:203], a[48:51], v[96:99]\n"
"s_addk_i32 s82, 14336\n"
"buffer_load_dwordx4 v225, s[44:47], s82 offen lds\n"
"v_mfma_f32_16x16x32_bf16 v[100:103], a[204:207], a[32:35], v[100:103]\n"
"s_addk_i32 m0, 8064\n"
"ds_read_b128 a[160:163], v223 offset:8192\n"
"s_lshl_b32 s94, s73, 2\n"
"v_mfma_f32_16x16x32_bf16 v[100:103], a[208:211], a[48:51], v[100:103]\n"
"s_add_u32 s94, s94, s72\n"
"ds_read_b128 a[164:167], v222 offset:512\n"
"s_mul_i32 s94, s94, 2176\n"
"v_mfma_f32_16x16x32_bf16 v[72:75], a[180:183], a[36:39], v[72:75]\n"
"s_add_u32 s94, s94, 0\n"
"ds_read_b128 a[168:171], v223 offset:512\n"
"v_lshlrev_b32 v209, 7, v253\n"
"v_mfma_f32_16x16x32_bf16 v[72:75], a[184:187], a[52:55], v[72:75]\n"
"ds_read_b128 a[172:175], v222 offset:8704\n"
"v_cmp_gt_u32 vcc, 17, v253\n"
"v_mfma_f32_16x16x32_bf16 v[76:79], a[188:191], a[36:39], v[76:79]\n"
"buffer_load_dwordx4 v225, s[44:47], s82 offen offset:128 lds\n"
"v_mfma_f32_16x16x32_bf16 v[76:79], a[192:195], a[52:55], v[76:79]\n"
"s_addk_i32 m0, 61568\n"
"ds_read_b128 a[176:179], v223 offset:8704\n"
"v_mov_b32 v210, 0x40000000\n"
"v_mfma_f32_16x16x32_bf16 v[104:107], a[196:199], a[36:39], v[104:107]\n"
"s_addk_i32 s82, 8192\n"
"v_cndmask_b32 v209, v210, v209, vcc\n"
"v_lshlrev_b32 v207, 7, v253\n"
"v_mfma_f32_16x16x32_bf16 v[104:107], a[200:203], a[52:55], v[104:107]\n"
"v_cmp_gt_u32 vcc, 17, v253\n"
"v_mov_b32 v208, 0x40000000\n"
"v_mfma_f32_16x16x32_bf16 v[108:111], a[204:207], a[36:39], v[108:111]\n"
"v_cndmask_b32 v207, v208, v207, vcc\n"
"v_fmac_f32 v112, v162, v152\n"
"v_mfma_f32_16x16x32_bf16 v[108:111], a[208:211], a[52:55], v[108:111]\n"
"buffer_load_dword v210, v209, s[4:7], s94 offen\n"
"s_lshl_b32 s6, s79, 7\n"
"s_sub_u32 s4, s40, s6\n"
"s_subb_u32 s5, s41, 0\n"
"s_mov_b32 s6, 2211840\n"
"s_mov_b32 s7, 0x20000\n"
"s_lshl_b32 s94, s73, 2\n"
"s_add_u32 s94, s94, s72\n"
"s_mul_i32 s94, s94, 2176\n"
"s_add_u32 s94, s94, 0\n"
"buffer_load_dword v208, v207, s[4:7], s94 offen\n"
"v_mfma_f32_16x16x32_bf16 v[80:83], a[180:183], a[40:43], v[80:83]\n"
"v_fmac_f32 v113, v162, v153\n"
"v_fmac_f32 v114, v162, v154\n"
"v_mfma_f32_16x16x32_bf16 v[80:83], a[184:187], a[56:59], v[80:83]\n"
"v_fmac_f32 v115, v162, v155\n"
"v_fmac_f32 v116, v162, v156\n"
"v_mfma_f32_16x16x32_bf16 v[84:87], a[188:191], a[40:43], v[84:87]\n"
"v_fmac_f32 v117, v162, v157\n"
"v_fmac_f32 v118, v162, v158\n"
"v_mfma_f32_16x16x32_bf16 v[84:87], a[192:195], a[56:59], v[84:87]\n"
"v_fmac_f32 v119, v162, v159\n"
"v_fmac_f32 v88, v163, v144\n"
"v_mfma_f32_16x16x32_bf16 v[112:115], a[196:199], a[40:43], v[112:115]\n"
"buffer_load_dwordx4 v225, s[44:47], s82 offen lds\n"
"v_mfma_f32_16x16x32_bf16 v[112:115], a[200:203], a[56:59], v[112:115]\n"
"s_addk_i32 m0, 8064\n"
"v_fmac_f32 v89, v163, v145\n"
"v_fmac_f32 v90, v163, v146\n"
"v_fmac_f32 v91, v163, v147\n"
"v_mfma_f32_16x16x32_bf16 v[116:119], a[204:207], a[40:43], v[116:119]\n"
"L_st4_%=:\n"
"s_cbranch_execz L_st8_%=\n"
"v_fmac_f32 v92, v163, v148\n"
"v_fmac_f32 v93, v163, v149\n"
"v_mfma_f32_16x16x32_bf16 v[116:119], a[208:211], a[56:59], v[116:119]\n"
"v_fmac_f32 v94, v163, v150\n"
"v_fmac_f32 v95, v163, v151\n"
"v_mfma_f32_16x16x32_bf16 v[88:91], a[180:183], a[44:47], v[88:91]\n"
"v_fmac_f32 v120, v163, v152\n"
"v_fmac_f32 v121, v163, v153\n"
"v_mfma_f32_16x16x32_bf16 v[88:91], a[184:187], a[60:63], v[88:91]\n"
"buffer_load_dwordx4 v225, s[44:47], s82 offen offset:128 lds\n"
"v_fmac_f32 v122, v163, v154\n"
"v_fmac_f32 v123, v163, v155\n"
"v_mfma_f32_16x16x32_bf16 v[92:95], a[188:191], a[44:47], v[92:95]\n"
"v_fmac_f32 v124, v163, v156\n"
"v_fmac_f32 v125, v163, v157\n"
"v_mfma_f32_16x16x32_bf16 v[92:95], a[192:195], a[60:63], v[92:95]\n"
"v_fmac_f32 v126, v163, v158\n"
"v_fmac_f32 v127, v163, v159\n"
"v_mfma_f32_16x16x32_bf16 v[120:123], a[196:199], a[44:47], v[120:123]\n"
"v_mul_f32 v0, v234, v0\n"
"v_mul_f32 v1, v234, v1\n"
"v_mfma_f32_16x16x32_bf16 v[120:123], a[200:203], a[60:63], v[120:123]\n"
"v_mul_f32 v2, v234, v2\n"
"v_mul_f32 v3, v234, v3\n"
"v_mul_f32 v4, v234, v4\n"
"v_mul_f32 v5, v234, v5\n"
"v_mul_f32 v6, v234, v6\n"
"v_mul_f32 v7, v234, v7\n"
"v_cvt_pk_bf16_f32 v0, v0, v1\n"
"v_cvt_pk_bf16_f32 v1, v2, v3\n"
"v_cvt_pk_bf16_f32 v2, v4, v5\n"
"v_cvt_pk_bf16_f32 v3, v6, v7\n"
"v_mfma_f32_16x16x32_bf16 v[124:127], a[204:207], a[44:47], v[124:127]\n"
"v_mul_f32 v32, v234, v32\n"
"v_mul_f32 v33, v234, v33\n"
"v_mul_f32 v34, v234, v34\n"
"v_mul_f32 v35, v234, v35\n"
"v_mul_f32 v36, v234, v36\n"
"v_mul_f32 v37, v234, v37\n"
"v_mul_f32 v38, v234, v38\n"
"v_mul_f32 v39, v234, v39\n"
"v_cvt_pk_bf16_f32 v32, v32, v33\n"
"v_cvt_pk_bf16_f32 v33, v34, v35\n"
"v_cvt_pk_bf16_f32 v34, v36, v37\n"
"v_cvt_pk_bf16_f32 v35, v38, v39\n"
"v_mfma_f32_16x16x32_bf16 v[124:127], a[208:211], a[60:63], v[124:127]\n"
"s_waitcnt lgkmcnt(0)\n"
"ds_read_b128 a[212:215], v222 offset:4096\n"
"v_mul_f32 v64, v234, v64\n"
"v_mul_f32 v65, v234, v65\n"
"v_mul_f32 v66, v234, v66\n"
"v_mul_f32 v67, v234, v67\n"
"v_mul_f32 v68, v234, v68\n"
"v_mul_f32 v69, v234, v69\n"
"v_mul_f32 v70, v234, v70\n"
"v_mul_f32 v71, v234, v71\n"
"v_cvt_pk_bf16_f32 v64, v64, v65\n"
"v_cvt_pk_bf16_f32 v65, v66, v67\n"
"v_cvt_pk_bf16_f32 v66, v68, v69\n"
"v_cvt_pk_bf16_f32 v67, v70, v71\n"
"v_mfma_f32_16x16x32_bf16 v[4:7], a[148:151], v[0:3], 0\n"
"ds_read_b128 a[216:219], v223 offset:4096\n"
"v_mul_f32 v96, v234, v96\n"
"v_mul_f32 v97, v234, v97\n"
"v_mul_f32 v98, v234, v98\n"
"v_mul_f32 v99, v234, v99\n"
"v_mul_f32 v100, v234, v100\n"
"v_mul_f32 v101, v234, v101\n"
"v_mul_f32 v102, v234, v102\n"
"v_mul_f32 v103, v234, v103\n"
"v_cvt_pk_bf16_f32 v96, v96, v97\n"
"v_cvt_pk_bf16_f32 v97, v98, v99\n"
"v_cvt_pk_bf16_f32 v98, v100, v101\n"
"v_cvt_pk_bf16_f32 v99, v102, v103\n"
"v_mfma_f32_16x16x32_bf16 v[4:7], a[152:155], v[32:35], v[4:7]\n"
"ds_read_b128 a[220:223], v222 offset:12288\n"
"v_mul_f32 v8, v235, v8\n"
"v_mfma_f32_16x16x32_bf16 v[4:7], a[156:159], v[64:67], v[4:7]\n"
"ds_read_b128 a[224:227], v223 offset:12288\n"
"v_mul_f32 v9, v235, v9\n"
"v_mfma_f32_16x16x32_bf16 v[4:7], a[160:163], v[96:99], v[4:7]\n"
"ds_read_b128 a[228:231], v222 offset:4608\n"
"v_mul_f32 v10, v235, v10\n"
"v_mfma_f32_16x16x32_bf16 v[36:39], a[164:167], v[0:3], 0\n"
"ds_read_b128 a[232:235], v223 offset:4608\n"
"v_mul_f32 v11, v235, v11\n"
"v_mfma_f32_16x16x32_bf16 v[36:39], a[168:171], v[32:35], v[36:39]\n"
"ds_read_b128 a[236:239], v222 offset:12800\n"
"v_mul_f32 v12, v235, v12\n"
"v_mul_f32 v13, v235, v13\n"
"v_mul_f32 v14, v235, v14\n"
"v_mul_f32 v15, v235, v15\n"
"v_cvt_pk_bf16_f32 v8, v8, v9\n"
"v_cvt_pk_bf16_f32 v9, v10, v11\n"
"v_cvt_pk_bf16_f32 v10, v12, v13\n"
"v_cvt_pk_bf16_f32 v11, v14, v15\n"
"v_mfma_f32_16x16x32_bf16 v[36:39], a[172:175], v[64:67], v[36:39]\n"
"ds_read_b128 a[240:243], v223 offset:12800\n"
"v_mul_f32 v40, v235, v40\n"
"v_mul_f32 v41, v235, v41\n"
"v_mul_f32 v42, v235, v42\n"
"v_mul_f32 v43, v235, v43\n"
"v_mul_f32 v44, v235, v44\n"
"v_mul_f32 v45, v235, v45\n"
"v_mul_f32 v46, v235, v46\n"
"v_mul_f32 v47, v235, v47\n"
"v_cvt_pk_bf16_f32 v40, v40, v41\n"
"v_cvt_pk_bf16_f32 v41, v42, v43\n"
"v_cvt_pk_bf16_f32 v42, v44, v45\n"
"v_cvt_pk_bf16_f32 v43, v46, v47\n"
"v_mfma_f32_16x16x32_bf16 v[36:39], a[176:179], v[96:99], v[36:39]\n"
"v_mul_f32 v72, v235, v72\n"
"v_mul_f32 v73, v235, v73\n"
"v_mul_f32 v74, v235, v74\n"
"v_mul_f32 v75, v235, v75\n"
"v_mul_f32 v76, v235, v76\n"
"v_mul_f32 v77, v235, v77\n"
"v_mul_f32 v78, v235, v78\n"
"v_mul_f32 v79, v235, v79\n"
"v_cvt_pk_bf16_f32 v72, v72, v73\n"
"v_cvt_pk_bf16_f32 v73, v74, v75\n"
"v_cvt_pk_bf16_f32 v74, v76, v77\n"
"v_cvt_pk_bf16_f32 v75, v78, v79\n"
"v_mfma_f32_16x16x32_bf16 v[12:15], a[148:151], v[8:11], 0\n"
"v_mul_f32 v104, v235, v104\n"
"v_mul_f32 v105, v235, v105\n"
"v_mul_f32 v106, v235, v106\n"
"v_mul_f32 v107, v235, v107\n"
"v_mul_f32 v108, v235, v108\n"
"v_mul_f32 v109, v235, v109\n"
"v_mul_f32 v110, v235, v110\n"
"v_mul_f32 v111, v235, v111\n"
"v_cvt_pk_bf16_f32 v104, v104, v105\n"
"v_cvt_pk_bf16_f32 v105, v106, v107\n"
"v_cvt_pk_bf16_f32 v106, v108, v109\n"
"v_cvt_pk_bf16_f32 v107, v110, v111\n"
"v_mfma_f32_16x16x32_bf16 v[12:15], a[152:155], v[40:43], v[12:15]\n"
"v_mul_f32 v16, v236, v16\n"
"v_mul_f32 v17, v236, v17\n"
"v_mfma_f32_16x16x32_bf16 v[12:15], a[156:159], v[72:75], v[12:15]\n"
"v_mul_f32 v18, v236, v18\n"
"v_mul_f32 v19, v236, v19\n"
"v_mfma_f32_16x16x32_bf16 v[12:15], a[160:163], v[104:107], v[12:15]\n"
"v_mul_f32 v20, v236, v20\n"
"v_mul_f32 v21, v236, v21\n"
"v_mfma_f32_16x16x32_bf16 v[44:47], a[164:167], v[8:11], 0\n"
"v_mul_f32 v22, v236, v22\n"
"v_mul_f32 v23, v236, v23\n"
"v_mfma_f32_16x16x32_bf16 v[44:47], a[168:171], v[40:43], v[44:47]\n"
"v_cvt_pk_bf16_f32 v16, v16, v17\n"
"v_cvt_pk_bf16_f32 v17, v18, v19\n"
"v_cvt_pk_bf16_f32 v18, v20, v21\n"
"v_cvt_pk_bf16_f32 v19, v22, v23\n"
"v_mfma_f32_16x16x32_bf16 v[44:47], a[172:175], v[72:75], v[44:47]\n"
"v_mul_f32 v48, v236, v48\n"
"v_mul_f32 v49, v236, v49\n"
"v_mul_f32 v50, v236, v50\n"
"v_mul_f32 v51, v236, v51\n"
"v_mul_f32 v52, v236, v52\n"
"v_mul_f32 v53, v236, v53\n"
"v_mul_f32 v54, v236, v54\n"
"v_mul_f32 v55, v236, v55\n"
"v_cvt_pk_bf16_f32 v48, v48, v49\n"
"v_cvt_pk_bf16_f32 v49, v50, v51\n"
"v_cvt_pk_bf16_f32 v50, v52, v53\n"
"v_cvt_pk_bf16_f32 v51, v54, v55\n"
"v_mfma_f32_16x16x32_bf16 v[44:47], a[176:179], v[104:107], v[44:47]\n"
"v_mul_f32 v80, v236, v80\n"
"v_mul_f32 v81, v236, v81\n"
"v_mul_f32 v82, v236, v82\n"
"v_mul_f32 v83, v236, v83\n"
"v_mul_f32 v84, v236, v84\n"
"v_mul_f32 v85, v236, v85\n"
"v_mul_f32 v86, v236, v86\n"
"v_mul_f32 v87, v236, v87\n"
"v_cvt_pk_bf16_f32 v80, v80, v81\n"
"v_cvt_pk_bf16_f32 v81, v82, v83\n"
"v_cvt_pk_bf16_f32 v82, v84, v85\n"
"v_cvt_pk_bf16_f32 v83, v86, v87\n"
"v_mfma_f32_16x16x32_bf16 v[20:23], a[148:151], v[16:19], 0\n"
"v_mul_f32 v112, v236, v112\n"
"v_mul_f32 v113, v236, v113\n"
"v_mul_f32 v114, v236, v114\n"
"v_mul_f32 v115, v236, v115\n"
"v_mul_f32 v116, v236, v116\n"
"v_mul_f32 v117, v236, v117\n"
"v_mul_f32 v118, v236, v118\n"
"v_mul_f32 v119, v236, v119\n"
"v_cvt_pk_bf16_f32 v112, v112, v113\n"
"v_cvt_pk_bf16_f32 v113, v114, v115\n"
"v_cvt_pk_bf16_f32 v114, v116, v117\n"
"v_cvt_pk_bf16_f32 v115, v118, v119\n"
"v_mfma_f32_16x16x32_bf16 v[20:23], a[152:155], v[48:51], v[20:23]\n"
"v_mul_f32 v24, v237, v24\n"
"v_mul_f32 v25, v237, v25\n"
"v_mfma_f32_16x16x32_bf16 v[20:23], a[156:159], v[80:83], v[20:23]\n"
"v_mul_f32 v26, v237, v26\n"
"v_mul_f32 v27, v237, v27\n"
"v_mfma_f32_16x16x32_bf16 v[20:23], a[160:163], v[112:115], v[20:23]\n"
"v_mul_f32 v28, v237, v28\n"
"v_mul_f32 v29, v237, v29\n"
"v_mfma_f32_16x16x32_bf16 v[52:55], a[164:167], v[16:19], 0\n"
"v_mul_f32 v30, v237, v30\n"
"v_mul_f32 v31, v237, v31\n"
"v_mfma_f32_16x16x32_bf16 v[52:55], a[168:171], v[48:51], v[52:55]\n"
"v_cvt_pk_bf16_f32 v24, v24, v25\n"
"v_cvt_pk_bf16_f32 v25, v26, v27\n"
"v_cvt_pk_bf16_f32 v26, v28, v29\n"
"v_cvt_pk_bf16_f32 v27, v30, v31\n"
"v_mfma_f32_16x16x32_bf16 v[52:55], a[172:175], v[80:83], v[52:55]\n"
"v_mul_f32 v56, v237, v56\n"
"v_mul_f32 v57, v237, v57\n"
"v_mul_f32 v58, v237, v58\n"
"v_mul_f32 v59, v237, v59\n"
"v_mul_f32 v60, v237, v60\n"
"v_mul_f32 v61, v237, v61\n"
"v_mul_f32 v62, v237, v62\n"
"v_mul_f32 v63, v237, v63\n"
"v_cvt_pk_bf16_f32 v56, v56, v57\n"
"v_cvt_pk_bf16_f32 v57, v58, v59\n"
"v_cvt_pk_bf16_f32 v58, v60, v61\n"
"v_cvt_pk_bf16_f32 v59, v62, v63\n"
"v_mfma_f32_16x16x32_bf16 v[52:55], a[176:179], v[112:115], v[52:55]\n"
"v_mul_f32 v88, v237, v88\n"
"v_mul_f32 v89, v237, v89\n"
"v_mul_f32 v90, v237, v90\n"
"v_mul_f32 v91, v237, v91\n"
"v_mul_f32 v92, v237, v92\n"
"v_mul_f32 v93, v237, v93\n"
"v_mul_f32 v94, v237, v94\n"
"v_mul_f32 v95, v237, v95\n"
"v_cvt_pk_bf16_f32 v88, v88, v89\n"
"v_cvt_pk_bf16_f32 v89, v90, v91\n"
"v_cvt_pk_bf16_f32 v90, v92, v93\n"
"v_cvt_pk_bf16_f32 v91, v94, v95\n"
"v_mfma_f32_16x16x32_bf16 v[28:31], a[148:151], v[24:27], 0\n"
"v_mul_f32 v120, v237, v120\n"
"v_mul_f32 v121, v237, v121\n"
"v_mul_f32 v122, v237, v122\n"
"v_mul_f32 v123, v237, v123\n"
"v_mul_f32 v124, v237, v124\n"
"v_mul_f32 v125, v237, v125\n"
"v_mul_f32 v126, v237, v126\n"
"v_mul_f32 v127, v237, v127\n"
"v_cvt_pk_bf16_f32 v120, v120, v121\n"
"v_cvt_pk_bf16_f32 v121, v122, v123\n"
"v_cvt_pk_bf16_f32 v122, v124, v125\n"
"v_cvt_pk_bf16_f32 v123, v126, v127\n"
"v_mfma_f32_16x16x32_bf16 v[28:31], a[152:155], v[56:59], v[28:31]\n"
"v_max3_f32 v248, v4, v5, v6\n"
"v_max3_f32 v249, v7, v36, v37\n"
"v_mfma_f32_16x16x32_bf16 v[28:31], a[156:159], v[88:91], v[28:31]\n"
"v_max3_f32 v248, v248, v38, v39\n"
"v_max_f32 v248, v248, v249\n"
"v_mfma_f32_16x16x32_bf16 v[28:31], a[160:163], v[120:123], v[28:31]\n"
"v_mov_b32 v249, v248\n"
"s_nop 1\n"
"v_permlane16_swap_b32 v248, v249\n"
"v_mfma_f32_16x16x32_bf16 v[60:63], a[164:167], v[24:27], 0\n"
"v_max_f32 v248, v248, v249\n"
"v_mov_b32 v249, v248\n"
"v_mfma_f32_16x16x32_bf16 v[60:63], a[168:171], v[56:59], v[60:63]\n"
"s_nop 0\n"
"v_permlane32_swap_b32 v248, v249\n"
"v_max_f32 v248, v248, v249\n"
"v_sub_f32 v160, 0, v248\n"
"v_sub_f32 v161, 0, v248\n"
"v_sub_f32 v162, 0, v248\n"
"v_sub_f32 v163, 0, v248\n"
"v_mfma_f32_16x16x32_bf16 v[60:63], a[172:175], v[88:91], v[60:63]\n"
"v_max3_f32 v248, v12, v13, v14\n"
"v_max3_f32 v249, v15, v44, v45\n"
"v_mfma_f32_16x16x32_bf16 v[60:63], a[176:179], v[120:123], v[60:63]\n"
"s_waitcnt vmcnt(10) lgkmcnt(0)\n"
"v_max3_f32 v248, v248, v46, v47\n"
"v_max_f32 v248, v248, v249\n"
"v_mfma_f32_16x16x32_bf16 v[68:71], a[212:215], v[0:3], v[160:163]\n"
"s_barrier\n"
"ds_read_b64_tr_b16 a[180:181], v221 offset:16400\n"
"v_mov_b32 v249, v248\n"
"v_mfma_f32_16x16x32_bf16 v[68:71], a[216:219], v[32:35], v[68:71]\n"
"ds_read_b64_tr_b16 a[182:183], v221 offset:16656\n"
"v_permlane16_swap_b32 v248, v249\n"
"v_mfma_f32_16x16x32_bf16 v[68:71], a[220:223], v[64:67], v[68:71]\n"
"ds_read_b64_tr_b16 a[184:185], v221 offset:16408\n"
"v_max_f32 v248, v248, v249\n"
"v_mfma_f32_16x16x32_bf16 v[68:71], a[224:227], v[96:99], v[68:71]\n"
"ds_read_b64_tr_b16 a[186:187], v221 offset:16664\n"
"v_mov_b32 v249, v248\n"
"v_mfma_f32_16x16x32_bf16 v[100:103], a[228:231], v[0:3], v[160:163]\n"
"ds_read_b64_tr_b16 a[188:189], v221 offset:16912\n"
"v_permlane32_swap_b32 v248, v249\n"
"v_mfma_f32_16x16x32_bf16 v[100:103], a[232:235], v[32:35], v[100:103]\n"
"ds_read_b64_tr_b16 a[190:191], v221 offset:17168\n"
"v_max_f32 v248, v248, v249\n"
"v_sub_f32 v164, 0, v248\n"
"v_sub_f32 v165, 0, v248\n"
"v_sub_f32 v166, 0, v248\n"
"v_sub_f32 v167, 0, v248\n"
"v_mfma_f32_16x16x32_bf16 v[100:103], a[236:239], v[64:67], v[100:103]\n"
"ds_read_b64_tr_b16 a[192:193], v221 offset:16920\n"
"v_max3_f32 v248, v20, v21, v22\n"
"v_mfma_f32_16x16x32_bf16 v[100:103], a[240:243], v[96:99], v[100:103]\n"
"ds_read_b64_tr_b16 a[194:195], v221 offset:17176\n"
"v_max3_f32 v249, v23, v52, v53\n"
"v_mfma_f32_16x16x32_bf16 v[76:79], a[212:215], v[8:11], v[164:167]\n"
"ds_read_b64_tr_b16 a[196:197], v221 offset:17424\n"
"v_max3_f32 v248, v248, v54, v55\n"
"v_mfma_f32_16x16x32_bf16 v[76:79], a[216:219], v[40:43], v[76:79]\n"
"ds_read_b64_tr_b16 a[198:199], v221 offset:17680\n"
"v_max_f32 v248, v248, v249\n"
"v_mfma_f32_16x16x32_bf16 v[76:79], a[220:223], v[72:75], v[76:79]\n"
"ds_read_b64_tr_b16 a[200:201], v221 offset:17432\n"
"v_mov_b32 v249, v248\n"
"v_mfma_f32_16x16x32_bf16 v[76:79], a[224:227], v[104:107], v[76:79]\n"
"ds_read_b64_tr_b16 a[202:203], v221 offset:17688\n"
"v_permlane16_swap_b32 v248, v249\n"
"v_mfma_f32_16x16x32_bf16 v[108:111], a[228:231], v[8:11], v[164:167]\n"
"ds_read_b64_tr_b16 a[204:205], v221 offset:17936\n"
"v_max_f32 v248, v248, v249\n"
"v_mfma_f32_16x16x32_bf16 v[108:111], a[232:235], v[40:43], v[108:111]\n"
"ds_read_b64_tr_b16 a[206:207], v221 offset:18192\n"
"v_mov_b32 v249, v248\n"
"s_nop 1\n"
"v_permlane32_swap_b32 v248, v249\n"
"v_max_f32 v248, v248, v249\n"
"v_sub_f32 v168, 0, v248\n"
"v_sub_f32 v169, 0, v248\n"
"v_sub_f32 v170, 0, v248\n"
"v_sub_f32 v171, 0, v248\n"
"v_mfma_f32_16x16x32_bf16 v[108:111], a[236:239], v[72:75], v[108:111]\n"
"ds_read_b64_tr_b16 a[208:209], v221 offset:17944\n"
"v_max3_f32 v248, v28, v29, v30\n"
"v_mfma_f32_16x16x32_bf16 v[108:111], a[240:243], v[104:107], v[108:111]\n"
"ds_read_b64_tr_b16 a[210:211], v221 offset:18200\n"
"v_max3_f32 v249, v31, v60, v61\n"
"v_mfma_f32_16x16x32_bf16 v[84:87], a[212:215], v[16:19], v[168:171]\n"
"v_max3_f32 v248, v248, v62, v63\n"
"v_max_f32 v248, v248, v249\n"
"v_mfma_f32_16x16x32_bf16 v[84:87], a[216:219], v[48:51], v[84:87]\n"
"v_mov_b32 v249, v248\n"
"s_nop 1\n"
"v_permlane16_swap_b32 v248, v249\n"
"v_mfma_f32_16x16x32_bf16 v[84:87], a[220:223], v[80:83], v[84:87]\n"
"v_max_f32 v248, v248, v249\n"
"v_mov_b32 v249, v248\n"
"v_mfma_f32_16x16x32_bf16 v[84:87], a[224:227], v[112:115], v[84:87]\n"
"s_nop 0\n"
"v_permlane32_swap_b32 v248, v249\n"
"v_max_f32 v248, v248, v249\n"
"v_mfma_f32_16x16x32_bf16 v[116:119], a[228:231], v[16:19], v[168:171]\n"
"v_sub_f32 v172, 0, v248\n"
"v_sub_f32 v173, 0, v248\n"
"v_mfma_f32_16x16x32_bf16 v[116:119], a[232:235], v[48:51], v[116:119]\n"
"v_sub_f32 v174, 0, v248\n"
"v_sub_f32 v175, 0, v248\n"
"v_mfma_f32_16x16x32_bf16 v[116:119], a[236:239], v[80:83], v[116:119]\n"
"v_add_f32 v4, v4, v160\n"
"v_add_f32 v5, v5, v160\n"
"v_mfma_f32_16x16x32_bf16 v[116:119], a[240:243], v[112:115], v[116:119]\n"
"v_add_f32 v6, v6, v160\n"
"v_add_f32 v7, v7, v160\n"
"v_mfma_f32_16x16x32_bf16 v[92:95], a[212:215], v[24:27], v[172:175]\n"
"v_add_f32 v36, v36, v160\n"
"v_add_f32 v37, v37, v160\n"
"v_mfma_f32_16x16x32_bf16 v[92:95], a[216:219], v[56:59], v[92:95]\n"
"v_add_f32 v38, v38, v160\n"
"v_add_f32 v39, v39, v160\n"
"v_mfma_f32_16x16x32_bf16 v[92:95], a[220:223], v[88:91], v[92:95]\n"
"v_exp_f32 v4, v4\n"
"v_mfma_f32_16x16x32_bf16 v[92:95], a[224:227], v[120:123], v[92:95]\n"
"v_exp_f32 v5, v5\n"
"v_mfma_f32_16x16x32_bf16 v[124:127], a[228:231], v[24:27], v[172:175]\n"
"v_exp_f32 v6, v6\n"
"v_exp_f32 v7, v7\n"
"v_exp_f32 v36, v36\n"
"v_exp_f32 v37, v37\n"
"v_exp_f32 v38, v38\n"
"v_exp_f32 v39, v39\n"
"v_mfma_f32_16x16x32_bf16 v[124:127], a[232:235], v[56:59], v[124:127]\n"
"v_cvt_pk_bf16_f32 v4, v4, v5\n"
"v_cvt_pk_bf16_f32 v5, v6, v7\n"
"v_cvt_pk_bf16_f32 v6, v36, v37\n"
"v_cvt_pk_bf16_f32 v7, v38, v39\n"
"v_mfma_f32_16x16x32_bf16 v[124:127], a[236:239], v[88:91], v[124:127]\n"
"v_add_f32 v12, v12, v164\n"
"v_add_f32 v13, v13, v164\n"
"v_mfma_f32_16x16x32_bf16 v[124:127], a[240:243], v[120:123], v[124:127]\n"
"s_add_u32 m0, s77, 98400\n"
"ds_read_b128 a[148:151], v222 offset:16400\n"
"v_add_f32 v14, v14, v164\n"
"v_mfma_f32_16x16x32_bf16 a[128:131], a[144:147], v[4:7], 0\n"
"s_waitcnt lgkmcnt(1)\n"
"ds_read_b128 a[152:155], v223 offset:16400\n"
"v_add_f32 v15, v15, v164\n"
"v_mfma_f32_16x16x32_bf16 a[0:3], a[180:183], v[4:7], 0\n"
"s_addk_i32 s82, 57344\n"
"buffer_load_dwordx4 v226, s[48:51], s82 offen lds\n"
"v_mfma_f32_16x16x32_bf16 a[16:19], a[184:187], v[4:7], 0\n"
"s_addk_i32 m0, 896\n"
"ds_read_b128 a[156:159], v222 offset:24592\n"
"v_add_f32 v44, v44, v164\n"
"v_mfma_f32_16x16x32_bf16 a[32:35], a[188:191], v[4:7], 0\n"
"ds_read_b128 a[160:163], v223 offset:24592\n"
"v_add_f32 v45, v45, v164\n"
"v_mfma_f32_16x16x32_bf16 a[48:51], a[192:195], v[4:7], 0\n"
"ds_read_b128 a[164:167], v222 offset:16912\n"
"v_add_f32 v46, v46, v164\n"
"v_mfma_f32_16x16x32_bf16 a[64:67], a[196:199], v[4:7], 0\n"
"ds_read_b128 a[168:171], v223 offset:16912\n"
"v_add_f32 v47, v47, v164\n"
"v_exp_f32 v12, v12\n"
"v_exp_f32 v13, v13\n"
"v_exp_f32 v14, v14\n"
"v_exp_f32 v15, v15\n"
"v_exp_f32 v44, v44\n"
"v_exp_f32 v45, v45\n"
"v_exp_f32 v46, v46\n"
"v_exp_f32 v47, v47\n"
"v_mfma_f32_16x16x32_bf16 a[80:83], a[200:203], v[4:7], 0\n"
"buffer_load_dwordx4 v226, s[48:51], s82 offen offset:128 lds\n"
"v_cvt_pk_bf16_f32 v12, v12, v13\n"
"v_cvt_pk_bf16_f32 v13, v14, v15\n"
"v_cvt_pk_bf16_f32 v14, v44, v45\n"
"v_cvt_pk_bf16_f32 v15, v46, v47\n"
"v_mfma_f32_16x16x32_bf16 a[96:99], a[204:207], v[4:7], 0\n"
"s_addk_i32 m0, 7304\n"
"ds_read_b128 a[172:175], v222 offset:25104\n"
"v_add_f32 v20, v20, v168\n"
"v_mfma_f32_16x16x32_bf16 a[112:115], a[208:211], v[4:7], 0\n"
"s_addk_i32 s82, 2048\n"
"ds_read_b128 a[176:179], v223 offset:25104\n"
"v_add_f32 v21, v21, v168\n"
"v_mfma_f32_16x16x32_bf16 a[132:135], a[144:147], v[12:15], 0\n"
"v_add_f32 v22, v22, v168\n"
"v_add_f32 v23, v23, v168\n"
"v_mfma_f32_16x16x32_bf16 a[116:119], a[208:211], v[12:15], 0\n"
"v_add_f32 v52, v52, v168\n"
"v_add_f32 v53, v53, v168\n"
"v_mfma_f32_16x16x32_bf16 a[100:103], a[204:207], v[12:15], 0\n"
"buffer_load_dwordx4 v226, s[48:51], s82 offen lds\n"
"v_mfma_f32_16x16x32_bf16 a[84:87], a[200:203], v[12:15], 0\n"
"s_addk_i32 m0, 896\n"
"v_add_f32 v54, v54, v168\n"
"v_add_f32 v55, v55, v168\n"
"v_mfma_f32_16x16x32_bf16 a[68:71], a[196:199], v[12:15], 0\n"
"v_exp_f32 v20, v20\n"
"v_mfma_f32_16x16x32_bf16 a[52:55], a[192:195], v[12:15], 0\n"
"v_exp_f32 v21, v21\n"
"v_exp_f32 v22, v22\n"
"v_exp_f32 v23, v23\n"
"v_exp_f32 v52, v52\n"
"v_exp_f32 v53, v53\n"
"v_exp_f32 v54, v54\n"
"v_exp_f32 v55, v55\n"
"v_mfma_f32_16x16x32_bf16 a[36:39], a[188:191], v[12:15], 0\n"
"v_cvt_pk_bf16_f32 v20, v20, v21\n"
"v_cvt_pk_bf16_f32 v21, v22, v23\n"
"v_cvt_pk_bf16_f32 v22, v52, v53\n"
"v_cvt_pk_bf16_f32 v23, v54, v55\n"
"v_mfma_f32_16x16x32_bf16 a[20:23], a[184:187], v[12:15], 0\n"
"buffer_load_dwordx4 v226, s[48:51], s82 offen offset:128 lds\n"
"v_mfma_f32_16x16x32_bf16 a[4:7], a[180:183], v[12:15], 0\n"
"s_add_u32 m0, s98, 49200\n"
"v_add_f32 v28, v28, v172\n"
"v_add_f32 v29, v29, v172\n"
"v_mfma_f32_16x16x32_bf16 a[136:139], a[144:147], v[20:23], 0\n"
"s_add_u32 s82, s24, 0\n"
"v_add_f32 v30, v30, v172\n"
"v_add_f32 v31, v31, v172\n"
"v_mfma_f32_16x16x32_bf16 a[8:11], a[180:183], v[20:23], 0\n"
"v_add_f32 v60, v60, v172\n"
"v_add_f32 v61, v61, v172\n"
"v_mfma_f32_16x16x32_bf16 a[24:27], a[184:187], v[20:23], 0\n"
"v_add_f32 v62, v62, v172\n"
"v_add_f32 v63, v63, v172\n"
"v_mfma_f32_16x16x32_bf16 a[40:43], a[188:191], v[20:23], 0\n"
"v_exp_f32 v28, v28\n"
"v_mfma_f32_16x16x32_bf16 a[56:59], a[192:195], v[20:23], 0\n"
"v_exp_f32 v29, v29\n"
"v_mfma_f32_16x16x32_bf16 a[72:75], a[196:199], v[20:23], 0\n"
"v_exp_f32 v30, v30\n"
"v_exp_f32 v31, v31\n"
"v_exp_f32 v60, v60\n"
"v_exp_f32 v61, v61\n"
"v_exp_f32 v62, v62\n"
"v_exp_f32 v63, v63\n"
"v_mfma_f32_16x16x32_bf16 a[88:91], a[200:203], v[20:23], 0\n"
"buffer_load_dwordx4 v228, s[52:55], s82 offen nt lds\n"
"v_cvt_pk_bf16_f32 v28, v28, v29\n"
"v_cvt_pk_bf16_f32 v29, v30, v31\n"
"v_cvt_pk_bf16_f32 v30, v60, v61\n"
"v_cvt_pk_bf16_f32 v31, v62, v63\n"
"v_mfma_f32_16x16x32_bf16 a[104:107], a[204:207], v[20:23], 0\n"
"s_addk_i32 m0, 8200\n"
"v_exp_f32 v68, v68\n"
"v_mfma_f32_16x16x32_bf16 a[120:123], a[208:211], v[20:23], 0\n"
"s_add_u32 s82, s24, 10240\n"
"v_exp_f32 v69, v69\n"
"v_mfma_f32_16x16x32_bf16 a[140:143], a[144:147], v[28:31], 0\n"
"v_exp_f32 v70, v70\n"
"v_mfma_f32_16x16x32_bf16 a[124:127], a[208:211], v[28:31], 0\n"
"v_exp_f32 v71, v71\n"
"v_mfma_f32_16x16x32_bf16 a[108:111], a[204:207], v[28:31], 0\n"
"buffer_load_dwordx4 v228, s[52:55], s82 offen nt lds\n"
"v_mfma_f32_16x16x32_bf16 a[92:95], a[200:203], v[28:31], 0\n"
"s_addk_i32 m0, 59384\n"
"v_exp_f32 v100, v100\n"
"v_mfma_f32_16x16x32_bf16 a[76:79], a[196:199], v[28:31], 0\n"
"s_add_u32 s82, s24, 20480\n"
"v_exp_f32 v101, v101\n"
"v_mfma_f32_16x16x32_bf16 a[60:63], a[192:195], v[28:31], 0\n"
"v_exp_f32 v102, v102\n"
"v_mfma_f32_16x16x32_bf16 a[44:47], a[188:191], v[28:31], 0\n"
"v_exp_f32 v103, v103\n"
"v_mfma_f32_16x16x32_bf16 a[28:31], a[184:187], v[28:31], 0\n"
"buffer_load_dwordx4 v228, s[52:55], s82 offen nt lds\n"
"v_mfma_f32_16x16x32_bf16 a[12:15], a[180:183], v[28:31], 0\n"
"s_waitcnt lgkmcnt(0)\n"
"ds_read_b64_tr_b16 a[180:181], v221 offset:20496\n"
"v_cvt_pk_bf16_f32 v68, v68, v69\n"
"v_mfma_f32_16x16x32_bf16 v[4:7], a[148:151], v[0:3], v[160:163]\n"
"s_addk_i32 m0, 8200\n"
"ds_read_b64_tr_b16 a[182:183], v221 offset:20752\n"
"v_cvt_pk_bf16_f32 v69, v70, v71\n"
"v_mfma_f32_16x16x32_bf16 v[4:7], a[152:155], v[32:35], v[4:7]\n"
"s_add_u32 s82, s24, 30720\n"
"ds_read_b64_tr_b16 a[184:185], v221 offset:20504\n"
"v_cvt_pk_bf16_f32 v70, v100, v101\n"
"v_mfma_f32_16x16x32_bf16 v[4:7], a[156:159], v[64:67], v[4:7]\n"
"ds_read_b64_tr_b16 a[186:187], v221 offset:20760\n"
"v_cvt_pk_bf16_f32 v71, v102, v103\n"
"v_mfma_f32_16x16x32_bf16 v[4:7], a[160:163], v[96:99], v[4:7]\n"
"buffer_load_dwordx4 v228, s[52:55], s82 offen nt lds\n"
"v_mfma_f32_16x16x32_bf16 v[36:39], a[164:167], v[0:3], v[160:163]\n"
"ds_read_b64_tr_b16 a[188:189], v221 offset:21008\n"
"v_mfma_f32_16x16x32_bf16 v[36:39], a[168:171], v[32:35], v[36:39]\n"
"ds_read_b64_tr_b16 a[190:191], v221 offset:21264\n"
"v_mfma_f32_16x16x32_bf16 v[36:39], a[172:175], v[64:67], v[36:39]\n"
"ds_read_b64_tr_b16 a[192:193], v221 offset:21016\n"
"v_mfma_f32_16x16x32_bf16 v[36:39], a[176:179], v[96:99], v[36:39]\n"
"ds_read_b64_tr_b16 a[194:195], v221 offset:21272\n"
"v_mfma_f32_16x16x32_bf16 v[12:15], a[148:151], v[8:11], v[164:167]\n"
"ds_read_b64_tr_b16 a[196:197], v221 offset:21520\n"
"v_mfma_f32_16x16x32_bf16 v[12:15], a[152:155], v[40:43], v[12:15]\n"
"ds_read_b64_tr_b16 a[198:199], v221 offset:21776\n"
"v_mfma_f32_16x16x32_bf16 v[12:15], a[156:159], v[72:75], v[12:15]\n"
"ds_read_b64_tr_b16 a[200:201], v221 offset:21528\n"
"v_mfma_f32_16x16x32_bf16 v[12:15], a[160:163], v[104:107], v[12:15]\n"
"ds_read_b64_tr_b16 a[202:203], v221 offset:21784\n"
"v_mfma_f32_16x16x32_bf16 v[44:47], a[164:167], v[8:11], v[164:167]\n"
"ds_read_b64_tr_b16 a[204:205], v221 offset:22032\n"
"v_mfma_f32_16x16x32_bf16 v[44:47], a[168:171], v[40:43], v[44:47]\n"
"ds_read_b64_tr_b16 a[206:207], v221 offset:22288\n"
"v_mfma_f32_16x16x32_bf16 v[44:47], a[172:175], v[72:75], v[44:47]\n"
"ds_read_b64_tr_b16 a[208:209], v221 offset:22040\n"
"v_mfma_f32_16x16x32_bf16 v[44:47], a[176:179], v[104:107], v[44:47]\n"
"ds_read_b64_tr_b16 a[210:211], v221 offset:22296\n"
"v_mfma_f32_16x16x32_bf16 v[20:23], a[148:151], v[16:19], v[168:171]\n"
"v_exp_f32 v76, v76\n"
"v_mfma_f32_16x16x32_bf16 v[20:23], a[152:155], v[48:51], v[20:23]\n"
"v_exp_f32 v77, v77\n"
"v_mfma_f32_16x16x32_bf16 v[20:23], a[156:159], v[80:83], v[20:23]\n"
"v_exp_f32 v78, v78\n"
"v_mfma_f32_16x16x32_bf16 v[20:23], a[160:163], v[112:115], v[20:23]\n"
"v_exp_f32 v79, v79\n"
"v_mfma_f32_16x16x32_bf16 v[52:55], a[164:167], v[16:19], v[168:171]\n"
"v_exp_f32 v108, v108\n"
"v_mfma_f32_16x16x32_bf16 v[52:55], a[168:171], v[48:51], v[52:55]\n"
"v_exp_f32 v109, v109\n"
"v_mfma_f32_16x16x32_bf16 v[52:55], a[172:175], v[80:83], v[52:55]\n"
"v_exp_f32 v110, v110\n"
"v_mfma_f32_16x16x32_bf16 v[52:55], a[176:179], v[112:115], v[52:55]\n"
"v_exp_f32 v111, v111\n"
"v_mfma_f32_16x16x32_bf16 v[28:31], a[148:151], v[24:27], v[172:175]\n"
"v_cvt_pk_bf16_f32 v76, v76, v77\n"
"v_cvt_pk_bf16_f32 v77, v78, v79\n"
"v_mfma_f32_16x16x32_bf16 v[28:31], a[152:155], v[56:59], v[28:31]\n"
"v_cvt_pk_bf16_f32 v78, v108, v109\n"
"v_cvt_pk_bf16_f32 v79, v110, v111\n"
"v_mfma_f32_16x16x32_bf16 v[28:31], a[156:159], v[88:91], v[28:31]\n"
"v_exp_f32 v84, v84\n"
"v_mfma_f32_16x16x32_bf16 v[28:31], a[160:163], v[120:123], v[28:31]\n"
"v_exp_f32 v85, v85\n"
"v_mfma_f32_16x16x32_bf16 v[60:63], a[164:167], v[24:27], v[172:175]\n"
"v_exp_f32 v86, v86\n"
"v_mfma_f32_16x16x32_bf16 v[60:63], a[168:171], v[56:59], v[60:63]\n"
"v_exp_f32 v87, v87\n"
"v_mfma_f32_16x16x32_bf16 v[60:63], a[172:175], v[88:91], v[60:63]\n"
"v_exp_f32 v116, v116\n"
"v_mfma_f32_16x16x32_bf16 v[60:63], a[176:179], v[120:123], v[60:63]\n"
"s_add_u32 m0, s76, 0\n"
"v_exp_f32 v117, v117\n"
"v_mfma_f32_16x16x32_bf16 a[128:131], a[144:147], v[68:71], a[128:131]\n"
"s_waitcnt lgkmcnt(0)\n"
"v_exp_f32 v118, v118\n"
"v_mfma_f32_16x16x32_bf16 a[0:3], a[180:183], v[68:71], a[0:3]\n"
"s_mov_b32 s82, 49152\n"
"buffer_load_dwordx4 v225, s[44:47], s82 offen lds\n"
"v_mfma_f32_16x16x32_bf16 a[16:19], a[184:187], v[68:71], a[16:19]\n"
"s_addk_i32 m0, 8064\n"
"v_exp_f32 v119, v119\n"
"v_mfma_f32_16x16x32_bf16 a[32:35], a[188:191], v[68:71], a[32:35]\n"
"ds_read_b128 a[148:151], v222 offset:20496\n"
"v_cvt_pk_bf16_f32 v84, v84, v85\n"
"v_mfma_f32_16x16x32_bf16 a[48:51], a[192:195], v[68:71], a[48:51]\n"
"ds_read_b128 a[152:155], v223 offset:20496\n"
"v_cvt_pk_bf16_f32 v85, v86, v87\n"
"v_mfma_f32_16x16x32_bf16 a[64:67], a[196:199], v[68:71], a[64:67]\n"
"ds_read_b128 a[156:159], v222 offset:28688\n"
"v_cvt_pk_bf16_f32 v86, v116, v117\n"
"v_mfma_f32_16x16x32_bf16 a[80:83], a[200:203], v[68:71], a[80:83]\n"
"buffer_load_dwordx4 v225, s[44:47], s82 offen offset:128 lds\n"
"v_mfma_f32_16x16x32_bf16 a[96:99], a[204:207], v[68:71], a[96:99]\n"
"s_addk_i32 m0, 61568\n"
"ds_read_b128 a[160:163], v223 offset:28688\n"
"v_cvt_pk_bf16_f32 v87, v118, v119\n"
"v_mfma_f32_16x16x32_bf16 a[112:115], a[208:211], v[68:71], a[112:115]\n"
"s_addk_i32 s82, 8192\n"
"v_exp_f32 v92, v92\n"
"v_mfma_f32_16x16x32_bf16 a[132:135], a[144:147], v[76:79], a[132:135]\n"
"v_exp_f32 v93, v93\n"
"v_mfma_f32_16x16x32_bf16 a[116:119], a[208:211], v[76:79], a[116:119]\n"
"v_exp_f32 v94, v94\n"
"v_mfma_f32_16x16x32_bf16 a[100:103], a[204:207], v[76:79], a[100:103]\n"
"buffer_load_dwordx4 v225, s[44:47], s82 offen lds\n"
"v_mfma_f32_16x16x32_bf16 a[84:87], a[200:203], v[76:79], a[84:87]\n"
"s_addk_i32 m0, 8064\n"
"v_exp_f32 v95, v95\n"
"v_mfma_f32_16x16x32_bf16 a[68:71], a[196:199], v[76:79], a[68:71]\n"
"v_exp_f32 v124, v124\n"
"v_mfma_f32_16x16x32_bf16 a[52:55], a[192:195], v[76:79], a[52:55]\n"
"v_exp_f32 v125, v125\n"
"v_mfma_f32_16x16x32_bf16 a[36:39], a[188:191], v[76:79], a[36:39]\n"
"v_exp_f32 v126, v126\n"
"v_mfma_f32_16x16x32_bf16 a[20:23], a[184:187], v[76:79], a[20:23]\n"
"buffer_load_dwordx4 v225, s[44:47], s82 offen offset:128 lds\n"
"v_mfma_f32_16x16x32_bf16 a[4:7], a[180:183], v[76:79], a[4:7]\n"
"s_add_u32 m0, s98, 65600\n"
"v_exp_f32 v127, v127\n"
"v_mfma_f32_16x16x32_bf16 a[136:139], a[144:147], v[84:87], a[136:139]\n"
"s_add_u32 s82, s25, 0\n"
"ds_read_b128 a[164:167], v222 offset:21008\n"
"v_cvt_pk_bf16_f32 v92, v92, v93\n"
"v_mfma_f32_16x16x32_bf16 a[8:11], a[180:183], v[84:87], a[8:11]\n"
"ds_read_b128 a[168:171], v223 offset:21008\n"
"v_cvt_pk_bf16_f32 v93, v94, v95\n"
"v_mfma_f32_16x16x32_bf16 a[24:27], a[184:187], v[84:87], a[24:27]\n"
"ds_read_b128 a[172:175], v222 offset:29200\n"
"v_cvt_pk_bf16_f32 v94, v124, v125\n"
"v_mfma_f32_16x16x32_bf16 a[40:43], a[188:191], v[84:87], a[40:43]\n"
"buffer_load_dwordx4 v228, s[56:59], s82 offen nt lds\n"
"v_mfma_f32_16x16x32_bf16 a[56:59], a[192:195], v[84:87], a[56:59]\n"
"s_addk_i32 m0, 8200\n"
"ds_read_b128 a[176:179], v223 offset:29200\n"
"v_cvt_pk_bf16_f32 v95, v126, v127\n"
"v_mfma_f32_16x16x32_bf16 a[72:75], a[196:199], v[84:87], a[72:75]\n"
"s_add_u32 s82, s25, 10240\n"
"v_exp_f32 v4, v4\n"
"v_mfma_f32_16x16x32_bf16 a[88:91], a[200:203], v[84:87], a[88:91]\n"
"v_exp_f32 v5, v5\n"
"v_mfma_f32_16x16x32_bf16 a[104:107], a[204:207], v[84:87], a[104:107]\n"
"v_exp_f32 v6, v6\n"
"v_mfma_f32_16x16x32_bf16 a[120:123], a[208:211], v[84:87], a[120:123]\n"
"buffer_load_dwordx4 v228, s[56:59], s82 offen nt lds\n"
"v_mfma_f32_16x16x32_bf16 a[140:143], a[144:147], v[92:95], a[140:143]\n"
"s_addk_i32 m0, 59384\n"
"v_exp_f32 v7, v7\n"
"v_mfma_f32_16x16x32_bf16 a[124:127], a[208:211], v[92:95], a[124:127]\n"
"s_add_u32 s82, s25, 20480\n"
"v_exp_f32 v36, v36\n"
"v_mfma_f32_16x16x32_bf16 a[108:111], a[204:207], v[92:95], a[108:111]\n"
"L_st5_%=:\n"
"s_cbranch_execz L_st9_%=\n"
"v_exp_f32 v37, v37\n"
"v_mfma_f32_16x16x32_bf16 a[92:95], a[200:203], v[92:95], a[92:95]\n"
"v_exp_f32 v38, v38\n"
"v_mfma_f32_16x16x32_bf16 a[76:79], a[196:199], v[92:95], a[76:79]\n"
"buffer_load_dwordx4 v228, s[56:59], s82 offen nt lds\n"
"v_mfma_f32_16x16x32_bf16 a[60:63], a[192:195], v[92:95], a[60:63]\n"
"s_addk_i32 m0, 8200\n"
"v_exp_f32 v39, v39\n"
"v_mfma_f32_16x16x32_bf16 a[44:47], a[188:191], v[92:95], a[44:47]\n"
"s_add_u32 s82, s25, 30720\n"
"v_cvt_pk_bf16_f32 v4, v4, v5\n"
"v_cvt_pk_bf16_f32 v5, v6, v7\n"
"v_mfma_f32_16x16x32_bf16 a[28:31], a[184:187], v[92:95], a[28:31]\n"
"v_cvt_pk_bf16_f32 v6, v36, v37\n"
"v_cvt_pk_bf16_f32 v7, v38, v39\n"
"v_mfma_f32_16x16x32_bf16 a[12:15], a[180:183], v[92:95], a[12:15]\n"
"s_waitcnt lgkmcnt(0)\n"
"s_getpc_b64 s[4:5]\n"
"L_pc1_%=:\n"
"s_mov_b32 s6, L_end_%=-L_pc1_%=\n"
"s_and_b32 s5, s5, 0xffff\n"
"s_mov_b32 s7, 0x20000\n"
"v_mfma_f32_16x16x32_bf16 v[68:71], a[148:151], v[0:3], v[160:163]\n"
"s_waitcnt vmcnt(15)\n"
"buffer_load_dword v247, v252, s[4:7], 0 offen\n"
"v_mfma_f32_16x16x32_bf16 v[68:71], a[152:155], v[32:35], v[68:71]\n"
"s_barrier\n"
"ds_read_b64_tr_b16 a[180:181], v221 offset:32800\n"
"v_mfma_f32_16x16x32_bf16 v[68:71], a[156:159], v[64:67], v[68:71]\n"
"ds_read_b64_tr_b16 a[182:183], v221 offset:33056\n"
"v_mfma_f32_16x16x32_bf16 v[68:71], a[160:163], v[96:99], v[68:71]\n"
"ds_read_b64_tr_b16 a[184:185], v221 offset:32808\n"
"v_mfma_f32_16x16x32_bf16 v[100:103], a[164:167], v[0:3], v[160:163]\n"
"ds_read_b64_tr_b16 a[186:187], v221 offset:33064\n"
"v_mfma_f32_16x16x32_bf16 v[100:103], a[168:171], v[32:35], v[100:103]\n"
"buffer_load_dwordx4 v228, s[56:59], s82 offen nt lds\n"
"v_mfma_f32_16x16x32_bf16 v[100:103], a[172:175], v[64:67], v[100:103]\n"
"ds_read_b64_tr_b16 a[188:189], v221 offset:33312\n"
"v_mfma_f32_16x16x32_bf16 v[100:103], a[176:179], v[96:99], v[100:103]\n"
"ds_read_b64_tr_b16 a[190:191], v221 offset:33568\n"
"v_mfma_f32_16x16x32_bf16 v[76:79], a[148:151], v[8:11], v[164:167]\n"
"ds_read_b64_tr_b16 a[192:193], v221 offset:33320\n"
"v_mfma_f32_16x16x32_bf16 v[76:79], a[152:155], v[40:43], v[76:79]\n"
"ds_read_b64_tr_b16 a[194:195], v221 offset:33576\n"
"v_mfma_f32_16x16x32_bf16 v[76:79], a[156:159], v[72:75], v[76:79]\n"
"ds_read_b64_tr_b16 a[196:197], v221 offset:33824\n"
"v_mfma_f32_16x16x32_bf16 v[76:79], a[160:163], v[104:107], v[76:79]\n"
"ds_read_b64_tr_b16 a[198:199], v221 offset:34080\n"
"v_mfma_f32_16x16x32_bf16 v[108:111], a[164:167], v[8:11], v[164:167]\n"
"ds_read_b64_tr_b16 a[200:201], v221 offset:33832\n"
"v_mfma_f32_16x16x32_bf16 v[108:111], a[168:171], v[40:43], v[108:111]\n"
"ds_read_b64_tr_b16 a[202:203], v221 offset:34088\n"
"v_mfma_f32_16x16x32_bf16 v[108:111], a[172:175], v[72:75], v[108:111]\n"
"ds_read_b64_tr_b16 a[204:205], v221 offset:34336\n"
"v_mfma_f32_16x16x32_bf16 v[108:111], a[176:179], v[104:107], v[108:111]\n"
"ds_read_b64_tr_b16 a[206:207], v221 offset:34592\n"
"v_mfma_f32_16x16x32_bf16 v[84:87], a[148:151], v[16:19], v[168:171]\n"
"ds_read_b64_tr_b16 a[208:209], v221 offset:34344\n"
"v_mfma_f32_16x16x32_bf16 v[84:87], a[152:155], v[48:51], v[84:87]\n"
"ds_read_b64_tr_b16 a[210:211], v221 offset:34600\n"
"v_mfma_f32_16x16x32_bf16 v[84:87], a[156:159], v[80:83], v[84:87]\n"
"v_exp_f32 v12, v12\n"
"v_mfma_f32_16x16x32_bf16 v[84:87], a[160:163], v[112:115], v[84:87]\n"
"v_exp_f32 v13, v13\n"
"v_mfma_f32_16x16x32_bf16 v[116:119], a[164:167], v[16:19], v[168:171]\n"
"v_exp_f32 v14, v14\n"
"v_mfma_f32_16x16x32_bf16 v[116:119], a[168:171], v[48:51], v[116:119]\n"
"v_exp_f32 v15, v15\n"
"v_mfma_f32_16x16x32_bf16 v[116:119], a[172:175], v[80:83], v[116:119]\n"
"v_exp_f32 v44, v44\n"
"v_mfma_f32_16x16x32_bf16 v[116:119], a[176:179], v[112:115], v[116:119]\n"
"v_exp_f32 v45, v45\n"
"v_mfma_f32_16x16x32_bf16 v[92:95], a[148:151], v[24:27], v[172:175]\n"
"v_exp_f32 v46, v46\n"
"v_mfma_f32_16x16x32_bf16 v[92:95], a[152:155], v[56:59], v[92:95]\n"
"v_exp_f32 v47, v47\n"
"v_mfma_f32_16x16x32_bf16 v[92:95], a[156:159], v[88:91], v[92:95]\n"
"v_cvt_pk_bf16_f32 v12, v12, v13\n"
"v_cvt_pk_bf16_f32 v13, v14, v15\n"
"v_mfma_f32_16x16x32_bf16 v[92:95], a[160:163], v[120:123], v[92:95]\n"
"v_cvt_pk_bf16_f32 v14, v44, v45\n"
"v_cvt_pk_bf16_f32 v15, v46, v47\n"
"v_mfma_f32_16x16x32_bf16 v[124:127], a[164:167], v[24:27], v[172:175]\n"
"v_exp_f32 v20, v20\n"
"v_mfma_f32_16x16x32_bf16 v[124:127], a[168:171], v[56:59], v[124:127]\n"
"v_exp_f32 v21, v21\n"
"v_mfma_f32_16x16x32_bf16 v[124:127], a[172:175], v[88:91], v[124:127]\n"
"v_exp_f32 v22, v22\n"
"v_mfma_f32_16x16x32_bf16 v[124:127], a[176:179], v[120:123], v[124:127]\n"
"s_add_u32 m0, s77, 114800\n"
"v_exp_f32 v23, v23\n"
"v_mfma_f32_16x16x32_bf16 a[128:131], a[144:147], v[4:7], a[128:131]\n"
"s_waitcnt lgkmcnt(0)\n"
"v_exp_f32 v52, v52\n"
"v_mfma_f32_16x16x32_bf16 a[0:3], a[180:183], v[4:7], a[0:3]\n"
"s_mov_b32 s82, 49152\n"
"buffer_load_dwordx4 v226, s[48:51], s82 offen lds\n"
"v_mfma_f32_16x16x32_bf16 a[16:19], a[184:187], v[4:7], a[16:19]\n"
"s_addk_i32 m0, 896\n"
"v_exp_f32 v53, v53\n"
"v_mfma_f32_16x16x32_bf16 a[32:35], a[188:191], v[4:7], a[32:35]\n"
"v_exp_f32 v54, v54\n"
"v_mfma_f32_16x16x32_bf16 a[48:51], a[192:195], v[4:7], a[48:51]\n"
"v_exp_f32 v55, v55\n"
"v_mfma_f32_16x16x32_bf16 a[64:67], a[196:199], v[4:7], a[64:67]\n"
"ds_read_b128 a[148:151], v222 offset:32800\n"
"v_cvt_pk_bf16_f32 v20, v20, v21\n"
"v_mfma_f32_16x16x32_bf16 a[80:83], a[200:203], v[4:7], a[80:83]\n"
"buffer_load_dwordx4 v226, s[48:51], s82 offen offset:128 lds\n"
"v_mfma_f32_16x16x32_bf16 a[96:99], a[204:207], v[4:7], a[96:99]\n"
"s_addk_i32 m0, 7304\n"
"ds_read_b128 a[152:155], v223 offset:32800\n"
"v_cvt_pk_bf16_f32 v21, v22, v23\n"
"v_mfma_f32_16x16x32_bf16 a[112:115], a[208:211], v[4:7], a[112:115]\n"
"s_addk_i32 s82, 2048\n"
"ds_read_b128 a[156:159], v222 offset:40992\n"
"v_cvt_pk_bf16_f32 v22, v52, v53\n"
"v_mfma_f32_16x16x32_bf16 a[132:135], a[144:147], v[12:15], a[132:135]\n"
"ds_read_b128 a[160:163], v223 offset:40992\n"
"v_cvt_pk_bf16_f32 v23, v54, v55\n"
"v_mfma_f32_16x16x32_bf16 a[116:119], a[208:211], v[12:15], a[116:119]\n"
"v_exp_f32 v28, v28\n"
"v_mfma_f32_16x16x32_bf16 a[100:103], a[204:207], v[12:15], a[100:103]\n"
"buffer_load_dwordx4 v226, s[48:51], s82 offen lds\n"
"v_mfma_f32_16x16x32_bf16 a[84:87], a[200:203], v[12:15], a[84:87]\n"
"s_addk_i32 m0, 896\n"
"v_exp_f32 v29, v29\n"
"v_mfma_f32_16x16x32_bf16 a[68:71], a[196:199], v[12:15], a[68:71]\n"
"v_exp_f32 v30, v30\n"
"v_mfma_f32_16x16x32_bf16 a[52:55], a[192:195], v[12:15], a[52:55]\n"
"v_exp_f32 v31, v31\n"
"v_mfma_f32_16x16x32_bf16 a[36:39], a[188:191], v[12:15], a[36:39]\n"
"v_exp_f32 v60, v60\n"
"v_mfma_f32_16x16x32_bf16 a[20:23], a[184:187], v[12:15], a[20:23]\n"
"buffer_load_dwordx4 v226, s[48:51], s82 offen offset:128 lds\n"
"v_mfma_f32_16x16x32_bf16 a[4:7], a[180:183], v[12:15], a[4:7]\n"
"s_add_u32 m0, s77, 82000\n"
"v_exp_f32 v61, v61\n"
"v_mfma_f32_16x16x32_bf16 a[136:139], a[144:147], v[20:23], a[136:139]\n"
"s_mov_b32 s82, 0\n"
"v_exp_f32 v62, v62\n"
"v_mfma_f32_16x16x32_bf16 a[8:11], a[180:183], v[20:23], a[8:11]\n"
"v_exp_f32 v63, v63\n"
"v_mfma_f32_16x16x32_bf16 a[24:27], a[184:187], v[20:23], a[24:27]\n"
"ds_read_b128 a[164:167], v222 offset:33312\n"
"v_cvt_pk_bf16_f32 v28, v28, v29\n"
"v_mfma_f32_16x16x32_bf16 a[40:43], a[188:191], v[20:23], a[40:43]\n"
"buffer_load_dwordx4 v227, s[36:39], s82 offen lds\n"
"v_mfma_f32_16x16x32_bf16 a[56:59], a[192:195], v[20:23], a[56:59]\n"
"s_addk_i32 m0, 1024\n"
"ds_read_b128 a[168:171], v223 offset:33312\n"
"v_cvt_pk_bf16_f32 v29, v30, v31\n"
"v_mfma_f32_16x16x32_bf16 a[72:75], a[196:199], v[20:23], a[72:75]\n"
"ds_read_b128 a[172:175], v222 offset:41504\n"
"v_cvt_pk_bf16_f32 v30, v60, v61\n"
"v_mfma_f32_16x16x32_bf16 a[88:91], a[200:203], v[20:23], a[88:91]\n"
"ds_read_b128 a[176:179], v223 offset:41504\n"
"v_cvt_pk_bf16_f32 v31, v62, v63\n"
"v_mfma_f32_16x16x32_bf16 a[104:107], a[204:207], v[20:23], a[104:107]\n"
"v_exp_f32 v68, v68\n"
"v_mfma_f32_16x16x32_bf16 a[120:123], a[208:211], v[20:23], a[120:123]\n"
"buffer_load_dwordx4 v227, s[40:43], s82 offen lds\n"
"v_mfma_f32_16x16x32_bf16 a[140:143], a[144:147], v[28:31], a[140:143]\n"
"s_addk_i32 m0, 7176\n"
"v_exp_f32 v69, v69\n"
"v_mfma_f32_16x16x32_bf16 a[124:127], a[208:211], v[28:31], a[124:127]\n"
"s_movk_i32 s82, 27648\n"
"v_exp_f32 v70, v70\n"
"v_mfma_f32_16x16x32_bf16 a[108:111], a[204:207], v[28:31], a[108:111]\n"
"v_exp_f32 v71, v71\n"
"v_mfma_f32_16x16x32_bf16 a[92:95], a[200:203], v[28:31], a[92:95]\n"
"v_exp_f32 v100, v100\n"
"v_mfma_f32_16x16x32_bf16 a[76:79], a[196:199], v[28:31], a[76:79]\n"
"buffer_load_dwordx4 v227, s[36:39], s82 offen lds\n"
"v_mfma_f32_16x16x32_bf16 a[60:63], a[192:195], v[28:31], a[60:63]\n"
"s_addk_i32 m0, 1024\n"
"v_exp_f32 v101, v101\n"
"v_mfma_f32_16x16x32_bf16 a[44:47], a[188:191], v[28:31], a[44:47]\n"
"v_exp_f32 v102, v102\n"
"v_mfma_f32_16x16x32_bf16 a[28:31], a[184:187], v[28:31], a[28:31]\n"
"v_exp_f32 v103, v103\n"
"v_mfma_f32_16x16x32_bf16 a[12:15], a[180:183], v[28:31], a[12:15]\n"
"s_waitcnt lgkmcnt(1)\n"
"ds_read_b64_tr_b16 a[180:181], v221 offset:36896\n"
"v_cvt_pk_bf16_f32 v68, v68, v69\n"
"v_mfma_f32_16x16x32_bf16 v[4:7], a[148:151], v[0:3], v[160:163]\n"
"buffer_load_dwordx4 v227, s[40:43], s82 offen lds\n"
"v_mfma_f32_16x16x32_bf16 v[4:7], a[152:155], v[32:35], v[4:7]\n"
"ds_read_b64_tr_b16 a[182:183], v221 offset:37152\n"
"v_cvt_pk_bf16_f32 v69, v70, v71\n"
"v_mfma_f32_16x16x32_bf16 v[4:7], a[156:159], v[64:67], v[4:7]\n"
"ds_read_b64_tr_b16 a[184:185], v221 offset:36904\n"
"v_cvt_pk_bf16_f32 v70, v100, v101\n"
"v_mfma_f32_16x16x32_bf16 v[4:7], a[160:163], v[96:99], v[4:7]\n"
"ds_read_b64_tr_b16 a[186:187], v221 offset:37160\n"
"v_cvt_pk_bf16_f32 v71, v102, v103\n"
"v_mfma_f32_16x16x32_bf16 v[36:39], a[164:167], v[0:3], v[160:163]\n"
"ds_read_b64_tr_b16 a[188:189], v221 offset:37408\n"
"v_mfma_f32_16x16x32_bf16 v[36:39], a[168:171], v[32:35], v[36:39]\n"
"ds_read_b64_tr_b16 a[190:191], v221 offset:37664\n"
"v_mfma_f32_16x16x32_bf16 v[36:39], a[172:175], v[64:67], v[36:39]\n"
"s_waitcnt lgkmcnt(6)\n"
"ds_read_b64_tr_b16 a[192:193], v221 offset:37416\n"
"v_mfma_f32_16x16x32_bf16 v[36:39], a[176:179], v[96:99], v[36:39]\n"
"ds_read_b64_tr_b16 a[194:195], v221 offset:37672\n"
"v_mfma_f32_16x16x32_bf16 v[12:15], a[148:151], v[8:11], v[164:167]\n"
"ds_read_b64_tr_b16 a[196:197], v221 offset:37920\n"
"v_mfma_f32_16x16x32_bf16 v[12:15], a[152:155], v[40:43], v[12:15]\n"
"ds_read_b64_tr_b16 a[198:199], v221 offset:38176\n"
"v_mfma_f32_16x16x32_bf16 v[12:15], a[156:159], v[72:75], v[12:15]\n"
"ds_read_b64_tr_b16 a[200:201], v221 offset:37928\n"
"v_mfma_f32_16x16x32_bf16 v[12:15], a[160:163], v[104:107], v[12:15]\n"
"ds_read_b64_tr_b16 a[202:203], v221 offset:38184\n"
"v_mfma_f32_16x16x32_bf16 v[44:47], a[164:167], v[8:11], v[164:167]\n"
"ds_read_b64_tr_b16 a[204:205], v221 offset:38432\n"
"v_mfma_f32_16x16x32_bf16 v[44:47], a[168:171], v[40:43], v[44:47]\n"
"ds_read_b64_tr_b16 a[206:207], v221 offset:38688\n"
"v_mfma_f32_16x16x32_bf16 v[44:47], a[172:175], v[72:75], v[44:47]\n"
"ds_read_b64_tr_b16 a[208:209], v221 offset:38440\n"
"v_mfma_f32_16x16x32_bf16 v[44:47], a[176:179], v[104:107], v[44:47]\n"
"ds_read_b64_tr_b16 a[210:211], v221 offset:38696\n"
"v_mfma_f32_16x16x32_bf16 v[20:23], a[148:151], v[16:19], v[168:171]\n"
"v_exp_f32 v76, v76\n"
"v_mfma_f32_16x16x32_bf16 v[20:23], a[152:155], v[48:51], v[20:23]\n"
"v_exp_f32 v77, v77\n"
"v_mfma_f32_16x16x32_bf16 v[20:23], a[156:159], v[80:83], v[20:23]\n"
"v_exp_f32 v78, v78\n"
"v_mfma_f32_16x16x32_bf16 v[20:23], a[160:163], v[112:115], v[20:23]\n"
"v_exp_f32 v79, v79\n"
"v_mfma_f32_16x16x32_bf16 v[52:55], a[164:167], v[16:19], v[168:171]\n"
"v_exp_f32 v108, v108\n"
"v_mfma_f32_16x16x32_bf16 v[52:55], a[168:171], v[48:51], v[52:55]\n"
"v_exp_f32 v109, v109\n"
"v_mfma_f32_16x16x32_bf16 v[52:55], a[172:175], v[80:83], v[52:55]\n"
"v_exp_f32 v110, v110\n"
"v_mfma_f32_16x16x32_bf16 v[52:55], a[176:179], v[112:115], v[52:55]\n"
"v_exp_f32 v111, v111\n"
"v_mfma_f32_16x16x32_bf16 v[28:31], a[148:151], v[24:27], v[172:175]\n"
"v_cvt_pk_bf16_f32 v76, v76, v77\n"
"v_cvt_pk_bf16_f32 v77, v78, v79\n"
"v_mfma_f32_16x16x32_bf16 v[28:31], a[152:155], v[56:59], v[28:31]\n"
"v_cvt_pk_bf16_f32 v78, v108, v109\n"
"v_cvt_pk_bf16_f32 v79, v110, v111\n"
"v_mfma_f32_16x16x32_bf16 v[28:31], a[156:159], v[88:91], v[28:31]\n"
"v_exp_f32 v84, v84\n"
"v_mfma_f32_16x16x32_bf16 v[28:31], a[160:163], v[120:123], v[28:31]\n"
"v_exp_f32 v85, v85\n"
"v_mfma_f32_16x16x32_bf16 v[60:63], a[164:167], v[24:27], v[172:175]\n"
"v_exp_f32 v86, v86\n"
"v_mfma_f32_16x16x32_bf16 v[60:63], a[168:171], v[56:59], v[60:63]\n"
"v_exp_f32 v87, v87\n"
"v_mfma_f32_16x16x32_bf16 v[60:63], a[172:175], v[88:91], v[60:63]\n"
"v_exp_f32 v116, v116\n"
"v_mfma_f32_16x16x32_bf16 v[60:63], a[176:179], v[120:123], v[60:63]\n"
"s_add_u32 m0, s76, 16400\n"
"v_exp_f32 v117, v117\n"
"v_mfma_f32_16x16x32_bf16 a[128:131], a[144:147], v[68:71], a[128:131]\n"
"s_waitcnt lgkmcnt(0)\n"
"v_exp_f32 v118, v118\n"
"v_mfma_f32_16x16x32_bf16 a[0:3], a[180:183], v[68:71], a[0:3]\n"
"s_mov_b32 s82, 65536\n"
"buffer_load_dwordx4 v225, s[44:47], s82 offen lds\n"
"v_mfma_f32_16x16x32_bf16 a[16:19], a[184:187], v[68:71], a[16:19]\n"
"s_addk_i32 m0, 8064\n"
"v_exp_f32 v119, v119\n"
"v_mfma_f32_16x16x32_bf16 a[32:35], a[188:191], v[68:71], a[32:35]\n"
"ds_read_b128 a[148:151], v222 offset:36896\n"
"v_cvt_pk_bf16_f32 v84, v84, v85\n"
"v_mfma_f32_16x16x32_bf16 a[48:51], a[192:195], v[68:71], a[48:51]\n"
"ds_read_b128 a[152:155], v223 offset:36896\n"
"v_cvt_pk_bf16_f32 v85, v86, v87\n"
"v_mfma_f32_16x16x32_bf16 a[64:67], a[196:199], v[68:71], a[64:67]\n"
"ds_read_b128 a[156:159], v222 offset:45088\n"
"v_cvt_pk_bf16_f32 v86, v116, v117\n"
"v_mfma_f32_16x16x32_bf16 a[80:83], a[200:203], v[68:71], a[80:83]\n"
"buffer_load_dwordx4 v225, s[44:47], s82 offen offset:128 lds\n"
"v_mfma_f32_16x16x32_bf16 a[96:99], a[204:207], v[68:71], a[96:99]\n"
"s_addk_i32 m0, 61568\n"
"ds_read_b128 a[160:163], v223 offset:45088\n"
"v_cvt_pk_bf16_f32 v87, v118, v119\n"
"v_mfma_f32_16x16x32_bf16 a[112:115], a[208:211], v[68:71], a[112:115]\n"
"s_addk_i32 s82, 8192\n"
"v_exp_f32 v92, v92\n"
"v_mfma_f32_16x16x32_bf16 a[132:135], a[144:147], v[76:79], a[132:135]\n"
"v_exp_f32 v93, v93\n"
"v_mfma_f32_16x16x32_bf16 a[116:119], a[208:211], v[76:79], a[116:119]\n"
"v_exp_f32 v94, v94\n"
"v_mfma_f32_16x16x32_bf16 a[100:103], a[204:207], v[76:79], a[100:103]\n"
"buffer_load_dwordx4 v225, s[44:47], s82 offen lds\n"
"v_mfma_f32_16x16x32_bf16 a[84:87], a[200:203], v[76:79], a[84:87]\n"
"s_addk_i32 m0, 8064\n"
"v_exp_f32 v95, v95\n"
"v_mfma_f32_16x16x32_bf16 a[68:71], a[196:199], v[76:79], a[68:71]\n"
"v_exp_f32 v124, v124\n"
"v_mfma_f32_16x16x32_bf16 a[52:55], a[192:195], v[76:79], a[52:55]\n"
"v_exp_f32 v125, v125\n"
"v_mfma_f32_16x16x32_bf16 a[36:39], a[188:191], v[76:79], a[36:39]\n"
"v_exp_f32 v126, v126\n"
"v_mfma_f32_16x16x32_bf16 a[20:23], a[184:187], v[76:79], a[20:23]\n"
"buffer_load_dwordx4 v225, s[44:47], s82 offen offset:128 lds\n"
"v_mfma_f32_16x16x32_bf16 a[4:7], a[180:183], v[76:79], a[4:7]\n"
"v_exp_f32 v127, v127\n"
"v_mfma_f32_16x16x32_bf16 a[136:139], a[144:147], v[84:87], a[136:139]\n"
"ds_read_b128 a[164:167], v222 offset:37408\n"
"v_cvt_pk_bf16_f32 v92, v92, v93\n"
"v_mfma_f32_16x16x32_bf16 a[8:11], a[180:183], v[84:87], a[8:11]\n"
"ds_read_b128 a[168:171], v223 offset:37408\n"
"v_cvt_pk_bf16_f32 v93, v94, v95\n"
"v_mfma_f32_16x16x32_bf16 a[24:27], a[184:187], v[84:87], a[24:27]\n"
"ds_read_b128 a[172:175], v222 offset:45600\n"
"v_cvt_pk_bf16_f32 v94, v124, v125\n"
"v_mfma_f32_16x16x32_bf16 a[40:43], a[188:191], v[84:87], a[40:43]\n"
"ds_read_b128 a[176:179], v223 offset:45600\n"
"v_cvt_pk_bf16_f32 v95, v126, v127\n"
"v_mfma_f32_16x16x32_bf16 a[56:59], a[192:195], v[84:87], a[56:59]\n"
"v_exp_f32 v4, v4\n"
"v_mfma_f32_16x16x32_bf16 a[72:75], a[196:199], v[84:87], a[72:75]\n"
"v_exp_f32 v5, v5\n"
"v_mfma_f32_16x16x32_bf16 a[88:91], a[200:203], v[84:87], a[88:91]\n"
"v_exp_f32 v6, v6\n"
"v_mfma_f32_16x16x32_bf16 a[104:107], a[204:207], v[84:87], a[104:107]\n"
"v_exp_f32 v7, v7\n"
"v_mfma_f32_16x16x32_bf16 a[120:123], a[208:211], v[84:87], a[120:123]\n"
"v_exp_f32 v36, v36\n"
"v_mfma_f32_16x16x32_bf16 a[140:143], a[144:147], v[92:95], a[140:143]\n"
"v_exp_f32 v37, v37\n"
"v_mfma_f32_16x16x32_bf16 a[124:127], a[208:211], v[92:95], a[124:127]\n"
"v_exp_f32 v38, v38\n"
"v_mfma_f32_16x16x32_bf16 a[108:111], a[204:207], v[92:95], a[108:111]\n"
"v_exp_f32 v39, v39\n"
"v_mfma_f32_16x16x32_bf16 a[92:95], a[200:203], v[92:95], a[92:95]\n"
"v_cvt_pk_bf16_f32 v4, v4, v5\n"
"v_cvt_pk_bf16_f32 v5, v6, v7\n"
"v_mfma_f32_16x16x32_bf16 a[76:79], a[196:199], v[92:95], a[76:79]\n"
"v_cvt_pk_bf16_f32 v6, v36, v37\n"
"v_cvt_pk_bf16_f32 v7, v38, v39\n"
"v_mfma_f32_16x16x32_bf16 a[60:63], a[192:195], v[92:95], a[60:63]\n"
"v_exp_f32 v12, v12\n"
"v_mfma_f32_16x16x32_bf16 a[44:47], a[188:191], v[92:95], a[44:47]\n"
"v_exp_f32 v13, v13\n"
"v_mfma_f32_16x16x32_bf16 a[28:31], a[184:187], v[92:95], a[28:31]\n"
"v_exp_f32 v14, v14\n"
"v_mfma_f32_16x16x32_bf16 a[12:15], a[180:183], v[92:95], a[12:15]\n"
"s_waitcnt vmcnt(12) lgkmcnt(0)\n"
"s_barrier\n"
"ds_read_b64_tr_b16 a[180:181], v221 offset:0\n"
"v_mfma_f32_16x16x32_bf16 v[68:71], a[148:151], v[0:3], v[160:163]\n"
"ds_read_b64_tr_b16 a[182:183], v221 offset:256\n"
"v_cvt_pk_bf16_f32 v12, v12, v13\n"
"v_mfma_f32_16x16x32_bf16 v[68:71], a[152:155], v[32:35], v[68:71]\n"
"ds_read_b64_tr_b16 a[184:185], v221 offset:8\n"
"v_mfma_f32_16x16x32_bf16 v[68:71], a[156:159], v[64:67], v[68:71]\n"
"ds_read_b64_tr_b16 a[186:187], v221 offset:264\n"
"v_mfma_f32_16x16x32_bf16 v[68:71], a[160:163], v[96:99], v[68:71]\n"
"ds_read_b64_tr_b16 a[188:189], v221 offset:512\n"
"v_mfma_f32_16x16x32_bf16 v[100:103], a[164:167], v[0:3], v[160:163]\n"
"ds_read_b64_tr_b16 a[190:191], v221 offset:768\n"
"v_mfma_f32_16x16x32_bf16 v[100:103], a[168:171], v[32:35], v[100:103]\n"
"ds_read_b64_tr_b16 a[192:193], v221 offset:520\n"
"v_mfma_f32_16x16x32_bf16 v[100:103], a[172:175], v[64:67], v[100:103]\n"
"ds_read_b64_tr_b16 a[194:195], v221 offset:776\n"
"v_mfma_f32_16x16x32_bf16 v[100:103], a[176:179], v[96:99], v[100:103]\n"
"ds_read_b64_tr_b16 a[196:197], v221 offset:1024\n"
"v_mfma_f32_16x16x32_bf16 v[76:79], a[148:151], v[8:11], v[164:167]\n"
"ds_read_b64_tr_b16 a[198:199], v221 offset:1280\n"
"v_mfma_f32_16x16x32_bf16 v[76:79], a[152:155], v[40:43], v[76:79]\n"
"ds_read_b64_tr_b16 a[200:201], v221 offset:1032\n"
"v_mfma_f32_16x16x32_bf16 v[76:79], a[156:159], v[72:75], v[76:79]\n"
"ds_read_b64_tr_b16 a[202:203], v221 offset:1288\n"
"v_mfma_f32_16x16x32_bf16 v[76:79], a[160:163], v[104:107], v[76:79]\n"
"ds_read_b64_tr_b16 a[204:205], v221 offset:1536\n"
"v_mfma_f32_16x16x32_bf16 v[108:111], a[164:167], v[8:11], v[164:167]\n"
"ds_read_b64_tr_b16 a[206:207], v221 offset:1792\n"
"v_mfma_f32_16x16x32_bf16 v[108:111], a[168:171], v[40:43], v[108:111]\n"
"ds_read_b64_tr_b16 a[208:209], v221 offset:1544\n"
"v_mfma_f32_16x16x32_bf16 v[108:111], a[172:175], v[72:75], v[108:111]\n"
"ds_read_b64_tr_b16 a[210:211], v221 offset:1800\n"
"v_mfma_f32_16x16x32_bf16 v[108:111], a[176:179], v[104:107], v[108:111]\n"
"v_exp_f32 v15, v15\n"
"v_mfma_f32_16x16x32_bf16 v[84:87], a[148:151], v[16:19], v[168:171]\n"
"v_exp_f32 v44, v44\n"
"v_mfma_f32_16x16x32_bf16 v[84:87], a[152:155], v[48:51], v[84:87]\n"
"v_exp_f32 v45, v45\n"
"v_mfma_f32_16x16x32_bf16 v[84:87], a[156:159], v[80:83], v[84:87]\n"
"v_exp_f32 v46, v46\n"
"v_mfma_f32_16x16x32_bf16 v[84:87], a[160:163], v[112:115], v[84:87]\n"
"v_exp_f32 v47, v47\n"
"v_mfma_f32_16x16x32_bf16 v[116:119], a[164:167], v[16:19], v[168:171]\n"
"v_cvt_pk_bf16_f32 v13, v14, v15\n"
"v_cvt_pk_bf16_f32 v14, v44, v45\n"
"v_mfma_f32_16x16x32_bf16 v[116:119], a[168:171], v[48:51], v[116:119]\n"
"v_cvt_pk_bf16_f32 v15, v46, v47\n"
"v_mfma_f32_16x16x32_bf16 v[116:119], a[172:175], v[80:83], v[116:119]\n"
"v_exp_f32 v20, v20\n"
"v_mfma_f32_16x16x32_bf16 v[116:119], a[176:179], v[112:115], v[116:119]\n"
"v_exp_f32 v21, v21\n"
"v_mfma_f32_16x16x32_bf16 v[92:95], a[148:151], v[24:27], v[172:175]\n"
"v_exp_f32 v22, v22\n"
"v_mfma_f32_16x16x32_bf16 v[92:95], a[152:155], v[56:59], v[92:95]\n"
"v_exp_f32 v23, v23\n"
"v_mfma_f32_16x16x32_bf16 v[92:95], a[156:159], v[88:91], v[92:95]\n"
"v_exp_f32 v52, v52\n"
"v_mfma_f32_16x16x32_bf16 v[92:95], a[160:163], v[120:123], v[92:95]\n"
"v_exp_f32 v53, v53\n"
"v_mfma_f32_16x16x32_bf16 v[124:127], a[164:167], v[24:27], v[172:175]\n"
"v_exp_f32 v54, v54\n"
"v_mfma_f32_16x16x32_bf16 v[124:127], a[168:171], v[56:59], v[124:127]\n"
"v_exp_f32 v55, v55\n"
"v_mfma_f32_16x16x32_bf16 v[124:127], a[172:175], v[88:91], v[124:127]\n"
"v_cvt_pk_bf16_f32 v20, v20, v21\n"
"v_cvt_pk_bf16_f32 v21, v22, v23\n"
"v_mfma_f32_16x16x32_bf16 v[124:127], a[176:179], v[120:123], v[124:127]\n"
"s_add_u32 m0, s77, 131200\n"
"ds_read_b128 a[148:151], v222 offset:0\n"
"v_cvt_pk_bf16_f32 v22, v52, v53\n"
"v_mfma_f32_16x16x32_bf16 a[128:131], a[144:147], v[4:7], a[128:131]\n"
"s_waitcnt lgkmcnt(1)\n"
"ds_read_b128 a[152:155], v223 offset:0\n"
"v_cvt_pk_bf16_f32 v23, v54, v55\n"
"v_mfma_f32_16x16x32_bf16 a[0:3], a[180:183], v[4:7], a[0:3]\n"
"s_addk_i32 s82, 57344\n"
"buffer_load_dwordx4 v226, s[48:51], s82 offen lds\n"
"v_mfma_f32_16x16x32_bf16 a[16:19], a[184:187], v[4:7], a[16:19]\n"
"s_addk_i32 m0, 896\n"
"v_exp_f32 v28, v28\n"
"v_mfma_f32_16x16x32_bf16 a[32:35], a[188:191], v[4:7], a[32:35]\n"
"v_exp_f32 v29, v29\n"
"v_mfma_f32_16x16x32_bf16 a[48:51], a[192:195], v[4:7], a[48:51]\n"
"v_exp_f32 v30, v30\n"
"v_mfma_f32_16x16x32_bf16 a[64:67], a[196:199], v[4:7], a[64:67]\n"
"v_exp_f32 v31, v31\n"
"v_mfma_f32_16x16x32_bf16 a[80:83], a[200:203], v[4:7], a[80:83]\n"
"buffer_load_dwordx4 v226, s[48:51], s82 offen offset:128 lds\n"
"v_mfma_f32_16x16x32_bf16 a[96:99], a[204:207], v[4:7], a[96:99]\n"
"s_addk_i32 m0, 7304\n"
"v_exp_f32 v60, v60\n"
"v_mfma_f32_16x16x32_bf16 a[112:115], a[208:211], v[4:7], a[112:115]\n"
"s_addk_i32 s82, 2048\n"
"v_exp_f32 v61, v61\n"
"v_mfma_f32_16x16x32_bf16 a[132:135], a[144:147], v[12:15], a[132:135]\n"
"v_exp_f32 v62, v62\n"
"v_mfma_f32_16x16x32_bf16 a[116:119], a[208:211], v[12:15], a[116:119]\n"
"v_exp_f32 v63, v63\n"
"v_mfma_f32_16x16x32_bf16 a[100:103], a[204:207], v[12:15], a[100:103]\n"
"buffer_load_dwordx4 v226, s[48:51], s82 offen lds\n"
"v_mfma_f32_16x16x32_bf16 a[84:87], a[200:203], v[12:15], a[84:87]\n"
"s_addk_i32 m0, 896\n"
"ds_read_b128 a[156:159], v222 offset:8192\n"
"v_cvt_pk_bf16_f32 v28, v28, v29\n"
"v_mfma_f32_16x16x32_bf16 a[68:71], a[196:199], v[12:15], a[68:71]\n"
"ds_read_b128 a[160:163], v223 offset:8192\n"
"v_cvt_pk_bf16_f32 v29, v30, v31\n"
"v_mfma_f32_16x16x32_bf16 a[52:55], a[192:195], v[12:15], a[52:55]\n"
"ds_read_b128 a[164:167], v222 offset:512\n"
"v_cvt_pk_bf16_f32 v30, v60, v61\n"
"v_mfma_f32_16x16x32_bf16 a[36:39], a[188:191], v[12:15], a[36:39]\n"
"ds_read_b128 a[168:171], v223 offset:512\n"
"v_cvt_pk_bf16_f32 v31, v62, v63\n"
"v_mfma_f32_16x16x32_bf16 a[20:23], a[184:187], v[12:15], a[20:23]\n"
"buffer_load_dwordx4 v226, s[48:51], s82 offen offset:128 lds\n"
"v_mfma_f32_16x16x32_bf16 a[4:7], a[180:183], v[12:15], a[4:7]\n"
"ds_read_b128 a[172:175], v222 offset:8704\n"
"v_mfma_f32_16x16x32_bf16 a[136:139], a[144:147], v[20:23], a[136:139]\n"
"ds_read_b128 a[176:179], v223 offset:8704\n"
"v_mfma_f32_16x16x32_bf16 a[8:11], a[180:183], v[20:23], a[8:11]\n"
"ds_read_b64_tr_b16 v[176:177], v229 offset:0\n"
"v_mfma_f32_16x16x32_bf16 a[24:27], a[184:187], v[20:23], a[24:27]\n"
"ds_read_b64_tr_b16 v[178:179], v229 offset:256\n"
"v_mfma_f32_16x16x32_bf16 a[40:43], a[188:191], v[20:23], a[40:43]\n"
"ds_read_b64_tr_b16 v[180:181], v229 offset:8\n"
"v_mfma_f32_16x16x32_bf16 a[56:59], a[192:195], v[20:23], a[56:59]\n"
"ds_read_b64_tr_b16 v[182:183], v229 offset:264\n"
"v_mfma_f32_16x16x32_bf16 a[72:75], a[196:199], v[20:23], a[72:75]\n"
"ds_read_b64_tr_b16 v[184:185], v229 offset:512\n"
"v_mfma_f32_16x16x32_bf16 a[88:91], a[200:203], v[20:23], a[88:91]\n"
"ds_read_b64_tr_b16 v[186:187], v229 offset:768\n"
"v_mfma_f32_16x16x32_bf16 a[104:107], a[204:207], v[20:23], a[104:107]\n"
"ds_read_b64_tr_b16 v[188:189], v229 offset:520\n"
"v_mfma_f32_16x16x32_bf16 a[120:123], a[208:211], v[20:23], a[120:123]\n"
"ds_read_b64_tr_b16 v[190:191], v229 offset:776\n"
"v_mfma_f32_16x16x32_bf16 a[140:143], a[144:147], v[28:31], a[140:143]\n"
"v_exp_f32 v68, v68\n"
"v_mfma_f32_16x16x32_bf16 a[124:127], a[208:211], v[28:31], a[124:127]\n"
"v_exp_f32 v69, v69\n"
"v_mfma_f32_16x16x32_bf16 a[108:111], a[204:207], v[28:31], a[108:111]\n"
"v_exp_f32 v70, v70\n"
"v_mfma_f32_16x16x32_bf16 a[92:95], a[200:203], v[28:31], a[92:95]\n"
"v_exp_f32 v71, v71\n"
"v_mfma_f32_16x16x32_bf16 a[76:79], a[196:199], v[28:31], a[76:79]\n"
"v_exp_f32 v100, v100\n"
"v_mfma_f32_16x16x32_bf16 a[60:63], a[192:195], v[28:31], a[60:63]\n"
"v_exp_f32 v101, v101\n"
"v_mfma_f32_16x16x32_bf16 a[44:47], a[188:191], v[28:31], a[44:47]\n"
"v_exp_f32 v102, v102\n"
"v_mfma_f32_16x16x32_bf16 a[28:31], a[184:187], v[28:31], a[28:31]\n"
"v_exp_f32 v103, v103\n"
"v_mfma_f32_16x16x32_bf16 a[12:15], a[180:183], v[28:31], a[12:15]\n"
"s_waitcnt lgkmcnt(3)\n"
"buffer_store_dwordx4 v[176:179], v216, s[60:63], s16 offen sc1\n"
"v_mfma_f32_16x16x32_bf16 v[4:7], a[148:151], v[0:3], v[160:163]\n"
"ds_read_b64_tr_b16 a[180:181], v221 offset:4096\n"
"v_cvt_pk_bf16_f32 v68, v68, v69\n"
"v_mfma_f32_16x16x32_bf16 v[4:7], a[152:155], v[32:35], v[4:7]\n"
"ds_read_b64_tr_b16 a[182:183], v221 offset:4352\n"
"v_cvt_pk_bf16_f32 v69, v70, v71\n"
"v_mfma_f32_16x16x32_bf16 v[4:7], a[156:159], v[64:67], v[4:7]\n"
"ds_read_b64_tr_b16 a[184:185], v221 offset:4104\n"
"v_cvt_pk_bf16_f32 v70, v100, v101\n"
"v_mfma_f32_16x16x32_bf16 v[4:7], a[160:163], v[96:99], v[4:7]\n"
"ds_read_b64_tr_b16 a[186:187], v221 offset:4360\n"
"v_cvt_pk_bf16_f32 v71, v102, v103\n"
"v_mfma_f32_16x16x32_bf16 v[36:39], a[164:167], v[0:3], v[160:163]\n"
"ds_read_b64_tr_b16 a[188:189], v221 offset:4608\n"
"v_mfma_f32_16x16x32_bf16 v[36:39], a[168:171], v[32:35], v[36:39]\n"
"ds_read_b64_tr_b16 a[190:191], v221 offset:4864\n"
"v_mfma_f32_16x16x32_bf16 v[36:39], a[172:175], v[64:67], v[36:39]\n"
"ds_read_b64_tr_b16 a[192:193], v221 offset:4616\n"
"v_mfma_f32_16x16x32_bf16 v[36:39], a[176:179], v[96:99], v[36:39]\n"
"ds_read_b64_tr_b16 a[194:195], v221 offset:4872\n"
"v_mfma_f32_16x16x32_bf16 v[12:15], a[148:151], v[8:11], v[164:167]\n"
"ds_read_b64_tr_b16 a[196:197], v221 offset:5120\n"
"v_mfma_f32_16x16x32_bf16 v[12:15], a[152:155], v[40:43], v[12:15]\n"
"ds_read_b64_tr_b16 a[198:199], v221 offset:5376\n"
"v_mfma_f32_16x16x32_bf16 v[12:15], a[156:159], v[72:75], v[12:15]\n"
"ds_read_b64_tr_b16 a[200:201], v221 offset:5128\n"
"v_mfma_f32_16x16x32_bf16 v[12:15], a[160:163], v[104:107], v[12:15]\n"
"ds_read_b64_tr_b16 a[202:203], v221 offset:5384\n"
"v_mfma_f32_16x16x32_bf16 v[44:47], a[164:167], v[8:11], v[164:167]\n"
"ds_read_b64_tr_b16 a[204:205], v221 offset:5632\n"
"v_mfma_f32_16x16x32_bf16 v[44:47], a[168:171], v[40:43], v[44:47]\n"
"ds_read_b64_tr_b16 a[206:207], v221 offset:5888\n"
"v_mfma_f32_16x16x32_bf16 v[44:47], a[172:175], v[72:75], v[44:47]\n"
"ds_read_b64_tr_b16 a[208:209], v221 offset:5640\n"
"v_mfma_f32_16x16x32_bf16 v[44:47], a[176:179], v[104:107], v[44:47]\n"
"ds_read_b64_tr_b16 a[210:211], v221 offset:5896\n"
"v_mfma_f32_16x16x32_bf16 v[20:23], a[148:151], v[16:19], v[168:171]\n"
"v_exp_f32 v76, v76\n"
"v_mfma_f32_16x16x32_bf16 v[20:23], a[152:155], v[48:51], v[20:23]\n"
"v_exp_f32 v77, v77\n"
"v_mfma_f32_16x16x32_bf16 v[20:23], a[156:159], v[80:83], v[20:23]\n"
"v_exp_f32 v78, v78\n"
"v_mfma_f32_16x16x32_bf16 v[20:23], a[160:163], v[112:115], v[20:23]\n"
"v_exp_f32 v79, v79\n"
"v_mfma_f32_16x16x32_bf16 v[52:55], a[164:167], v[16:19], v[168:171]\n"
"v_exp_f32 v108, v108\n"
"v_mfma_f32_16x16x32_bf16 v[52:55], a[168:171], v[48:51], v[52:55]\n"
"v_exp_f32 v109, v109\n"
"v_mfma_f32_16x16x32_bf16 v[52:55], a[172:175], v[80:83], v[52:55]\n"
"v_exp_f32 v110, v110\n"
"v_mfma_f32_16x16x32_bf16 v[52:55], a[176:179], v[112:115], v[52:55]\n"
"buffer_store_dwordx4 v[180:183], v216, s[60:63], s17 offen sc1\n"
"v_mfma_f32_16x16x32_bf16 v[28:31], a[148:151], v[24:27], v[172:175]\n"
"v_exp_f32 v111, v111\n"
"v_mfma_f32_16x16x32_bf16 v[28:31], a[152:155], v[56:59], v[28:31]\n"
"v_cvt_pk_bf16_f32 v76, v76, v77\n"
"v_cvt_pk_bf16_f32 v77, v78, v79\n"
"v_mfma_f32_16x16x32_bf16 v[28:31], a[156:159], v[88:91], v[28:31]\n"
"v_cvt_pk_bf16_f32 v78, v108, v109\n"
"v_cvt_pk_bf16_f32 v79, v110, v111\n"
"v_mfma_f32_16x16x32_bf16 v[28:31], a[160:163], v[120:123], v[28:31]\n"
"v_exp_f32 v84, v84\n"
"v_mfma_f32_16x16x32_bf16 v[60:63], a[164:167], v[24:27], v[172:175]\n"
"v_exp_f32 v85, v85\n"
"v_mfma_f32_16x16x32_bf16 v[60:63], a[168:171], v[56:59], v[60:63]\n"
"v_exp_f32 v86, v86\n"
"v_mfma_f32_16x16x32_bf16 v[60:63], a[172:175], v[88:91], v[60:63]\n"
"v_exp_f32 v87, v87\n"
"v_mfma_f32_16x16x32_bf16 v[60:63], a[176:179], v[120:123], v[60:63]\n"
"s_add_u32 m0, s76, 32800\n"
"v_exp_f32 v116, v116\n"
"v_mfma_f32_16x16x32_bf16 a[128:131], a[144:147], v[68:71], a[128:131]\n"
"s_waitcnt lgkmcnt(0)\n"
"v_exp_f32 v117, v117\n"
"v_mfma_f32_16x16x32_bf16 a[0:3], a[180:183], v[68:71], a[0:3]\n"
"s_addk_i32 s82, 14336\n"
"buffer_load_dwordx4 v225, s[44:47], s82 offen lds\n"
"v_mfma_f32_16x16x32_bf16 a[16:19], a[184:187], v[68:71], a[16:19]\n"
"s_addk_i32 m0, 8064\n"
"v_exp_f32 v118, v118\n"
"v_mfma_f32_16x16x32_bf16 a[32:35], a[188:191], v[68:71], a[32:35]\n"
"v_exp_f32 v119, v119\n"
"v_mfma_f32_16x16x32_bf16 a[48:51], a[192:195], v[68:71], a[48:51]\n"
"L_st6_%=:\n"
"s_cbranch_execz L_st10_%=\n"
"ds_read_b128 a[148:151], v222 offset:4096\n"
"v_cvt_pk_bf16_f32 v84, v84, v85\n"
"v_mfma_f32_16x16x32_bf16 a[64:67], a[196:199], v[68:71], a[64:67]\n"
"ds_read_b128 a[152:155], v223 offset:4096\n"
"v_cvt_pk_bf16_f32 v85, v86, v87\n"
"v_mfma_f32_16x16x32_bf16 a[80:83], a[200:203], v[68:71], a[80:83]\n"
"buffer_load_dwordx4 v225, s[44:47], s82 offen offset:128 lds\n"
"v_mfma_f32_16x16x32_bf16 a[96:99], a[204:207], v[68:71], a[96:99]\n"
"s_addk_i32 m0, 61568\n"
"ds_read_b128 a[156:159], v222 offset:12288\n"
"v_cvt_pk_bf16_f32 v86, v116, v117\n"
"v_mfma_f32_16x16x32_bf16 a[112:115], a[208:211], v[68:71], a[112:115]\n"
"s_addk_i32 s82, 8192\n"
"ds_read_b128 a[160:163], v223 offset:12288\n"
"v_cvt_pk_bf16_f32 v87, v118, v119\n"
"v_mfma_f32_16x16x32_bf16 a[132:135], a[144:147], v[76:79], a[132:135]\n"
"v_exp_f32 v92, v92\n"
"v_mfma_f32_16x16x32_bf16 a[116:119], a[208:211], v[76:79], a[116:119]\n"
"v_exp_f32 v93, v93\n"
"v_mfma_f32_16x16x32_bf16 a[100:103], a[204:207], v[76:79], a[100:103]\n"
"buffer_load_dwordx4 v225, s[44:47], s82 offen lds\n"
"v_mfma_f32_16x16x32_bf16 a[84:87], a[200:203], v[76:79], a[84:87]\n"
"s_addk_i32 m0, 8064\n"
"v_exp_f32 v94, v94\n"
"v_mfma_f32_16x16x32_bf16 a[68:71], a[196:199], v[76:79], a[68:71]\n"
"v_exp_f32 v95, v95\n"
"v_mfma_f32_16x16x32_bf16 a[52:55], a[192:195], v[76:79], a[52:55]\n"
"v_exp_f32 v124, v124\n"
"v_mfma_f32_16x16x32_bf16 a[36:39], a[188:191], v[76:79], a[36:39]\n"
"v_exp_f32 v125, v125\n"
"v_mfma_f32_16x16x32_bf16 a[20:23], a[184:187], v[76:79], a[20:23]\n"
"buffer_store_dwordx4 v[184:187], v216, s[60:63], s18 offen sc1\n"
"v_mfma_f32_16x16x32_bf16 a[4:7], a[180:183], v[76:79], a[4:7]\n"
"v_exp_f32 v126, v126\n"
"v_mfma_f32_16x16x32_bf16 a[136:139], a[144:147], v[84:87], a[136:139]\n"
"v_exp_f32 v127, v127\n"
"v_mfma_f32_16x16x32_bf16 a[8:11], a[180:183], v[84:87], a[8:11]\n"
"ds_read_b128 a[164:167], v222 offset:4608\n"
"v_cvt_pk_bf16_f32 v92, v92, v93\n"
"v_mfma_f32_16x16x32_bf16 a[24:27], a[184:187], v[84:87], a[24:27]\n"
"ds_read_b128 a[168:171], v223 offset:4608\n"
"v_cvt_pk_bf16_f32 v93, v94, v95\n"
"v_mfma_f32_16x16x32_bf16 a[40:43], a[188:191], v[84:87], a[40:43]\n"
"buffer_load_dwordx4 v225, s[44:47], s82 offen offset:128 lds\n"
"v_mfma_f32_16x16x32_bf16 a[56:59], a[192:195], v[84:87], a[56:59]\n"
"ds_read_b128 a[172:175], v222 offset:12800\n"
"v_cvt_pk_bf16_f32 v94, v124, v125\n"
"v_mfma_f32_16x16x32_bf16 a[72:75], a[196:199], v[84:87], a[72:75]\n"
"ds_read_b128 a[176:179], v223 offset:12800\n"
"v_cvt_pk_bf16_f32 v95, v126, v127\n"
"v_mfma_f32_16x16x32_bf16 a[88:91], a[200:203], v[84:87], a[88:91]\n"
"ds_read_b64_tr_b16 v[192:193], v229 offset:16400\n"
"v_mfma_f32_16x16x32_bf16 a[104:107], a[204:207], v[84:87], a[104:107]\n"
"ds_read_b64_tr_b16 v[194:195], v229 offset:16656\n"
"v_mfma_f32_16x16x32_bf16 a[120:123], a[208:211], v[84:87], a[120:123]\n"
"ds_read_b64_tr_b16 v[196:197], v229 offset:16408\n"
"v_mfma_f32_16x16x32_bf16 a[140:143], a[144:147], v[92:95], a[140:143]\n"
"ds_read_b64_tr_b16 v[198:199], v229 offset:16664\n"
"v_mfma_f32_16x16x32_bf16 a[124:127], a[208:211], v[92:95], a[124:127]\n"
"ds_read_b64_tr_b16 v[200:201], v229 offset:16912\n"
"v_mfma_f32_16x16x32_bf16 a[108:111], a[204:207], v[92:95], a[108:111]\n"
"ds_read_b64_tr_b16 v[202:203], v229 offset:17168\n"
"v_mfma_f32_16x16x32_bf16 a[92:95], a[200:203], v[92:95], a[92:95]\n"
"ds_read_b64_tr_b16 v[204:205], v229 offset:16920\n"
"v_mfma_f32_16x16x32_bf16 a[76:79], a[196:199], v[92:95], a[76:79]\n"
"ds_read_b64_tr_b16 v[206:207], v229 offset:17176\n"
"v_mfma_f32_16x16x32_bf16 a[60:63], a[192:195], v[92:95], a[60:63]\n"
"v_exp_f32 v4, v4\n"
"v_mfma_f32_16x16x32_bf16 a[44:47], a[188:191], v[92:95], a[44:47]\n"
"v_exp_f32 v5, v5\n"
"v_mfma_f32_16x16x32_bf16 a[28:31], a[184:187], v[92:95], a[28:31]\n"
"v_exp_f32 v6, v6\n"
"v_mfma_f32_16x16x32_bf16 a[12:15], a[180:183], v[92:95], a[12:15]\n"
"s_waitcnt lgkmcnt(8)\n"
"v_exp_f32 v7, v7\n"
"v_mfma_f32_16x16x32_bf16 v[68:71], a[148:151], v[0:3], v[160:163]\n"
"v_exp_f32 v36, v36\n"
"v_mfma_f32_16x16x32_bf16 v[68:71], a[152:155], v[32:35], v[68:71]\n"
"v_exp_f32 v37, v37\n"
"v_mfma_f32_16x16x32_bf16 v[68:71], a[156:159], v[64:67], v[68:71]\n"
"v_exp_f32 v38, v38\n"
"v_mfma_f32_16x16x32_bf16 v[68:71], a[160:163], v[96:99], v[68:71]\n"
"buffer_store_dwordx4 v[188:191], v216, s[60:63], s19 offen sc1\n"
"v_mfma_f32_16x16x32_bf16 v[100:103], a[164:167], v[0:3], v[160:163]\n"
"s_waitcnt vmcnt(12) lgkmcnt(0)\n"
"s_barrier\n"
"ds_read_b64_tr_b16 a[180:181], v221 offset:16400\n"
"v_mfma_f32_16x16x32_bf16 v[100:103], a[168:171], v[32:35], v[100:103]\n"
"ds_read_b64_tr_b16 a[182:183], v221 offset:16656\n"
"v_cvt_pk_bf16_f32 v4, v4, v5\n"
"v_mfma_f32_16x16x32_bf16 v[100:103], a[172:175], v[64:67], v[100:103]\n"
"ds_read_b64_tr_b16 a[184:185], v221 offset:16408\n"
"v_cvt_pk_bf16_f32 v5, v6, v7\n"
"v_mfma_f32_16x16x32_bf16 v[100:103], a[176:179], v[96:99], v[100:103]\n"
"ds_read_b64_tr_b16 a[186:187], v221 offset:16664\n"
"v_cvt_pk_bf16_f32 v6, v36, v37\n"
"v_mfma_f32_16x16x32_bf16 v[76:79], a[148:151], v[8:11], v[164:167]\n"
"ds_read_b64_tr_b16 a[188:189], v221 offset:16912\n"
"v_mfma_f32_16x16x32_bf16 v[76:79], a[152:155], v[40:43], v[76:79]\n"
"ds_read_b64_tr_b16 a[190:191], v221 offset:17168\n"
"v_mfma_f32_16x16x32_bf16 v[76:79], a[156:159], v[72:75], v[76:79]\n"
"ds_read_b64_tr_b16 a[192:193], v221 offset:16920\n"
"v_mfma_f32_16x16x32_bf16 v[76:79], a[160:163], v[104:107], v[76:79]\n"
"ds_read_b64_tr_b16 a[194:195], v221 offset:17176\n"
"v_mfma_f32_16x16x32_bf16 v[108:111], a[164:167], v[8:11], v[164:167]\n"
"ds_read_b64_tr_b16 a[196:197], v221 offset:17424\n"
"v_mfma_f32_16x16x32_bf16 v[108:111], a[168:171], v[40:43], v[108:111]\n"
"ds_read_b64_tr_b16 a[198:199], v221 offset:17680\n"
"v_mfma_f32_16x16x32_bf16 v[108:111], a[172:175], v[72:75], v[108:111]\n"
"v_exp_f32 v39, v39\n"
"v_mfma_f32_16x16x32_bf16 v[108:111], a[176:179], v[104:107], v[108:111]\n"
"ds_read_b64_tr_b16 a[200:201], v221 offset:17432\n"
"v_cvt_pk_bf16_f32 v7, v38, v39\n"
"v_mfma_f32_16x16x32_bf16 v[84:87], a[148:151], v[16:19], v[168:171]\n"
"ds_read_b64_tr_b16 a[202:203], v221 offset:17688\n"
"v_mfma_f32_16x16x32_bf16 v[84:87], a[152:155], v[48:51], v[84:87]\n"
"ds_read_b64_tr_b16 a[204:205], v221 offset:17936\n"
"v_mfma_f32_16x16x32_bf16 v[84:87], a[156:159], v[80:83], v[84:87]\n"
"ds_read_b64_tr_b16 a[206:207], v221 offset:18192\n"
"v_mfma_f32_16x16x32_bf16 v[84:87], a[160:163], v[112:115], v[84:87]\n"
"ds_read_b64_tr_b16 a[208:209], v221 offset:17944\n"
"v_mfma_f32_16x16x32_bf16 v[116:119], a[164:167], v[16:19], v[168:171]\n"
"ds_read_b64_tr_b16 a[210:211], v221 offset:18200\n"
"v_mfma_f32_16x16x32_bf16 v[116:119], a[168:171], v[48:51], v[116:119]\n"
"v_exp_f32 v12, v12\n"
"v_mfma_f32_16x16x32_bf16 v[116:119], a[172:175], v[80:83], v[116:119]\n"
"v_exp_f32 v13, v13\n"
"v_mfma_f32_16x16x32_bf16 v[116:119], a[176:179], v[112:115], v[116:119]\n"
"v_exp_f32 v14, v14\n"
"v_mfma_f32_16x16x32_bf16 v[92:95], a[148:151], v[24:27], v[172:175]\n"
"v_exp_f32 v15, v15\n"
"v_mfma_f32_16x16x32_bf16 v[92:95], a[152:155], v[56:59], v[92:95]\n"
"v_exp_f32 v44, v44\n"
"v_mfma_f32_16x16x32_bf16 v[92:95], a[156:159], v[88:91], v[92:95]\n"
"v_exp_f32 v45, v45\n"
"v_mfma_f32_16x16x32_bf16 v[92:95], a[160:163], v[120:123], v[92:95]\n"
"buffer_store_dwordx4 v[192:195], v216, s[60:63], s20 offen sc1\n"
"v_mfma_f32_16x16x32_bf16 v[124:127], a[164:167], v[24:27], v[172:175]\n"
"v_exp_f32 v46, v46\n"
"v_mfma_f32_16x16x32_bf16 v[124:127], a[168:171], v[56:59], v[124:127]\n"
"v_exp_f32 v47, v47\n"
"v_mfma_f32_16x16x32_bf16 v[124:127], a[172:175], v[88:91], v[124:127]\n"
"v_cvt_pk_bf16_f32 v12, v12, v13\n"
"v_cvt_pk_bf16_f32 v13, v14, v15\n"
"v_mfma_f32_16x16x32_bf16 v[124:127], a[176:179], v[120:123], v[124:127]\n"
"s_add_u32 m0, s77, 98400\n"
"ds_read_b128 a[148:151], v222 offset:16400\n"
"v_cvt_pk_bf16_f32 v14, v44, v45\n"
"v_mfma_f32_16x16x32_bf16 a[128:131], a[144:147], v[4:7], a[128:131]\n"
"s_waitcnt lgkmcnt(1)\n"
"ds_read_b128 a[152:155], v223 offset:16400\n"
"v_cvt_pk_bf16_f32 v15, v46, v47\n"
"v_mfma_f32_16x16x32_bf16 a[0:3], a[180:183], v[4:7], a[0:3]\n"
"s_addk_i32 s82, 57344\n"
"buffer_load_dwordx4 v226, s[48:51], s82 offen lds\n"
"v_mfma_f32_16x16x32_bf16 a[16:19], a[184:187], v[4:7], a[16:19]\n"
"s_addk_i32 m0, 896\n"
"v_exp_f32 v20, v20\n"
"v_mfma_f32_16x16x32_bf16 a[32:35], a[188:191], v[4:7], a[32:35]\n"
"v_exp_f32 v21, v21\n"
"v_mfma_f32_16x16x32_bf16 a[48:51], a[192:195], v[4:7], a[48:51]\n"
"v_exp_f32 v22, v22\n"
"v_mfma_f32_16x16x32_bf16 a[64:67], a[196:199], v[4:7], a[64:67]\n"
"v_exp_f32 v23, v23\n"
"v_mfma_f32_16x16x32_bf16 a[80:83], a[200:203], v[4:7], a[80:83]\n"
"buffer_load_dwordx4 v226, s[48:51], s82 offen offset:128 lds\n"
"v_mfma_f32_16x16x32_bf16 a[96:99], a[204:207], v[4:7], a[96:99]\n"
"s_addk_i32 m0, 7304\n"
"v_exp_f32 v52, v52\n"
"v_mfma_f32_16x16x32_bf16 a[112:115], a[208:211], v[4:7], a[112:115]\n"
"s_addk_i32 s82, 2048\n"
"v_exp_f32 v53, v53\n"
"v_mfma_f32_16x16x32_bf16 a[132:135], a[144:147], v[12:15], a[132:135]\n"
"v_exp_f32 v54, v54\n"
"v_mfma_f32_16x16x32_bf16 a[116:119], a[208:211], v[12:15], a[116:119]\n"
"v_exp_f32 v55, v55\n"
"v_mfma_f32_16x16x32_bf16 a[100:103], a[204:207], v[12:15], a[100:103]\n"
"buffer_load_dwordx4 v226, s[48:51], s82 offen lds\n"
"v_mfma_f32_16x16x32_bf16 a[84:87], a[200:203], v[12:15], a[84:87]\n"
"s_addk_i32 m0, 896\n"
"ds_read_b128 a[156:159], v222 offset:24592\n"
"v_cvt_pk_bf16_f32 v20, v20, v21\n"
"v_mfma_f32_16x16x32_bf16 a[68:71], a[196:199], v[12:15], a[68:71]\n"
"ds_read_b128 a[160:163], v223 offset:24592\n"
"v_cvt_pk_bf16_f32 v21, v22, v23\n"
"v_mfma_f32_16x16x32_bf16 a[52:55], a[192:195], v[12:15], a[52:55]\n"
"ds_read_b128 a[164:167], v222 offset:16912\n"
"v_cvt_pk_bf16_f32 v22, v52, v53\n"
"v_mfma_f32_16x16x32_bf16 a[36:39], a[188:191], v[12:15], a[36:39]\n"
"ds_read_b128 a[168:171], v223 offset:16912\n"
"v_cvt_pk_bf16_f32 v23, v54, v55\n"
"v_mfma_f32_16x16x32_bf16 a[20:23], a[184:187], v[12:15], a[20:23]\n"
"buffer_load_dwordx4 v226, s[48:51], s82 offen offset:128 lds\n"
"v_mfma_f32_16x16x32_bf16 a[4:7], a[180:183], v[12:15], a[4:7]\n"
"v_exp_f32 v28, v28\n"
"v_mfma_f32_16x16x32_bf16 a[136:139], a[144:147], v[20:23], a[136:139]\n"
"v_exp_f32 v29, v29\n"
"v_mfma_f32_16x16x32_bf16 a[8:11], a[180:183], v[20:23], a[8:11]\n"
"v_exp_f32 v30, v30\n"
"v_mfma_f32_16x16x32_bf16 a[24:27], a[184:187], v[20:23], a[24:27]\n"
"v_exp_f32 v31, v31\n"
"v_mfma_f32_16x16x32_bf16 a[40:43], a[188:191], v[20:23], a[40:43]\n"
"v_exp_f32 v60, v60\n"
"v_mfma_f32_16x16x32_bf16 a[56:59], a[192:195], v[20:23], a[56:59]\n"
"v_exp_f32 v61, v61\n"
"v_mfma_f32_16x16x32_bf16 a[72:75], a[196:199], v[20:23], a[72:75]\n"
"buffer_store_dwordx4 v[196:199], v216, s[60:63], s21 offen sc1\n"
"v_exp_f32 v62, v62\n"
"v_exp_f32 v63, v63\n"
"v_mfma_f32_16x16x32_bf16 a[88:91], a[200:203], v[20:23], a[88:91]\n"
"ds_read_b128 a[172:175], v222 offset:25104\n"
"v_cvt_pk_bf16_f32 v28, v28, v29\n"
"v_cvt_pk_bf16_f32 v29, v30, v31\n"
"v_cvt_pk_bf16_f32 v30, v60, v61\n"
"v_cvt_pk_bf16_f32 v31, v62, v63\n"
"v_mfma_f32_16x16x32_bf16 a[104:107], a[204:207], v[20:23], a[104:107]\n"
"ds_read_b128 a[176:179], v223 offset:25104\n"
"v_mfma_f32_16x16x32_bf16 a[120:123], a[208:211], v[20:23], a[120:123]\n"
"v_exp_f32 v68, v68\n"
"v_mfma_f32_16x16x32_bf16 a[140:143], a[144:147], v[28:31], a[140:143]\n"
"v_exp_f32 v69, v69\n"
"v_mfma_f32_16x16x32_bf16 a[124:127], a[208:211], v[28:31], a[124:127]\n"
"v_exp_f32 v70, v70\n"
"v_mfma_f32_16x16x32_bf16 a[108:111], a[204:207], v[28:31], a[108:111]\n"
"v_exp_f32 v71, v71\n"
"v_mfma_f32_16x16x32_bf16 a[92:95], a[200:203], v[28:31], a[92:95]\n"
"v_exp_f32 v100, v100\n"
"v_mfma_f32_16x16x32_bf16 a[76:79], a[196:199], v[28:31], a[76:79]\n"
"v_exp_f32 v101, v101\n"
"v_mfma_f32_16x16x32_bf16 a[60:63], a[192:195], v[28:31], a[60:63]\n"
"v_exp_f32 v102, v102\n"
"v_mfma_f32_16x16x32_bf16 a[44:47], a[188:191], v[28:31], a[44:47]\n"
"v_exp_f32 v103, v103\n"
"v_mfma_f32_16x16x32_bf16 a[28:31], a[184:187], v[28:31], a[28:31]\n"
"v_cvt_pk_bf16_f32 v68, v68, v69\n"
"v_cvt_pk_bf16_f32 v69, v70, v71\n"
"v_mfma_f32_16x16x32_bf16 a[12:15], a[180:183], v[28:31], a[12:15]\n"
"s_waitcnt lgkmcnt(2)\n"
"ds_read_b64_tr_b16 a[180:181], v221 offset:20496\n"
"v_cvt_pk_bf16_f32 v70, v100, v101\n"
"v_mfma_f32_16x16x32_bf16 v[4:7], a[148:151], v[0:3], v[160:163]\n"
"ds_read_b64_tr_b16 a[182:183], v221 offset:20752\n"
"v_cvt_pk_bf16_f32 v71, v102, v103\n"
"v_mfma_f32_16x16x32_bf16 v[4:7], a[152:155], v[32:35], v[4:7]\n"
"ds_read_b64_tr_b16 a[184:185], v221 offset:20504\n"
"v_mfma_f32_16x16x32_bf16 v[4:7], a[156:159], v[64:67], v[4:7]\n"
"ds_read_b64_tr_b16 a[186:187], v221 offset:20760\n"
"v_mfma_f32_16x16x32_bf16 v[4:7], a[160:163], v[96:99], v[4:7]\n"
"ds_read_b64_tr_b16 a[188:189], v221 offset:21008\n"
"v_mfma_f32_16x16x32_bf16 v[36:39], a[164:167], v[0:3], v[160:163]\n"
"ds_read_b64_tr_b16 a[190:191], v221 offset:21264\n"
"v_mfma_f32_16x16x32_bf16 v[36:39], a[168:171], v[32:35], v[36:39]\n"
"s_waitcnt lgkmcnt(6)\n"
"ds_read_b64_tr_b16 a[192:193], v221 offset:21016\n"
"v_mfma_f32_16x16x32_bf16 v[36:39], a[172:175], v[64:67], v[36:39]\n"
"ds_read_b64_tr_b16 a[194:195], v221 offset:21272\n"
"v_mfma_f32_16x16x32_bf16 v[36:39], a[176:179], v[96:99], v[36:39]\n"
"buffer_store_dwordx4 v[200:203], v216, s[60:63], s22 offen sc1\n"
"v_mfma_f32_16x16x32_bf16 v[12:15], a[148:151], v[8:11], v[164:167]\n"
"ds_read_b64_tr_b16 a[196:197], v221 offset:21520\n"
"v_mfma_f32_16x16x32_bf16 v[12:15], a[152:155], v[40:43], v[12:15]\n"
"ds_read_b64_tr_b16 a[198:199], v221 offset:21776\n"
"v_mfma_f32_16x16x32_bf16 v[12:15], a[156:159], v[72:75], v[12:15]\n"
"ds_read_b64_tr_b16 a[200:201], v221 offset:21528\n"
"v_mfma_f32_16x16x32_bf16 v[12:15], a[160:163], v[104:107], v[12:15]\n"
"ds_read_b64_tr_b16 a[202:203], v221 offset:21784\n"
"v_mfma_f32_16x16x32_bf16 v[44:47], a[164:167], v[8:11], v[164:167]\n"
"ds_read_b64_tr_b16 a[204:205], v221 offset:22032\n"
"v_mfma_f32_16x16x32_bf16 v[44:47], a[168:171], v[40:43], v[44:47]\n"
"ds_read_b64_tr_b16 a[206:207], v221 offset:22288\n"
"v_mfma_f32_16x16x32_bf16 v[44:47], a[172:175], v[72:75], v[44:47]\n"
"ds_read_b64_tr_b16 a[208:209], v221 offset:22040\n"
"v_mfma_f32_16x16x32_bf16 v[44:47], a[176:179], v[104:107], v[44:47]\n"
"ds_read_b64_tr_b16 a[210:211], v221 offset:22296\n"
"v_mfma_f32_16x16x32_bf16 v[20:23], a[148:151], v[16:19], v[168:171]\n"
"v_exp_f32 v76, v76\n"
"v_mfma_f32_16x16x32_bf16 v[20:23], a[152:155], v[48:51], v[20:23]\n"
"v_exp_f32 v77, v77\n"
"v_mfma_f32_16x16x32_bf16 v[20:23], a[156:159], v[80:83], v[20:23]\n"
"v_exp_f32 v78, v78\n"
"v_mfma_f32_16x16x32_bf16 v[20:23], a[160:163], v[112:115], v[20:23]\n"
"v_exp_f32 v79, v79\n"
"v_mfma_f32_16x16x32_bf16 v[52:55], a[164:167], v[16:19], v[168:171]\n"
"v_exp_f32 v108, v108\n"
"v_mfma_f32_16x16x32_bf16 v[52:55], a[168:171], v[48:51], v[52:55]\n"
"v_exp_f32 v109, v109\n"
"v_mfma_f32_16x16x32_bf16 v[52:55], a[172:175], v[80:83], v[52:55]\n"
"v_exp_f32 v110, v110\n"
"v_mfma_f32_16x16x32_bf16 v[52:55], a[176:179], v[112:115], v[52:55]\n"
"v_exp_f32 v111, v111\n"
"v_mfma_f32_16x16x32_bf16 v[28:31], a[148:151], v[24:27], v[172:175]\n"
"v_cvt_pk_bf16_f32 v76, v76, v77\n"
"v_cvt_pk_bf16_f32 v77, v78, v79\n"
"v_mfma_f32_16x16x32_bf16 v[28:31], a[152:155], v[56:59], v[28:31]\n"
"v_cvt_pk_bf16_f32 v78, v108, v109\n"
"v_cvt_pk_bf16_f32 v79, v110, v111\n"
"v_mfma_f32_16x16x32_bf16 v[28:31], a[156:159], v[88:91], v[28:31]\n"
"v_exp_f32 v84, v84\n"
"v_mfma_f32_16x16x32_bf16 v[28:31], a[160:163], v[120:123], v[28:31]\n"
"v_exp_f32 v85, v85\n"
"v_mfma_f32_16x16x32_bf16 v[60:63], a[164:167], v[24:27], v[172:175]\n"
"v_exp_f32 v86, v86\n"
"v_mfma_f32_16x16x32_bf16 v[60:63], a[168:171], v[56:59], v[60:63]\n"
"v_exp_f32 v87, v87\n"
"v_mfma_f32_16x16x32_bf16 v[60:63], a[172:175], v[88:91], v[60:63]\n"
"v_exp_f32 v116, v116\n"
"v_mfma_f32_16x16x32_bf16 v[60:63], a[176:179], v[120:123], v[60:63]\n"
"s_add_u32 m0, s76, 0\n"
"buffer_store_dwordx4 v[204:207], v216, s[60:63], s23 offen sc1\n"
"v_mfma_f32_16x16x32_bf16 a[128:131], a[144:147], v[68:71], a[128:131]\n"
"s_waitcnt lgkmcnt(0)\n"
"v_exp_f32 v117, v117\n"
"v_mfma_f32_16x16x32_bf16 a[0:3], a[180:183], v[68:71], a[0:3]\n"
"s_addk_i32 s82, 14336\n"
"v_exp_f32 v118, v118\n"
"v_mfma_f32_16x16x32_bf16 a[16:19], a[184:187], v[68:71], a[16:19]\n"
"v_exp_f32 v119, v119\n"
"v_mfma_f32_16x16x32_bf16 a[32:35], a[188:191], v[68:71], a[32:35]\n"
"ds_read_b128 a[148:151], v222 offset:20496\n"
"v_cvt_pk_bf16_f32 v84, v84, v85\n"
"v_mfma_f32_16x16x32_bf16 a[48:51], a[192:195], v[68:71], a[48:51]\n"
"buffer_load_dwordx4 v225, s[44:47], s82 offen lds\n"
"v_mfma_f32_16x16x32_bf16 a[64:67], a[196:199], v[68:71], a[64:67]\n"
"s_addk_i32 m0, 8064\n"
"ds_read_b128 a[152:155], v223 offset:20496\n"
"v_cvt_pk_bf16_f32 v85, v86, v87\n"
"v_mfma_f32_16x16x32_bf16 a[80:83], a[200:203], v[68:71], a[80:83]\n"
"ds_read_b128 a[156:159], v222 offset:28688\n"
"v_cvt_pk_bf16_f32 v86, v116, v117\n"
"v_mfma_f32_16x16x32_bf16 a[96:99], a[204:207], v[68:71], a[96:99]\n"
"ds_read_b128 a[160:163], v223 offset:28688\n"
"v_cvt_pk_bf16_f32 v87, v118, v119\n"
"v_mfma_f32_16x16x32_bf16 a[112:115], a[208:211], v[68:71], a[112:115]\n"
"v_exp_f32 v92, v92\n"
"v_mfma_f32_16x16x32_bf16 a[132:135], a[144:147], v[76:79], a[132:135]\n"
"buffer_load_dwordx4 v225, s[44:47], s82 offen offset:128 lds\n"
"v_mfma_f32_16x16x32_bf16 a[116:119], a[208:211], v[76:79], a[116:119]\n"
"s_addk_i32 m0, 61568\n"
"v_exp_f32 v93, v93\n"
"v_mfma_f32_16x16x32_bf16 a[100:103], a[204:207], v[76:79], a[100:103]\n"
"s_addk_i32 s82, 8192\n"
"v_exp_f32 v94, v94\n"
"v_mfma_f32_16x16x32_bf16 a[84:87], a[200:203], v[76:79], a[84:87]\n"
"v_exp_f32 v95, v95\n"
"v_mfma_f32_16x16x32_bf16 a[68:71], a[196:199], v[76:79], a[68:71]\n"
"v_exp_f32 v124, v124\n"
"v_mfma_f32_16x16x32_bf16 a[52:55], a[192:195], v[76:79], a[52:55]\n"
"buffer_load_dwordx4 v225, s[44:47], s82 offen lds\n"
"v_mfma_f32_16x16x32_bf16 a[36:39], a[188:191], v[76:79], a[36:39]\n"
"s_addk_i32 m0, 8064\n"
"v_exp_f32 v125, v125\n"
"v_mfma_f32_16x16x32_bf16 a[20:23], a[184:187], v[76:79], a[20:23]\n"
"v_exp_f32 v126, v126\n"
"v_mfma_f32_16x16x32_bf16 a[4:7], a[180:183], v[76:79], a[4:7]\n"
"v_exp_f32 v127, v127\n"
"v_mfma_f32_16x16x32_bf16 a[136:139], a[144:147], v[84:87], a[136:139]\n"
"ds_read_b128 a[164:167], v222 offset:21008\n"
"v_cvt_pk_bf16_f32 v92, v92, v93\n"
"v_mfma_f32_16x16x32_bf16 a[8:11], a[180:183], v[84:87], a[8:11]\n"
"buffer_load_dwordx4 v225, s[44:47], s82 offen offset:128 lds\n"
"v_mfma_f32_16x16x32_bf16 a[24:27], a[184:187], v[84:87], a[24:27]\n"
"ds_read_b128 a[168:171], v223 offset:21008\n"
"v_cvt_pk_bf16_f32 v93, v94, v95\n"
"v_mfma_f32_16x16x32_bf16 a[40:43], a[188:191], v[84:87], a[40:43]\n"
"ds_read_b128 a[172:175], v222 offset:29200\n"
"v_cvt_pk_bf16_f32 v94, v124, v125\n"
"v_mfma_f32_16x16x32_bf16 a[56:59], a[192:195], v[84:87], a[56:59]\n"
"ds_read_b128 a[176:179], v223 offset:29200\n"
"v_cvt_pk_bf16_f32 v95, v126, v127\n"
"v_mfma_f32_16x16x32_bf16 a[72:75], a[196:199], v[84:87], a[72:75]\n"
"v_exp_f32 v4, v4\n"
"v_mfma_f32_16x16x32_bf16 a[88:91], a[200:203], v[84:87], a[88:91]\n"
"v_exp_f32 v5, v5\n"
"v_mfma_f32_16x16x32_bf16 a[104:107], a[204:207], v[84:87], a[104:107]\n"
"v_exp_f32 v6, v6\n"
"v_mfma_f32_16x16x32_bf16 a[120:123], a[208:211], v[84:87], a[120:123]\n"
"v_exp_f32 v7, v7\n"
"v_mfma_f32_16x16x32_bf16 a[140:143], a[144:147], v[92:95], a[140:143]\n"
"v_exp_f32 v36, v36\n"
"v_mfma_f32_16x16x32_bf16 a[124:127], a[208:211], v[92:95], a[124:127]\n"
"v_exp_f32 v37, v37\n"
"v_mfma_f32_16x16x32_bf16 a[108:111], a[204:207], v[92:95], a[108:111]\n"
"v_exp_f32 v38, v38\n"
"v_mfma_f32_16x16x32_bf16 a[92:95], a[200:203], v[92:95], a[92:95]\n"
"v_exp_f32 v39, v39\n"
"v_mfma_f32_16x16x32_bf16 a[76:79], a[196:199], v[92:95], a[76:79]\n"
"v_cvt_pk_bf16_f32 v4, v4, v5\n"
"v_cvt_pk_bf16_f32 v5, v6, v7\n"
"v_mfma_f32_16x16x32_bf16 a[60:63], a[192:195], v[92:95], a[60:63]\n"
"v_cvt_pk_bf16_f32 v6, v36, v37\n"
"v_cvt_pk_bf16_f32 v7, v38, v39\n"
"v_mfma_f32_16x16x32_bf16 a[44:47], a[188:191], v[92:95], a[44:47]\n"
"v_exp_f32 v12, v12\n"
"v_mfma_f32_16x16x32_bf16 a[28:31], a[184:187], v[92:95], a[28:31]\n"
"v_exp_f32 v13, v13\n"
"v_mfma_f32_16x16x32_bf16 a[12:15], a[180:183], v[92:95], a[12:15]\n"
"s_waitcnt vmcnt(13) lgkmcnt(0)\n"
"s_barrier\n"
"ds_read_b64_tr_b16 a[180:181], v221 offset:32800\n"
"v_mfma_f32_16x16x32_bf16 v[68:71], a[148:151], v[0:3], v[160:163]\n"
"ds_read_b64_tr_b16 a[182:183], v221 offset:33056\n"
"v_cvt_pk_bf16_f32 v12, v12, v13\n"
"v_mfma_f32_16x16x32_bf16 v[68:71], a[152:155], v[32:35], v[68:71]\n"
"ds_read_b64_tr_b16 a[184:185], v221 offset:32808\n"
"v_mfma_f32_16x16x32_bf16 v[68:71], a[156:159], v[64:67], v[68:71]\n"
"ds_read_b64_tr_b16 a[186:187], v221 offset:33064\n"
"v_mfma_f32_16x16x32_bf16 v[68:71], a[160:163], v[96:99], v[68:71]\n"
"ds_read_b64_tr_b16 a[188:189], v221 offset:33312\n"
"v_mfma_f32_16x16x32_bf16 v[100:103], a[164:167], v[0:3], v[160:163]\n"
"ds_read_b64_tr_b16 a[190:191], v221 offset:33568\n"
"v_mfma_f32_16x16x32_bf16 v[100:103], a[168:171], v[32:35], v[100:103]\n"
"ds_read_b64_tr_b16 a[192:193], v221 offset:33320\n"
"v_mfma_f32_16x16x32_bf16 v[100:103], a[172:175], v[64:67], v[100:103]\n"
"ds_read_b64_tr_b16 a[194:195], v221 offset:33576\n"
"v_mfma_f32_16x16x32_bf16 v[100:103], a[176:179], v[96:99], v[100:103]\n"
"ds_read_b64_tr_b16 a[196:197], v221 offset:33824\n"
"v_mfma_f32_16x16x32_bf16 v[76:79], a[148:151], v[8:11], v[164:167]\n"
"ds_read_b64_tr_b16 a[198:199], v221 offset:34080\n"
"v_mfma_f32_16x16x32_bf16 v[76:79], a[152:155], v[40:43], v[76:79]\n"
"ds_read_b64_tr_b16 a[200:201], v221 offset:33832\n"
"v_mfma_f32_16x16x32_bf16 v[76:79], a[156:159], v[72:75], v[76:79]\n"
"ds_read_b64_tr_b16 a[202:203], v221 offset:34088\n"
"v_mfma_f32_16x16x32_bf16 v[76:79], a[160:163], v[104:107], v[76:79]\n"
"ds_read_b64_tr_b16 a[204:205], v221 offset:34336\n"
"v_mfma_f32_16x16x32_bf16 v[108:111], a[164:167], v[8:11], v[164:167]\n"
"ds_read_b64_tr_b16 a[206:207], v221 offset:34592\n"
"v_mfma_f32_16x16x32_bf16 v[108:111], a[168:171], v[40:43], v[108:111]\n"
"ds_read_b64_tr_b16 a[208:209], v221 offset:34344\n"
"v_mfma_f32_16x16x32_bf16 v[108:111], a[172:175], v[72:75], v[108:111]\n"
"ds_read_b64_tr_b16 a[210:211], v221 offset:34600\n"
"v_mfma_f32_16x16x32_bf16 v[108:111], a[176:179], v[104:107], v[108:111]\n"
"v_exp_f32 v14, v14\n"
"v_mfma_f32_16x16x32_bf16 v[84:87], a[148:151], v[16:19], v[168:171]\n"
"v_exp_f32 v15, v15\n"
"v_mfma_f32_16x16x32_bf16 v[84:87], a[152:155], v[48:51], v[84:87]\n"
"v_exp_f32 v44, v44\n"
"v_mfma_f32_16x16x32_bf16 v[84:87], a[156:159], v[80:83], v[84:87]\n"
"v_exp_f32 v45, v45\n"
"v_mfma_f32_16x16x32_bf16 v[84:87], a[160:163], v[112:115], v[84:87]\n"
"v_exp_f32 v46, v46\n"
"v_mfma_f32_16x16x32_bf16 v[116:119], a[164:167], v[16:19], v[168:171]\n"
"v_exp_f32 v47, v47\n"
"v_mfma_f32_16x16x32_bf16 v[116:119], a[168:171], v[48:51], v[116:119]\n"
"v_cvt_pk_bf16_f32 v13, v14, v15\n"
"v_cvt_pk_bf16_f32 v14, v44, v45\n"
"v_mfma_f32_16x16x32_bf16 v[116:119], a[172:175], v[80:83], v[116:119]\n"
"v_cvt_pk_bf16_f32 v15, v46, v47\n"
"v_mfma_f32_16x16x32_bf16 v[116:119], a[176:179], v[112:115], v[116:119]\n"
"v_exp_f32 v20, v20\n"
"v_mfma_f32_16x16x32_bf16 v[92:95], a[148:151], v[24:27], v[172:175]\n"
"v_exp_f32 v21, v21\n"
"v_mfma_f32_16x16x32_bf16 v[92:95], a[152:155], v[56:59], v[92:95]\n"
"v_exp_f32 v22, v22\n"
"v_mfma_f32_16x16x32_bf16 v[92:95], a[156:159], v[88:91], v[92:95]\n"
"v_exp_f32 v23, v23\n"
"v_mfma_f32_16x16x32_bf16 v[92:95], a[160:163], v[120:123], v[92:95]\n"
"v_exp_f32 v52, v52\n"
"v_mfma_f32_16x16x32_bf16 v[124:127], a[164:167], v[24:27], v[172:175]\n"
"v_exp_f32 v53, v53\n"
"v_mfma_f32_16x16x32_bf16 v[124:127], a[168:171], v[56:59], v[124:127]\n"
"v_exp_f32 v54, v54\n"
"v_mfma_f32_16x16x32_bf16 v[124:127], a[172:175], v[88:91], v[124:127]\n"
"v_exp_f32 v55, v55\n"
"v_mfma_f32_16x16x32_bf16 v[124:127], a[176:179], v[120:123], v[124:127]\n"
"s_add_u32 m0, s77, 114800\n"
"ds_read_b128 a[148:151], v222 offset:32800\n"
"s_lshr_b32 s93, s27, 4\n"
"v_mfma_f32_16x16x32_bf16 a[128:131], a[144:147], v[4:7], a[128:131]\n"
"s_waitcnt lgkmcnt(1)\n"
"ds_read_b128 a[152:155], v223 offset:32800\n"
"s_and_b32 s93, s93, 4\n"
"v_mfma_f32_16x16x32_bf16 a[0:3], a[180:183], v[4:7], a[0:3]\n"
"s_addk_i32 s82, 57344\n"
"buffer_load_dwordx4 v226, s[48:51], s82 offen lds\n"
"v_mfma_f32_16x16x32_bf16 a[16:19], a[184:187], v[4:7], a[16:19]\n"
"s_addk_i32 m0, 896\n"
"ds_read_b128 a[156:159], v222 offset:40992\n"
"s_and_b32 s3, s27, 3\n"
"v_mfma_f32_16x16x32_bf16 a[32:35], a[188:191], v[4:7], a[32:35]\n"
"s_add_u32 s3, s3, s93\n"
"ds_read_b128 a[160:163], v223 offset:40992\n"
"s_lshl_b32 s3, s3, 5\n"
"v_mfma_f32_16x16x32_bf16 a[48:51], a[192:195], v[4:7], a[48:51]\n"
"s_lshl_b32 s3, s3, 7\n"
"ds_read_b128 a[164:167], v222 offset:33312\n"
"s_add_u32 s3, s3, 65536\n"
"v_mfma_f32_16x16x32_bf16 a[64:67], a[196:199], v[4:7], a[64:67]\n"
"ds_read_b128 a[168:171], v223 offset:33312\n"
"v_and_b32 v239, 31, v253\n"
"v_lshlrev_b32 v239, 7, v239\n"
"buffer_load_dwordx2 v[240:241], v239, s[60:63], s3 offen sc1\n"
"v_mfma_f32_16x16x32_bf16 a[80:83], a[200:203], v[4:7], a[80:83]\n"
"ds_read_b128 a[172:175], v222 offset:41504\n"
"v_cvt_pk_bf16_f32 v20, v20, v21\n"
"v_mfma_f32_16x16x32_bf16 a[96:99], a[204:207], v[4:7], a[96:99]\n"
"ds_read_b128 a[176:179], v223 offset:41504\n"
"v_cvt_pk_bf16_f32 v21, v22, v23\n"
"v_mfma_f32_16x16x32_bf16 a[112:115], a[208:211], v[4:7], a[112:115]\n"
"v_cvt_pk_bf16_f32 v22, v52, v53\n"
"v_cvt_pk_bf16_f32 v23, v54, v55\n"
"v_mfma_f32_16x16x32_bf16 a[132:135], a[144:147], v[12:15], a[132:135]\n"
"v_exp_f32 v28, v28\n"
"v_mfma_f32_16x16x32_bf16 a[116:119], a[208:211], v[12:15], a[116:119]\n"
"buffer_load_dwordx4 v226, s[48:51], s82 offen offset:128 lds\n"
"v_mfma_f32_16x16x32_bf16 a[100:103], a[204:207], v[12:15], a[100:103]\n"
"s_addk_i32 m0, 7304\n"
"v_exp_f32 v29, v29\n"
"v_mfma_f32_16x16x32_bf16 a[84:87], a[200:203], v[12:15], a[84:87]\n"
"s_addk_i32 s82, 2048\n"
"v_exp_f32 v30, v30\n"
"v_mfma_f32_16x16x32_bf16 a[68:71], a[196:199], v[12:15], a[68:71]\n"
"v_exp_f32 v31, v31\n"
"v_mfma_f32_16x16x32_bf16 a[52:55], a[192:195], v[12:15], a[52:55]\n"
"v_exp_f32 v60, v60\n"
"v_mfma_f32_16x16x32_bf16 a[36:39], a[188:191], v[12:15], a[36:39]\n"
"buffer_load_dwordx4 v226, s[48:51], s82 offen lds\n"
"v_mfma_f32_16x16x32_bf16 a[20:23], a[184:187], v[12:15], a[20:23]\n"
"s_addk_i32 m0, 896\n"
"v_exp_f32 v61, v61\n"
"v_mfma_f32_16x16x32_bf16 a[4:7], a[180:183], v[12:15], a[4:7]\n"
"v_exp_f32 v62, v62\n"
"v_mfma_f32_16x16x32_bf16 a[136:139], a[144:147], v[20:23], a[136:139]\n"
"v_exp_f32 v63, v63\n"
"v_mfma_f32_16x16x32_bf16 a[8:11], a[180:183], v[20:23], a[8:11]\n"
"v_cvt_pk_bf16_f32 v28, v28, v29\n"
"v_cvt_pk_bf16_f32 v29, v30, v31\n"
"v_mfma_f32_16x16x32_bf16 a[24:27], a[184:187], v[20:23], a[24:27]\n"
"buffer_load_dwordx4 v226, s[48:51], s82 offen offset:128 lds\n"
"v_mfma_f32_16x16x32_bf16 a[40:43], a[188:191], v[20:23], a[40:43]\n"
"s_add_u32 m0, s77, 49200\n"
"v_cvt_pk_bf16_f32 v30, v60, v61\n"
"v_cvt_pk_bf16_f32 v31, v62, v63\n"
"v_mfma_f32_16x16x32_bf16 a[56:59], a[192:195], v[20:23], a[56:59]\n"
"s_mov_b32 s82, 221184\n"
"v_exp_f32 v68, v68\n"
"v_mfma_f32_16x16x32_bf16 a[72:75], a[196:199], v[20:23], a[72:75]\n"
"v_exp_f32 v69, v69\n"
"v_mfma_f32_16x16x32_bf16 a[88:91], a[200:203], v[20:23], a[88:91]\n"
"v_exp_f32 v70, v70\n"
"v_mfma_f32_16x16x32_bf16 a[104:107], a[204:207], v[20:23], a[104:107]\n"
"buffer_load_dwordx4 v227, s[36:39], s82 offen lds\n"
"v_mfma_f32_16x16x32_bf16 a[120:123], a[208:211], v[20:23], a[120:123]\n"
"s_addk_i32 m0, 1024\n"
"v_exp_f32 v71, v71\n"
"v_mfma_f32_16x16x32_bf16 a[140:143], a[144:147], v[28:31], a[140:143]\n"
"v_exp_f32 v100, v100\n"
"v_mfma_f32_16x16x32_bf16 a[124:127], a[208:211], v[28:31], a[124:127]\n"
"v_exp_f32 v101, v101\n"
"v_mfma_f32_16x16x32_bf16 a[108:111], a[204:207], v[28:31], a[108:111]\n"
"v_exp_f32 v102, v102\n"
"v_mfma_f32_16x16x32_bf16 a[92:95], a[200:203], v[28:31], a[92:95]\n"
"buffer_load_dwordx4 v227, s[40:43], s82 offen lds\n"
"v_mfma_f32_16x16x32_bf16 a[76:79], a[196:199], v[28:31], a[76:79]\n"
"s_addk_i32 m0, 7176\n"
"v_exp_f32 v103, v103\n"
"v_mfma_f32_16x16x32_bf16 a[60:63], a[192:195], v[28:31], a[60:63]\n"
"s_addk_i32 s82, 27648\n"
"v_cvt_pk_bf16_f32 v68, v68, v69\n"
"v_cvt_pk_bf16_f32 v69, v70, v71\n"
"v_mfma_f32_16x16x32_bf16 a[44:47], a[188:191], v[28:31], a[44:47]\n"
"v_cvt_pk_bf16_f32 v70, v100, v101\n"
"v_cvt_pk_bf16_f32 v71, v102, v103\n"
"v_mfma_f32_16x16x32_bf16 a[28:31], a[184:187], v[28:31], a[28:31]\n"
"v_exp_f32 v76, v76\n"
"v_mfma_f32_16x16x32_bf16 a[12:15], a[180:183], v[28:31], a[12:15]\n"
"s_waitcnt lgkmcnt(0)\n"
"s_getpc_b64 s[4:5]\n"
"L_pc2_%=:\n"
"s_mov_b32 s6, L_end_%=-L_pc2_%=\n"
"s_and_b32 s5, s5, 0xffff\n"
"s_mov_b32 s7, 0x20000\n"
"v_mfma_f32_16x16x32_bf16 v[4:7], a[148:151], v[0:3], v[160:163]\n"
"buffer_load_dword v247, v252, s[4:7], 0 offen\n"
"v_mfma_f32_16x16x32_bf16 v[4:7], a[152:155], v[32:35], v[4:7]\n"
"ds_read_b64_tr_b16 a[180:181], v221 offset:36896\n"
"v_mfma_f32_16x16x32_bf16 v[4:7], a[156:159], v[64:67], v[4:7]\n"
"ds_read_b64_tr_b16 a[182:183], v221 offset:37152\n"
"v_mfma_f32_16x16x32_bf16 v[4:7], a[160:163], v[96:99], v[4:7]\n"
"ds_read_b64_tr_b16 a[184:185], v221 offset:36904\n"
"v_mfma_f32_16x16x32_bf16 v[36:39], a[164:167], v[0:3], v[160:163]\n"
"ds_read_b64_tr_b16 a[186:187], v221 offset:37160\n"
"v_mfma_f32_16x16x32_bf16 v[36:39], a[168:171], v[32:35], v[36:39]\n"
"buffer_load_dwordx4 v227, s[36:39], s82 offen lds\n"
"v_mfma_f32_16x16x32_bf16 v[36:39], a[172:175], v[64:67], v[36:39]\n"
"s_addk_i32 m0, 1024\n"
"ds_read_b64_tr_b16 a[188:189], v221 offset:37408\n"
"v_mfma_f32_16x16x32_bf16 v[36:39], a[176:179], v[96:99], v[36:39]\n"
"ds_read_b64_tr_b16 a[190:191], v221 offset:37664\n"
"v_mfma_f32_16x16x32_bf16 v[12:15], a[148:151], v[8:11], v[164:167]\n"
"ds_read_b64_tr_b16 a[192:193], v221 offset:37416\n"
"v_mfma_f32_16x16x32_bf16 v[12:15], a[152:155], v[40:43], v[12:15]\n"
"L_st7_%=:\n"
"s_cbranch_execz L_st11_%=\n"
"ds_read_b64_tr_b16 a[194:195], v221 offset:37672\n"
"v_mfma_f32_16x16x32_bf16 v[12:15], a[156:159], v[72:75], v[12:15]\n"
"buffer_load_dwordx4 v227, s[40:43], s82 offen lds\n"
"v_mfma_f32_16x16x32_bf16 v[12:15], a[160:163], v[104:107], v[12:15]\n"
"ds_read_b64_tr_b16 a[196:197], v221 offset:37920\n"
"v_mfma_f32_16x16x32_bf16 v[44:47], a[164:167], v[8:11], v[164:167]\n"
"ds_read_b64_tr_b16 a[198:199], v221 offset:38176\n"
"v_mfma_f32_16x16x32_bf16 v[44:47], a[168:171], v[40:43], v[44:47]\n"
"ds_read_b64_tr_b16 a[200:201], v221 offset:37928\n"
"v_mfma_f32_16x16x32_bf16 v[44:47], a[172:175], v[72:75], v[44:47]\n"
"ds_read_b64_tr_b16 a[202:203], v221 offset:38184\n"
"v_mfma_f32_16x16x32_bf16 v[44:47], a[176:179], v[104:107], v[44:47]\n"
"ds_read_b64_tr_b16 a[204:205], v221 offset:38432\n"
"v_mfma_f32_16x16x32_bf16 v[20:23], a[148:151], v[16:19], v[168:171]\n"
"ds_read_b64_tr_b16 a[206:207], v221 offset:38688\n"
"v_mfma_f32_16x16x32_bf16 v[20:23], a[152:155], v[48:51], v[20:23]\n"
"ds_read_b64_tr_b16 a[208:209], v221 offset:38440\n"
"v_mfma_f32_16x16x32_bf16 v[20:23], a[156:159], v[80:83], v[20:23]\n"
"ds_read_b64_tr_b16 a[210:211], v221 offset:38696\n"
"v_mfma_f32_16x16x32_bf16 v[20:23], a[160:163], v[112:115], v[20:23]\n"
"v_exp_f32 v77, v77\n"
"v_mfma_f32_16x16x32_bf16 v[52:55], a[164:167], v[16:19], v[168:171]\n"
"v_exp_f32 v78, v78\n"
"v_mfma_f32_16x16x32_bf16 v[52:55], a[168:171], v[48:51], v[52:55]\n"
"v_exp_f32 v79, v79\n"
"v_mfma_f32_16x16x32_bf16 v[52:55], a[172:175], v[80:83], v[52:55]\n"
"v_exp_f32 v108, v108\n"
"v_mfma_f32_16x16x32_bf16 v[52:55], a[176:179], v[112:115], v[52:55]\n"
"v_exp_f32 v109, v109\n"
"v_mfma_f32_16x16x32_bf16 v[28:31], a[148:151], v[24:27], v[172:175]\n"
"v_exp_f32 v110, v110\n"
"v_mfma_f32_16x16x32_bf16 v[28:31], a[152:155], v[56:59], v[28:31]\n"
"v_exp_f32 v111, v111\n"
"v_mfma_f32_16x16x32_bf16 v[28:31], a[156:159], v[88:91], v[28:31]\n"
"v_cvt_pk_bf16_f32 v76, v76, v77\n"
"v_cvt_pk_bf16_f32 v77, v78, v79\n"
"v_mfma_f32_16x16x32_bf16 v[28:31], a[160:163], v[120:123], v[28:31]\n"
"v_cvt_pk_bf16_f32 v78, v108, v109\n"
"v_cvt_pk_bf16_f32 v79, v110, v111\n"
"v_mfma_f32_16x16x32_bf16 v[60:63], a[164:167], v[24:27], v[172:175]\n"
"v_exp_f32 v84, v84\n"
"v_mfma_f32_16x16x32_bf16 v[60:63], a[168:171], v[56:59], v[60:63]\n"
"v_exp_f32 v85, v85\n"
"v_mfma_f32_16x16x32_bf16 v[60:63], a[172:175], v[88:91], v[60:63]\n"
"v_exp_f32 v86, v86\n"
"v_mfma_f32_16x16x32_bf16 v[60:63], a[176:179], v[120:123], v[60:63]\n"
"s_add_u32 m0, s76, 16400\n"
"v_exp_f32 v87, v87\n"
"v_mfma_f32_16x16x32_bf16 a[128:131], a[144:147], v[68:71], a[128:131]\n"
"s_waitcnt lgkmcnt(0)\n"
"v_exp_f32 v116, v116\n"
"v_mfma_f32_16x16x32_bf16 a[0:3], a[180:183], v[68:71], a[0:3]\n"
"s_mov_b32 s82, 114688\n"
"buffer_load_dwordx4 v225, s[44:47], s82 offen lds\n"
"v_mfma_f32_16x16x32_bf16 a[16:19], a[184:187], v[68:71], a[16:19]\n"
"s_addk_i32 m0, 8064\n"
"v_exp_f32 v117, v117\n"
"v_mfma_f32_16x16x32_bf16 a[32:35], a[188:191], v[68:71], a[32:35]\n"
"v_exp_f32 v118, v118\n"
"v_mfma_f32_16x16x32_bf16 a[48:51], a[192:195], v[68:71], a[48:51]\n"
"v_exp_f32 v119, v119\n"
"v_mfma_f32_16x16x32_bf16 a[64:67], a[196:199], v[68:71], a[64:67]\n"
"ds_read_b128 a[148:151], v222 offset:36896\n"
"v_cvt_pk_bf16_f32 v84, v84, v85\n"
"v_mfma_f32_16x16x32_bf16 a[80:83], a[200:203], v[68:71], a[80:83]\n"
"buffer_load_dwordx4 v225, s[44:47], s82 offen offset:128 lds\n"
"v_mfma_f32_16x16x32_bf16 a[96:99], a[204:207], v[68:71], a[96:99]\n"
"s_addk_i32 m0, 61568\n"
"ds_read_b128 a[152:155], v223 offset:36896\n"
"v_cvt_pk_bf16_f32 v85, v86, v87\n"
"v_mfma_f32_16x16x32_bf16 a[112:115], a[208:211], v[68:71], a[112:115]\n"
"s_addk_i32 s82, 8192\n"
"ds_read_b128 a[156:159], v222 offset:45088\n"
"v_cvt_pk_bf16_f32 v86, v116, v117\n"
"v_mfma_f32_16x16x32_bf16 a[132:135], a[144:147], v[76:79], a[132:135]\n"
"ds_read_b128 a[160:163], v223 offset:45088\n"
"v_cvt_pk_bf16_f32 v87, v118, v119\n"
"v_mfma_f32_16x16x32_bf16 a[116:119], a[208:211], v[76:79], a[116:119]\n"
"v_exp_f32 v92, v92\n"
"v_mfma_f32_16x16x32_bf16 a[100:103], a[204:207], v[76:79], a[100:103]\n"
"buffer_load_dwordx4 v225, s[44:47], s82 offen lds\n"
"v_mfma_f32_16x16x32_bf16 a[84:87], a[200:203], v[76:79], a[84:87]\n"
"s_addk_i32 m0, 8064\n"
"v_exp_f32 v93, v93\n"
"v_mfma_f32_16x16x32_bf16 a[68:71], a[196:199], v[76:79], a[68:71]\n"
"v_exp_f32 v94, v94\n"
"v_mfma_f32_16x16x32_bf16 a[52:55], a[192:195], v[76:79], a[52:55]\n"
"v_exp_f32 v95, v95\n"
"v_mfma_f32_16x16x32_bf16 a[36:39], a[188:191], v[76:79], a[36:39]\n"
"v_exp_f32 v124, v124\n"
"v_mfma_f32_16x16x32_bf16 a[20:23], a[184:187], v[76:79], a[20:23]\n"
"buffer_load_dwordx4 v225, s[44:47], s82 offen offset:128 lds\n"
"v_mfma_f32_16x16x32_bf16 a[4:7], a[180:183], v[76:79], a[4:7]\n"
"s_add_u32 m0, s77, 65600\n"
"v_exp_f32 v125, v125\n"
"v_mfma_f32_16x16x32_bf16 a[136:139], a[144:147], v[84:87], a[136:139]\n"
"s_mov_b32 s82, 442368\n"
"v_exp_f32 v126, v126\n"
"v_mfma_f32_16x16x32_bf16 a[8:11], a[180:183], v[84:87], a[8:11]\n"
"v_exp_f32 v127, v127\n"
"v_mfma_f32_16x16x32_bf16 a[24:27], a[184:187], v[84:87], a[24:27]\n"
"ds_read_b128 a[164:167], v222 offset:37408\n"
"v_cvt_pk_bf16_f32 v92, v92, v93\n"
"v_mfma_f32_16x16x32_bf16 a[40:43], a[188:191], v[84:87], a[40:43]\n"
"buffer_load_dwordx4 v227, s[36:39], s82 offen lds\n"
"v_mfma_f32_16x16x32_bf16 a[56:59], a[192:195], v[84:87], a[56:59]\n"
"s_addk_i32 m0, 1024\n"
"ds_read_b128 a[168:171], v223 offset:37408\n"
"v_cvt_pk_bf16_f32 v93, v94, v95\n"
"v_mfma_f32_16x16x32_bf16 a[72:75], a[196:199], v[84:87], a[72:75]\n"
"ds_read_b128 a[172:175], v222 offset:45600\n"
"v_cvt_pk_bf16_f32 v94, v124, v125\n"
"v_mfma_f32_16x16x32_bf16 a[88:91], a[200:203], v[84:87], a[88:91]\n"
"ds_read_b128 a[176:179], v223 offset:45600\n"
"v_cvt_pk_bf16_f32 v95, v126, v127\n"
"v_mfma_f32_16x16x32_bf16 a[104:107], a[204:207], v[84:87], a[104:107]\n"
"v_exp_f32 v4, v4\n"
"v_mfma_f32_16x16x32_bf16 a[120:123], a[208:211], v[84:87], a[120:123]\n"
"buffer_load_dwordx4 v227, s[40:43], s82 offen lds\n"
"v_mfma_f32_16x16x32_bf16 a[140:143], a[144:147], v[92:95], a[140:143]\n"
"s_addk_i32 m0, 7176\n"
"v_exp_f32 v5, v5\n"
"v_mfma_f32_16x16x32_bf16 a[124:127], a[208:211], v[92:95], a[124:127]\n"
"s_addk_i32 s82, 27648\n"
"v_exp_f32 v6, v6\n"
"v_mfma_f32_16x16x32_bf16 a[108:111], a[204:207], v[92:95], a[108:111]\n"
"v_exp_f32 v7, v7\n"
"v_mfma_f32_16x16x32_bf16 a[92:95], a[200:203], v[92:95], a[92:95]\n"
"v_exp_f32 v36, v36\n"
"v_mfma_f32_16x16x32_bf16 a[76:79], a[196:199], v[92:95], a[76:79]\n"
"buffer_load_dwordx4 v227, s[36:39], s82 offen lds\n"
"v_mfma_f32_16x16x32_bf16 a[60:63], a[192:195], v[92:95], a[60:63]\n"
"s_addk_i32 m0, 1024\n"
"v_exp_f32 v37, v37\n"
"v_mfma_f32_16x16x32_bf16 a[44:47], a[188:191], v[92:95], a[44:47]\n"
"v_exp_f32 v38, v38\n"
"v_mfma_f32_16x16x32_bf16 a[28:31], a[184:187], v[92:95], a[28:31]\n"
"v_exp_f32 v39, v39\n"
"v_mfma_f32_16x16x32_bf16 a[12:15], a[180:183], v[92:95], a[12:15]\n"
"s_waitcnt vmcnt(17) lgkmcnt(0)\n"
"s_barrier\n"
"s_lshl_b32 s99, s73, 3\n"
"v_mfma_f32_16x16x32_bf16 v[68:71], a[148:151], v[0:3], v[160:163]\n"
"s_add_u32 s99, s99, 32768\n"
"buffer_store_dwordx2 v[232:233], v231, s[60:63], s99 offen sc1\n"
"v_mfma_f32_16x16x32_bf16 v[68:71], a[152:155], v[32:35], v[68:71]\n"
"ds_read_b64_tr_b16 a[180:181], v221 offset:0\n"
"v_cvt_pk_bf16_f32 v4, v4, v5\n"
"v_mfma_f32_16x16x32_bf16 v[68:71], a[156:159], v[64:67], v[68:71]\n"
"ds_read_b64_tr_b16 a[182:183], v221 offset:256\n"
"v_cvt_pk_bf16_f32 v5, v6, v7\n"
"v_mfma_f32_16x16x32_bf16 v[68:71], a[160:163], v[96:99], v[68:71]\n"
"ds_read_b64_tr_b16 a[184:185], v221 offset:8\n"
"v_cvt_pk_bf16_f32 v6, v36, v37\n"
"v_mfma_f32_16x16x32_bf16 v[100:103], a[164:167], v[0:3], v[160:163]\n"
"ds_read_b64_tr_b16 a[186:187], v221 offset:264\n"
"v_cvt_pk_bf16_f32 v7, v38, v39\n"
"v_mfma_f32_16x16x32_bf16 v[100:103], a[168:171], v[32:35], v[100:103]\n"
"buffer_load_dwordx4 v227, s[40:43], s82 offen lds\n"
"v_mfma_f32_16x16x32_bf16 v[100:103], a[172:175], v[64:67], v[100:103]\n"
"ds_read_b64_tr_b16 a[188:189], v221 offset:512\n"
"v_mfma_f32_16x16x32_bf16 v[100:103], a[176:179], v[96:99], v[100:103]\n"
"ds_read_b64_tr_b16 a[190:191], v221 offset:768\n"
"v_mfma_f32_16x16x32_bf16 v[76:79], a[148:151], v[8:11], v[164:167]\n"
"ds_read_b64_tr_b16 a[192:193], v221 offset:520\n"
"v_mfma_f32_16x16x32_bf16 v[76:79], a[152:155], v[40:43], v[76:79]\n"
"ds_read_b64_tr_b16 a[194:195], v221 offset:776\n"
"v_mfma_f32_16x16x32_bf16 v[76:79], a[156:159], v[72:75], v[76:79]\n"
"ds_read_b64_tr_b16 a[196:197], v221 offset:1024\n"
"v_mfma_f32_16x16x32_bf16 v[76:79], a[160:163], v[104:107], v[76:79]\n"
"ds_read_b64_tr_b16 a[198:199], v221 offset:1280\n"
"v_mfma_f32_16x16x32_bf16 v[108:111], a[164:167], v[8:11], v[164:167]\n"
"ds_read_b64_tr_b16 a[200:201], v221 offset:1032\n"
"v_mfma_f32_16x16x32_bf16 v[108:111], a[168:171], v[40:43], v[108:111]\n"
"ds_read_b64_tr_b16 a[202:203], v221 offset:1288\n"
"v_mfma_f32_16x16x32_bf16 v[108:111], a[172:175], v[72:75], v[108:111]\n"
"ds_read_b64_tr_b16 a[204:205], v221 offset:1536\n"
"v_mfma_f32_16x16x32_bf16 v[108:111], a[176:179], v[104:107], v[108:111]\n"
"ds_read_b64_tr_b16 a[206:207], v221 offset:1792\n"
"v_mfma_f32_16x16x32_bf16 v[84:87], a[148:151], v[16:19], v[168:171]\n"
"ds_read_b64_tr_b16 a[208:209], v221 offset:1544\n"
"v_mfma_f32_16x16x32_bf16 v[84:87], a[152:155], v[48:51], v[84:87]\n"
"ds_read_b64_tr_b16 a[210:211], v221 offset:1800\n"
"v_mfma_f32_16x16x32_bf16 v[84:87], a[156:159], v[80:83], v[84:87]\n"
"v_exp_f32 v12, v12\n"
"v_mfma_f32_16x16x32_bf16 v[84:87], a[160:163], v[112:115], v[84:87]\n"
"v_exp_f32 v13, v13\n"
"v_mfma_f32_16x16x32_bf16 v[116:119], a[164:167], v[16:19], v[168:171]\n"
"v_exp_f32 v14, v14\n"
"v_mfma_f32_16x16x32_bf16 v[116:119], a[168:171], v[48:51], v[116:119]\n"
"v_exp_f32 v15, v15\n"
"v_mfma_f32_16x16x32_bf16 v[116:119], a[172:175], v[80:83], v[116:119]\n"
"v_exp_f32 v44, v44\n"
"v_mfma_f32_16x16x32_bf16 v[116:119], a[176:179], v[112:115], v[116:119]\n"
"v_exp_f32 v45, v45\n"
"v_mfma_f32_16x16x32_bf16 v[92:95], a[148:151], v[24:27], v[172:175]\n"
"v_exp_f32 v46, v46\n"
"v_mfma_f32_16x16x32_bf16 v[92:95], a[152:155], v[56:59], v[92:95]\n"
"v_exp_f32 v47, v47\n"
"v_mfma_f32_16x16x32_bf16 v[92:95], a[156:159], v[88:91], v[92:95]\n"
"v_cvt_pk_bf16_f32 v12, v12, v13\n"
"v_cvt_pk_bf16_f32 v13, v14, v15\n"
"v_mfma_f32_16x16x32_bf16 v[92:95], a[160:163], v[120:123], v[92:95]\n"
"v_cvt_pk_bf16_f32 v14, v44, v45\n"
"v_cvt_pk_bf16_f32 v15, v46, v47\n"
"v_mfma_f32_16x16x32_bf16 v[124:127], a[164:167], v[24:27], v[172:175]\n"
"v_exp_f32 v20, v20\n"
"v_mfma_f32_16x16x32_bf16 v[124:127], a[168:171], v[56:59], v[124:127]\n"
"v_exp_f32 v21, v21\n"
"v_mfma_f32_16x16x32_bf16 v[124:127], a[172:175], v[88:91], v[124:127]\n"
"v_exp_f32 v22, v22\n"
"v_mfma_f32_16x16x32_bf16 v[124:127], a[176:179], v[120:123], v[124:127]\n"
"s_waitcnt vmcnt(17) lgkmcnt(0)\n"
"v_cmp_eq_u64_e64 s[4:5], v[240:241], s[80:81]\n"
"s_nop 1\n"
"s_cmp_eq_u64 s[4:5], -1\n"
"s_cbranch_scc0 L_spinX_%=\n"
"L_retX_%=:\n"
"v_mfma_f32_16x16x32_bf16 a[128:131], a[144:147], v[4:7], a[128:131]\n"
"s_add_u32 m0, s77, 131200\n"
"v_exp_f32 v23, v23\n"
"v_mfma_f32_16x16x32_bf16 a[0:3], a[180:183], v[4:7], a[0:3]\n"
"s_mov_b32 s82, 114688\n"
"buffer_load_dwordx4 v226, s[48:51], s82 offen lds\n"
"v_mfma_f32_16x16x32_bf16 a[16:19], a[184:187], v[4:7], a[16:19]\n"
"s_addk_i32 m0, 896\n"
"v_exp_f32 v52, v52\n"
"v_mfma_f32_16x16x32_bf16 a[32:35], a[188:191], v[4:7], a[32:35]\n"
"s_add_u32 s92, s27, 0\n"
"v_exp_f32 v53, v53\n"
"v_mfma_f32_16x16x32_bf16 a[48:51], a[192:195], v[4:7], a[48:51]\n"
"s_mul_i32 s92, s92, 21504\n"
"v_exp_f32 v54, v54\n"
"v_mfma_f32_16x16x32_bf16 a[64:67], a[196:199], v[4:7], a[64:67]\n"
"s_add_u32 s84, s92, 1179648\n"
"v_exp_f32 v55, v55\n"
"v_mfma_f32_16x16x32_bf16 a[80:83], a[200:203], v[4:7], a[80:83]\n"
"s_add_u32 s92, s27, 4\n"
"buffer_load_dwordx4 v226, s[48:51], s82 offen offset:128 lds\n"
"v_mfma_f32_16x16x32_bf16 a[96:99], a[204:207], v[4:7], a[96:99]\n"
"s_addk_i32 m0, 7304\n"
"ds_read_b128 a[148:151], v222 offset:0\n"
"v_cvt_pk_bf16_f32 v20, v20, v21\n"
"v_mfma_f32_16x16x32_bf16 a[112:115], a[208:211], v[4:7], a[112:115]\n"
"s_addk_i32 s82, 2048\n"
"ds_read_b128 a[152:155], v223 offset:0\n"
"v_cvt_pk_bf16_f32 v21, v22, v23\n"
"v_mfma_f32_16x16x32_bf16 a[132:135], a[144:147], v[12:15], a[132:135]\n"
"s_mul_i32 s92, s92, 21504\n"
"ds_read_b128 a[156:159], v222 offset:8192\n"
"v_cvt_pk_bf16_f32 v22, v52, v53\n"
"v_mfma_f32_16x16x32_bf16 a[116:119], a[208:211], v[12:15], a[116:119]\n"
"s_add_u32 s85, s92, 1179648\n"
"ds_read_b128 a[160:163], v223 offset:8192\n"
"v_cvt_pk_bf16_f32 v23, v54, v55\n"
"v_mfma_f32_16x16x32_bf16 a[100:103], a[204:207], v[12:15], a[100:103]\n"
"s_add_u32 s92, s27, 8\n"
"buffer_load_dwordx4 v226, s[48:51], s82 offen lds\n"
"v_mfma_f32_16x16x32_bf16 a[84:87], a[200:203], v[12:15], a[84:87]\n"
"s_addk_i32 m0, 896\n"
"v_exp_f32 v28, v28\n"
"v_mfma_f32_16x16x32_bf16 a[68:71], a[196:199], v[12:15], a[68:71]\n"
"s_mul_i32 s92, s92, 21504\n"
"v_exp_f32 v29, v29\n"
"v_mfma_f32_16x16x32_bf16 a[52:55], a[192:195], v[12:15], a[52:55]\n"
"s_add_u32 s86, s92, 1179648\n"
"v_exp_f32 v30, v30\n"
"v_mfma_f32_16x16x32_bf16 a[36:39], a[188:191], v[12:15], a[36:39]\n"
"s_add_u32 s92, s27, 12\n"
"v_exp_f32 v31, v31\n"
"v_mfma_f32_16x16x32_bf16 a[20:23], a[184:187], v[12:15], a[20:23]\n"
"s_mul_i32 s92, s92, 21504\n"
"buffer_load_dwordx4 v226, s[48:51], s82 offen offset:128 lds\n"
"v_mfma_f32_16x16x32_bf16 a[4:7], a[180:183], v[12:15], a[4:7]\n"
"s_add_u32 s87, s92, 1179648\n"
"v_exp_f32 v60, v60\n"
"v_mfma_f32_16x16x32_bf16 a[136:139], a[144:147], v[20:23], a[136:139]\n"
"v_exp_f32 v61, v61\n"
"v_mfma_f32_16x16x32_bf16 a[8:11], a[180:183], v[20:23], a[8:11]\n"
"v_exp_f32 v62, v62\n"
"v_mfma_f32_16x16x32_bf16 a[24:27], a[184:187], v[20:23], a[24:27]\n"
"v_exp_f32 v63, v63\n"
"v_mfma_f32_16x16x32_bf16 a[40:43], a[188:191], v[20:23], a[40:43]\n"
"buffer_load_dwordx4 a[212:215], v216, s[68:71], s84 offen sc1\n"
"v_mfma_f32_16x16x32_bf16 a[56:59], a[192:195], v[20:23], a[56:59]\n"
"ds_read_b128 a[164:167], v222 offset:512\n"
"v_cvt_pk_bf16_f32 v28, v28, v29\n"
"v_mfma_f32_16x16x32_bf16 a[72:75], a[196:199], v[20:23], a[72:75]\n"
"ds_read_b128 a[168:171], v223 offset:512\n"
"v_cvt_pk_bf16_f32 v29, v30, v31\n"
"v_mfma_f32_16x16x32_bf16 a[88:91], a[200:203], v[20:23], a[88:91]\n"
"ds_read_b128 a[172:175], v222 offset:8704\n"
"v_cvt_pk_bf16_f32 v30, v60, v61\n"
"v_cvt_pk_bf16_f32 v31, v62, v63\n"
"v_mfma_f32_16x16x32_bf16 a[104:107], a[204:207], v[20:23], a[104:107]\n"
"ds_read_b128 a[176:179], v223 offset:8704\n"
"v_mfma_f32_16x16x32_bf16 a[120:123], a[208:211], v[20:23], a[120:123]\n"
"buffer_load_dwordx4 a[216:219], v216, s[68:71], s85 offen sc1\n"
"v_mfma_f32_16x16x32_bf16 a[140:143], a[144:147], v[28:31], a[140:143]\n"
"v_exp_f32 v68, v68\n"
"v_mfma_f32_16x16x32_bf16 a[124:127], a[208:211], v[28:31], a[124:127]\n"
"v_exp_f32 v69, v69\n"
"v_mfma_f32_16x16x32_bf16 a[108:111], a[204:207], v[28:31], a[108:111]\n"
"v_exp_f32 v70, v70\n"
"v_mfma_f32_16x16x32_bf16 a[92:95], a[200:203], v[28:31], a[92:95]\n"
"v_exp_f32 v71, v71\n"
"v_mfma_f32_16x16x32_bf16 a[76:79], a[196:199], v[28:31], a[76:79]\n"
"buffer_load_dwordx4 a[220:223], v216, s[68:71], s86 offen sc1\n"
"v_mfma_f32_16x16x32_bf16 a[60:63], a[192:195], v[28:31], a[60:63]\n"
"v_exp_f32 v100, v100\n"
"v_mfma_f32_16x16x32_bf16 a[44:47], a[188:191], v[28:31], a[44:47]\n"
"v_exp_f32 v101, v101\n"
"v_mfma_f32_16x16x32_bf16 a[28:31], a[184:187], v[28:31], a[28:31]\n"
"v_exp_f32 v102, v102\n"
"v_mfma_f32_16x16x32_bf16 a[12:15], a[180:183], v[28:31], a[12:15]\n"
"s_waitcnt lgkmcnt(2)\n"
"ds_read_b64_tr_b16 a[180:181], v221 offset:4096\n"
"v_cvt_pk_bf16_f32 v68, v68, v69\n"
"v_mfma_f32_16x16x32_bf16 v[4:7], a[148:151], v[0:3], v[160:163]\n"
"buffer_load_dwordx4 a[224:227], v216, s[68:71], s87 offen sc1\n"
"v_mfma_f32_16x16x32_bf16 v[4:7], a[152:155], v[32:35], v[4:7]\n"
"ds_read_b64_tr_b16 a[182:183], v221 offset:4352\n"
"v_cvt_pk_bf16_f32 v69, v70, v71\n"
"v_mfma_f32_16x16x32_bf16 v[4:7], a[156:159], v[64:67], v[4:7]\n"
"ds_read_b64_tr_b16 a[184:185], v221 offset:4104\n"
"v_cvt_pk_bf16_f32 v70, v100, v101\n"
"v_mfma_f32_16x16x32_bf16 v[4:7], a[160:163], v[96:99], v[4:7]\n"
"ds_read_b64_tr_b16 a[186:187], v221 offset:4360\n"
"v_mfma_f32_16x16x32_bf16 v[36:39], a[164:167], v[0:3], v[160:163]\n"
"ds_read_b64_tr_b16 a[188:189], v221 offset:4608\n"
"v_mfma_f32_16x16x32_bf16 v[36:39], a[168:171], v[32:35], v[36:39]\n"
"s_waitcnt lgkmcnt(5)\n"
"ds_read_b64_tr_b16 a[190:191], v221 offset:4864\n"
"v_mfma_f32_16x16x32_bf16 v[36:39], a[172:175], v[64:67], v[36:39]\n"
"ds_read_b64_tr_b16 a[192:193], v221 offset:4616\n"
"v_mfma_f32_16x16x32_bf16 v[36:39], a[176:179], v[96:99], v[36:39]\n"
"ds_read_b64_tr_b16 a[194:195], v221 offset:4872\n"
"v_mfma_f32_16x16x32_bf16 v[12:15], a[148:151], v[8:11], v[164:167]\n"
"ds_read_b64_tr_b16 a[196:197], v221 offset:5120\n"
"v_mfma_f32_16x16x32_bf16 v[12:15], a[152:155], v[40:43], v[12:15]\n"
"ds_read_b64_tr_b16 a[198:199], v221 offset:5376\n"
"v_mfma_f32_16x16x32_bf16 v[12:15], a[156:159], v[72:75], v[12:15]\n"
"v_exp_f32 v103, v103\n"
"v_mfma_f32_16x16x32_bf16 v[12:15], a[160:163], v[104:107], v[12:15]\n"
"ds_read_b64_tr_b16 a[200:201], v221 offset:5128\n"
"v_cvt_pk_bf16_f32 v71, v102, v103\n"
"v_mfma_f32_16x16x32_bf16 v[44:47], a[164:167], v[8:11], v[164:167]\n"
"ds_read_b64_tr_b16 a[202:203], v221 offset:5384\n"
"v_mfma_f32_16x16x32_bf16 v[44:47], a[168:171], v[40:43], v[44:47]\n"
"ds_read_b64_tr_b16 a[204:205], v221 offset:5632\n"
"v_mfma_f32_16x16x32_bf16 v[44:47], a[172:175], v[72:75], v[44:47]\n"
"ds_read_b64_tr_b16 a[206:207], v221 offset:5888\n"
"v_mfma_f32_16x16x32_bf16 v[44:47], a[176:179], v[104:107], v[44:47]\n"
"ds_read_b64_tr_b16 a[208:209], v221 offset:5640\n"
"v_mfma_f32_16x16x32_bf16 v[20:23], a[148:151], v[16:19], v[168:171]\n"
"ds_read_b64_tr_b16 a[210:211], v221 offset:5896\n"
"v_mfma_f32_16x16x32_bf16 v[20:23], a[152:155], v[48:51], v[20:23]\n"
"v_exp_f32 v76, v76\n"
"v_mfma_f32_16x16x32_bf16 v[20:23], a[156:159], v[80:83], v[20:23]\n"
"v_exp_f32 v77, v77\n"
"v_mfma_f32_16x16x32_bf16 v[20:23], a[160:163], v[112:115], v[20:23]\n"
"v_exp_f32 v78, v78\n"
"v_mfma_f32_16x16x32_bf16 v[52:55], a[164:167], v[16:19], v[168:171]\n"
"v_exp_f32 v79, v79\n"
"v_mfma_f32_16x16x32_bf16 v[52:55], a[168:171], v[48:51], v[52:55]\n"
"v_exp_f32 v108, v108\n"
"v_mfma_f32_16x16x32_bf16 v[52:55], a[172:175], v[80:83], v[52:55]\n"
"v_exp_f32 v109, v109\n"
"v_mfma_f32_16x16x32_bf16 v[52:55], a[176:179], v[112:115], v[52:55]\n"
"v_exp_f32 v110, v110\n"
"v_mfma_f32_16x16x32_bf16 v[28:31], a[148:151], v[24:27], v[172:175]\n"
"v_exp_f32 v111, v111\n"
"v_mfma_f32_16x16x32_bf16 v[28:31], a[152:155], v[56:59], v[28:31]\n"
"v_cvt_pk_bf16_f32 v76, v76, v77\n"
"v_cvt_pk_bf16_f32 v77, v78, v79\n"
"v_mfma_f32_16x16x32_bf16 v[28:31], a[156:159], v[88:91], v[28:31]\n"
"v_cvt_pk_bf16_f32 v78, v108, v109\n"
"v_cvt_pk_bf16_f32 v79, v110, v111\n"
"v_mfma_f32_16x16x32_bf16 v[28:31], a[160:163], v[120:123], v[28:31]\n"
"v_exp_f32 v84, v84\n"
"v_mfma_f32_16x16x32_bf16 v[60:63], a[164:167], v[24:27], v[172:175]\n"
"v_exp_f32 v85, v85\n"
"v_mfma_f32_16x16x32_bf16 v[60:63], a[168:171], v[56:59], v[60:63]\n"
"v_exp_f32 v86, v86\n"
"v_mfma_f32_16x16x32_bf16 v[60:63], a[172:175], v[88:91], v[60:63]\n"
"v_exp_f32 v87, v87\n"
"v_mfma_f32_16x16x32_bf16 v[60:63], a[176:179], v[120:123], v[60:63]\n"
"s_add_u32 m0, s77, 32800\n"
"v_exp_f32 v116, v116\n"
"v_mfma_f32_16x16x32_bf16 a[128:131], a[144:147], v[68:71], a[128:131]\n"
"s_waitcnt lgkmcnt(0)\n"
"v_exp_f32 v117, v117\n"
"v_mfma_f32_16x16x32_bf16 a[0:3], a[180:183], v[68:71], a[0:3]\n"
"s_mov_b32 s82, 663552\n"
"buffer_load_dwordx4 v227, s[36:39], s82 offen lds\n"
"v_mfma_f32_16x16x32_bf16 a[16:19], a[184:187], v[68:71], a[16:19]\n"
"s_addk_i32 m0, 1024\n"
"v_exp_f32 v118, v118\n"
"v_mfma_f32_16x16x32_bf16 a[32:35], a[188:191], v[68:71], a[32:35]\n"
"v_exp_f32 v119, v119\n"
"v_mfma_f32_16x16x32_bf16 a[48:51], a[192:195], v[68:71], a[48:51]\n"
"ds_read_b128 a[148:151], v222 offset:4096\n"
"v_cvt_pk_bf16_f32 v84, v84, v85\n"
"v_mfma_f32_16x16x32_bf16 a[64:67], a[196:199], v[68:71], a[64:67]\n"
"ds_read_b128 a[152:155], v223 offset:4096\n"
"v_cvt_pk_bf16_f32 v85, v86, v87\n"
"v_mfma_f32_16x16x32_bf16 a[80:83], a[200:203], v[68:71], a[80:83]\n"
"buffer_load_dwordx4 v227, s[40:43], s82 offen lds\n"
"v_mfma_f32_16x16x32_bf16 a[96:99], a[204:207], v[68:71], a[96:99]\n"
"s_addk_i32 m0, 7176\n"
"ds_read_b128 a[156:159], v222 offset:12288\n"
"v_cvt_pk_bf16_f32 v86, v116, v117\n"
"v_mfma_f32_16x16x32_bf16 a[112:115], a[208:211], v[68:71], a[112:115]\n"
"s_addk_i32 s82, 27648\n"
"ds_read_b128 a[160:163], v223 offset:12288\n"
"v_cvt_pk_bf16_f32 v87, v118, v119\n"
"v_mfma_f32_16x16x32_bf16 a[132:135], a[144:147], v[76:79], a[132:135]\n"
"v_exp_f32 v92, v92\n"
"v_mfma_f32_16x16x32_bf16 a[116:119], a[208:211], v[76:79], a[116:119]\n"
"v_exp_f32 v93, v93\n"
"v_mfma_f32_16x16x32_bf16 a[100:103], a[204:207], v[76:79], a[100:103]\n"
"buffer_load_dwordx4 v227, s[36:39], s82 offen lds\n"
"v_mfma_f32_16x16x32_bf16 a[84:87], a[200:203], v[76:79], a[84:87]\n"
"s_addk_i32 m0, 1024\n"
"v_exp_f32 v94, v94\n"
"v_mfma_f32_16x16x32_bf16 a[68:71], a[196:199], v[76:79], a[68:71]\n"
"v_exp_f32 v95, v95\n"
"v_mfma_f32_16x16x32_bf16 a[52:55], a[192:195], v[76:79], a[52:55]\n"
"v_exp_f32 v124, v124\n"
"v_mfma_f32_16x16x32_bf16 a[36:39], a[188:191], v[76:79], a[36:39]\n"
"v_exp_f32 v125, v125\n"
"v_mfma_f32_16x16x32_bf16 a[20:23], a[184:187], v[76:79], a[20:23]\n"
"buffer_load_dwordx4 v227, s[40:43], s82 offen lds\n"
"v_mfma_f32_16x16x32_bf16 a[4:7], a[180:183], v[76:79], a[4:7]\n"
"v_exp_f32 v126, v126\n"
"v_mfma_f32_16x16x32_bf16 a[136:139], a[144:147], v[84:87], a[136:139]\n"
"v_exp_f32 v127, v127\n"
"v_mfma_f32_16x16x32_bf16 a[8:11], a[180:183], v[84:87], a[8:11]\n"
"ds_read_b128 a[164:167], v222 offset:4608\n"
"v_cvt_pk_bf16_f32 v92, v92, v93\n"
"v_mfma_f32_16x16x32_bf16 a[24:27], a[184:187], v[84:87], a[24:27]\n"
"ds_read_b128 a[168:171], v223 offset:4608\n"
"v_cvt_pk_bf16_f32 v93, v94, v95\n"
"v_mfma_f32_16x16x32_bf16 a[40:43], a[188:191], v[84:87], a[40:43]\n"
"buffer_load_dwordx4 a[228:231], v216, s[68:71], s84 offen offset:1024 sc1\n"
"v_mfma_f32_16x16x32_bf16 a[56:59], a[192:195], v[84:87], a[56:59]\n"
"ds_read_b128 a[172:175], v222 offset:12800\n"
"v_cvt_pk_bf16_f32 v94, v124, v125\n"
"v_mfma_f32_16x16x32_bf16 a[72:75], a[196:199], v[84:87], a[72:75]\n"
"ds_read_b128 a[176:179], v223 offset:12800\n"
"v_cvt_pk_bf16_f32 v95, v126, v127\n"
"v_mfma_f32_16x16x32_bf16 a[88:91], a[200:203], v[84:87], a[88:91]\n"
"v_exp_f32 v4, v4\n"
"v_mfma_f32_16x16x32_bf16 a[104:107], a[204:207], v[84:87], a[104:107]\n"
"v_exp_f32 v5, v5\n"
"v_mfma_f32_16x16x32_bf16 a[120:123], a[208:211], v[84:87], a[120:123]\n"
"buffer_load_dwordx4 a[232:235], v216, s[68:71], s85 offen offset:1024 sc1\n"
"v_mfma_f32_16x16x32_bf16 a[140:143], a[144:147], v[92:95], a[140:143]\n"
"v_exp_f32 v6, v6\n"
"v_mfma_f32_16x16x32_bf16 a[124:127], a[208:211], v[92:95], a[124:127]\n"
"v_exp_f32 v7, v7\n"
"v_mfma_f32_16x16x32_bf16 a[108:111], a[204:207], v[92:95], a[108:111]\n"
"v_exp_f32 v36, v36\n"
"v_mfma_f32_16x16x32_bf16 a[92:95], a[200:203], v[92:95], a[92:95]\n"
"v_exp_f32 v37, v37\n"
"v_mfma_f32_16x16x32_bf16 a[76:79], a[196:199], v[92:95], a[76:79]\n"
"buffer_load_dwordx4 a[236:239], v216, s[68:71], s86 offen offset:1024 sc1\n"
"v_mfma_f32_16x16x32_bf16 a[60:63], a[192:195], v[92:95], a[60:63]\n"
"v_exp_f32 v38, v38\n"
"v_mfma_f32_16x16x32_bf16 a[44:47], a[188:191], v[92:95], a[44:47]\n"
"v_exp_f32 v39, v39\n"
"v_mfma_f32_16x16x32_bf16 a[28:31], a[184:187], v[92:95], a[28:31]\n"
"v_cvt_pk_bf16_f32 v4, v4, v5\n"
"v_cvt_pk_bf16_f32 v5, v6, v7\n"
"v_mfma_f32_16x16x32_bf16 a[12:15], a[180:183], v[92:95], a[12:15]\n"
"s_waitcnt vmcnt(20) lgkmcnt(0)\n"
"s_barrier\n"
"ds_read_b64_tr_b16 a[180:181], v221 offset:16400\n"
"v_mfma_f32_16x16x32_bf16 v[68:71], a[148:151], v[0:3], v[160:163]\n"
"buffer_load_dwordx4 a[240:243], v216, s[68:71], s87 offen offset:1024 sc1\n"
"v_mfma_f32_16x16x32_bf16 v[68:71], a[152:155], v[32:35], v[68:71]\n"
"ds_read_b64_tr_b16 a[182:183], v221 offset:16656\n"
"v_cvt_pk_bf16_f32 v6, v36, v37\n"
"v_mfma_f32_16x16x32_bf16 v[68:71], a[156:159], v[64:67], v[68:71]\n"
"ds_read_b64_tr_b16 a[184:185], v221 offset:16408\n"
"v_cvt_pk_bf16_f32 v7, v38, v39\n"
"v_mfma_f32_16x16x32_bf16 v[68:71], a[160:163], v[96:99], v[68:71]\n"
"ds_read_b64_tr_b16 a[186:187], v221 offset:16664\n"
"v_mfma_f32_16x16x32_bf16 v[100:103], a[164:167], v[0:3], v[160:163]\n"
"ds_read_b64_tr_b16 a[188:189], v221 offset:16912\n"
"v_mfma_f32_16x16x32_bf16 v[100:103], a[168:171], v[32:35], v[100:103]\n"
"ds_read_b64_tr_b16 a[190:191], v221 offset:17168\n"
"v_mfma_f32_16x16x32_bf16 v[100:103], a[172:175], v[64:67], v[100:103]\n"
"ds_read_b64_tr_b16 a[192:193], v221 offset:16920\n"
"v_mfma_f32_16x16x32_bf16 v[100:103], a[176:179], v[96:99], v[100:103]\n"
"ds_read_b64_tr_b16 a[194:195], v221 offset:17176\n"
"v_mfma_f32_16x16x32_bf16 v[76:79], a[148:151], v[8:11], v[164:167]\n"
"ds_read_b64_tr_b16 a[196:197], v221 offset:17424\n"
"v_mfma_f32_16x16x32_bf16 v[76:79], a[152:155], v[40:43], v[76:79]\n"
"ds_read_b64_tr_b16 a[198:199], v221 offset:17680\n"
"v_mfma_f32_16x16x32_bf16 v[76:79], a[156:159], v[72:75], v[76:79]\n"
"ds_read_b64_tr_b16 a[200:201], v221 offset:17432\n"
"v_mfma_f32_16x16x32_bf16 v[76:79], a[160:163], v[104:107], v[76:79]\n"
"ds_read_b64_tr_b16 a[202:203], v221 offset:17688\n"
"v_mfma_f32_16x16x32_bf16 v[108:111], a[164:167], v[8:11], v[164:167]\n"
"ds_read_b64_tr_b16 a[204:205], v221 offset:17936\n"
"v_mfma_f32_16x16x32_bf16 v[108:111], a[168:171], v[40:43], v[108:111]\n"
"ds_read_b64_tr_b16 a[206:207], v221 offset:18192\n"
"v_mfma_f32_16x16x32_bf16 v[108:111], a[172:175], v[72:75], v[108:111]\n"
"ds_read_b64_tr_b16 a[208:209], v221 offset:17944\n"
"v_mfma_f32_16x16x32_bf16 v[108:111], a[176:179], v[104:107], v[108:111]\n"
"ds_read_b64_tr_b16 a[210:211], v221 offset:18200\n"
"v_mfma_f32_16x16x32_bf16 v[84:87], a[148:151], v[16:19], v[168:171]\n"
"v_exp_f32 v12, v12\n"
"v_mfma_f32_16x16x32_bf16 v[84:87], a[152:155], v[48:51], v[84:87]\n"
"v_exp_f32 v13, v13\n"
"v_mfma_f32_16x16x32_bf16 v[84:87], a[156:159], v[80:83], v[84:87]\n"
"v_exp_f32 v14, v14\n"
"v_mfma_f32_16x16x32_bf16 v[84:87], a[160:163], v[112:115], v[84:87]\n"
"v_exp_f32 v15, v15\n"
"v_mfma_f32_16x16x32_bf16 v[116:119], a[164:167], v[16:19], v[168:171]\n"
"v_exp_f32 v44, v44\n"
"v_mfma_f32_16x16x32_bf16 v[116:119], a[168:171], v[48:51], v[116:119]\n"
"v_exp_f32 v45, v45\n"
"v_mfma_f32_16x16x32_bf16 v[116:119], a[172:175], v[80:83], v[116:119]\n"
"v_exp_f32 v46, v46\n"
"v_mfma_f32_16x16x32_bf16 v[116:119], a[176:179], v[112:115], v[116:119]\n"
"v_exp_f32 v47, v47\n"
"v_mfma_f32_16x16x32_bf16 v[92:95], a[148:151], v[24:27], v[172:175]\n"
"v_cvt_pk_bf16_f32 v12, v12, v13\n"
"v_cvt_pk_bf16_f32 v13, v14, v15\n"
"v_mfma_f32_16x16x32_bf16 v[92:95], a[152:155], v[56:59], v[92:95]\n"
"v_cvt_pk_bf16_f32 v14, v44, v45\n"
"v_cvt_pk_bf16_f32 v15, v46, v47\n"
"v_mfma_f32_16x16x32_bf16 v[92:95], a[156:159], v[88:91], v[92:95]\n"
"v_exp_f32 v20, v20\n"
"v_mfma_f32_16x16x32_bf16 v[92:95], a[160:163], v[120:123], v[92:95]\n"
"v_exp_f32 v21, v21\n"
"v_mfma_f32_16x16x32_bf16 v[124:127], a[164:167], v[24:27], v[172:175]\n"
"v_exp_f32 v22, v22\n"
"v_mfma_f32_16x16x32_bf16 v[124:127], a[168:171], v[56:59], v[124:127]\n"
"v_exp_f32 v23, v23\n"
"v_mfma_f32_16x16x32_bf16 v[124:127], a[172:175], v[88:91], v[124:127]\n"
"v_exp_f32 v52, v52\n"
"v_mfma_f32_16x16x32_bf16 v[124:127], a[176:179], v[120:123], v[124:127]\n"
"s_add_u32 m0, s77, 0\n"
"v_exp_f32 v53, v53\n"
"v_mfma_f32_16x16x32_bf16 a[128:131], a[144:147], v[4:7], a[128:131]\n"
"s_waitcnt lgkmcnt(0)\n"
"v_exp_f32 v54, v54\n"
"v_mfma_f32_16x16x32_bf16 a[0:3], a[180:183], v[4:7], a[0:3]\n"
"s_mov_b32 s82, 884736\n"
"buffer_load_dwordx4 v227, s[36:39], s82 offen lds\n"
"v_mfma_f32_16x16x32_bf16 a[16:19], a[184:187], v[4:7], a[16:19]\n"
"s_addk_i32 m0, 1024\n"
"v_exp_f32 v55, v55\n"
"v_mfma_f32_16x16x32_bf16 a[32:35], a[188:191], v[4:7], a[32:35]\n"
"ds_read_b128 a[148:151], v222 offset:16400\n"
"v_cvt_pk_bf16_f32 v20, v20, v21\n"
"v_mfma_f32_16x16x32_bf16 a[48:51], a[192:195], v[4:7], a[48:51]\n"
"ds_read_b128 a[152:155], v223 offset:16400\n"
"v_cvt_pk_bf16_f32 v21, v22, v23\n"
"v_mfma_f32_16x16x32_bf16 a[64:67], a[196:199], v[4:7], a[64:67]\n"
"ds_read_b128 a[156:159], v222 offset:24592\n"
"v_cvt_pk_bf16_f32 v22, v52, v53\n"
"v_mfma_f32_16x16x32_bf16 a[80:83], a[200:203], v[4:7], a[80:83]\n"
"buffer_load_dwordx4 v227, s[40:43], s82 offen lds\n"
"v_mfma_f32_16x16x32_bf16 a[96:99], a[204:207], v[4:7], a[96:99]\n"
"s_addk_i32 m0, 7176\n"
"ds_read_b128 a[160:163], v223 offset:24592\n"
"v_cvt_pk_bf16_f32 v23, v54, v55\n"
"v_mfma_f32_16x16x32_bf16 a[112:115], a[208:211], v[4:7], a[112:115]\n"
"s_addk_i32 s82, 27648\n"
"v_exp_f32 v28, v28\n"
"v_mfma_f32_16x16x32_bf16 a[132:135], a[144:147], v[12:15], a[132:135]\n"
"v_exp_f32 v29, v29\n"
"v_mfma_f32_16x16x32_bf16 a[116:119], a[208:211], v[12:15], a[116:119]\n"
"v_exp_f32 v30, v30\n"
"v_mfma_f32_16x16x32_bf16 a[100:103], a[204:207], v[12:15], a[100:103]\n"
"buffer_load_dwordx4 v227, s[36:39], s82 offen lds\n"
"v_mfma_f32_16x16x32_bf16 a[84:87], a[200:203], v[12:15], a[84:87]\n"
"s_addk_i32 m0, 1024\n"
"v_exp_f32 v31, v31\n"
"v_mfma_f32_16x16x32_bf16 a[68:71], a[196:199], v[12:15], a[68:71]\n"
"v_exp_f32 v60, v60\n"
"v_mfma_f32_16x16x32_bf16 a[52:55], a[192:195], v[12:15], a[52:55]\n"
"v_exp_f32 v61, v61\n"
"v_mfma_f32_16x16x32_bf16 a[36:39], a[188:191], v[12:15], a[36:39]\n"
"v_exp_f32 v62, v62\n"
"v_mfma_f32_16x16x32_bf16 a[20:23], a[184:187], v[12:15], a[20:23]\n"
"buffer_load_dwordx4 v227, s[40:43], s82 offen lds\n"
"v_mfma_f32_16x16x32_bf16 a[4:7], a[180:183], v[12:15], a[4:7]\n"
"L_st8_%=:\n"
"s_cbranch_execz L_st12_%=\n"
"v_exp_f32 v63, v63\n"
"v_mfma_f32_16x16x32_bf16 a[136:139], a[144:147], v[20:23], a[136:139]\n"
"ds_read_b128 a[164:167], v222 offset:16912\n"
"v_cvt_pk_bf16_f32 v28, v28, v29\n"
"v_mfma_f32_16x16x32_bf16 a[8:11], a[180:183], v[20:23], a[8:11]\n"
"ds_read_b128 a[168:171], v223 offset:16912\n"
"v_cvt_pk_bf16_f32 v29, v30, v31\n"
"v_mfma_f32_16x16x32_bf16 a[24:27], a[184:187], v[20:23], a[24:27]\n"
"ds_read_b128 a[172:175], v222 offset:25104\n"
"v_cvt_pk_bf16_f32 v30, v60, v61\n"
"v_mfma_f32_16x16x32_bf16 a[40:43], a[188:191], v[20:23], a[40:43]\n"
"buffer_load_dwordx4 a[244:247], v216, s[68:71], s84 offen offset:2048 sc1\n"
"v_mfma_f32_16x16x32_bf16 a[56:59], a[192:195], v[20:23], a[56:59]\n"
"ds_read_b128 a[176:179], v223 offset:25104\n"
"v_cvt_pk_bf16_f32 v31, v62, v63\n"
"v_mfma_f32_16x16x32_bf16 a[72:75], a[196:199], v[20:23], a[72:75]\n"
"v_exp_f32 v68, v68\n"
"v_mfma_f32_16x16x32_bf16 a[88:91], a[200:203], v[20:23], a[88:91]\n"
"v_exp_f32 v69, v69\n"
"v_mfma_f32_16x16x32_bf16 a[104:107], a[204:207], v[20:23], a[104:107]\n"
"v_exp_f32 v70, v70\n"
"v_mfma_f32_16x16x32_bf16 a[120:123], a[208:211], v[20:23], a[120:123]\n"
"buffer_load_dwordx4 a[248:251], v216, s[68:71], s85 offen offset:2048 sc1\n"
"v_mfma_f32_16x16x32_bf16 a[140:143], a[144:147], v[28:31], a[140:143]\n"
"v_exp_f32 v71, v71\n"
"v_mfma_f32_16x16x32_bf16 a[124:127], a[208:211], v[28:31], a[124:127]\n"
"v_exp_f32 v100, v100\n"
"v_mfma_f32_16x16x32_bf16 a[108:111], a[204:207], v[28:31], a[108:111]\n"
"v_exp_f32 v101, v101\n"
"v_mfma_f32_16x16x32_bf16 a[92:95], a[200:203], v[28:31], a[92:95]\n"
"v_exp_f32 v102, v102\n"
"v_mfma_f32_16x16x32_bf16 a[76:79], a[196:199], v[28:31], a[76:79]\n"
"buffer_load_dwordx4 a[252:255], v216, s[68:71], s86 offen offset:2048 sc1\n"
"v_mfma_f32_16x16x32_bf16 a[60:63], a[192:195], v[28:31], a[60:63]\n"
"v_exp_f32 v103, v103\n"
"v_mfma_f32_16x16x32_bf16 a[44:47], a[188:191], v[28:31], a[44:47]\n"
"v_cvt_pk_bf16_f32 v68, v68, v69\n"
"v_cvt_pk_bf16_f32 v69, v70, v71\n"
"v_mfma_f32_16x16x32_bf16 a[28:31], a[184:187], v[28:31], a[28:31]\n"
"v_cvt_pk_bf16_f32 v70, v100, v101\n"
"v_cvt_pk_bf16_f32 v71, v102, v103\n"
"v_mfma_f32_16x16x32_bf16 a[12:15], a[180:183], v[28:31], a[12:15]\n"
"s_waitcnt lgkmcnt(0)\n"
"ds_read_b64_tr_b16 a[180:181], v221 offset:20496\n"
"v_mfma_f32_16x16x32_bf16 v[4:7], a[148:151], v[0:3], v[160:163]\n"
"buffer_load_dwordx4 v[176:179], v216, s[68:71], s87 offen offset:2048 sc1\n"
"v_mfma_f32_16x16x32_bf16 v[4:7], a[152:155], v[32:35], v[4:7]\n"
"ds_read_b64_tr_b16 a[182:183], v221 offset:20752\n"
"v_mfma_f32_16x16x32_bf16 v[4:7], a[156:159], v[64:67], v[4:7]\n"
"ds_read_b64_tr_b16 a[184:185], v221 offset:20504\n"
"v_mfma_f32_16x16x32_bf16 v[4:7], a[160:163], v[96:99], v[4:7]\n"
"ds_read_b64_tr_b16 a[186:187], v221 offset:20760\n"
"v_mfma_f32_16x16x32_bf16 v[36:39], a[164:167], v[0:3], v[160:163]\n"
"ds_read_b64_tr_b16 a[188:189], v221 offset:21008\n"
"v_mfma_f32_16x16x32_bf16 v[36:39], a[168:171], v[32:35], v[36:39]\n"
"ds_read_b64_tr_b16 a[190:191], v221 offset:21264\n"
"v_mfma_f32_16x16x32_bf16 v[36:39], a[172:175], v[64:67], v[36:39]\n"
"ds_read_b64_tr_b16 a[192:193], v221 offset:21016\n"
"v_mfma_f32_16x16x32_bf16 v[36:39], a[176:179], v[96:99], v[36:39]\n"
"ds_read_b64_tr_b16 a[194:195], v221 offset:21272\n"
"v_mfma_f32_16x16x32_bf16 v[12:15], a[148:151], v[8:11], v[164:167]\n"
"ds_read_b64_tr_b16 a[196:197], v221 offset:21520\n"
"v_mfma_f32_16x16x32_bf16 v[12:15], a[152:155], v[40:43], v[12:15]\n"
"ds_read_b64_tr_b16 a[198:199], v221 offset:21776\n"
"v_mfma_f32_16x16x32_bf16 v[12:15], a[156:159], v[72:75], v[12:15]\n"
"ds_read_b64_tr_b16 a[200:201], v221 offset:21528\n"
"v_mfma_f32_16x16x32_bf16 v[12:15], a[160:163], v[104:107], v[12:15]\n"
"ds_read_b64_tr_b16 a[202:203], v221 offset:21784\n"
"v_mfma_f32_16x16x32_bf16 v[44:47], a[164:167], v[8:11], v[164:167]\n"
"ds_read_b64_tr_b16 a[204:205], v221 offset:22032\n"
"v_mfma_f32_16x16x32_bf16 v[44:47], a[168:171], v[40:43], v[44:47]\n"
"ds_read_b64_tr_b16 a[206:207], v221 offset:22288\n"
"v_mfma_f32_16x16x32_bf16 v[44:47], a[172:175], v[72:75], v[44:47]\n"
"ds_read_b64_tr_b16 a[208:209], v221 offset:22040\n"
"v_mfma_f32_16x16x32_bf16 v[44:47], a[176:179], v[104:107], v[44:47]\n"
"ds_read_b64_tr_b16 a[210:211], v221 offset:22296\n"
"v_mfma_f32_16x16x32_bf16 v[20:23], a[148:151], v[16:19], v[168:171]\n"
"v_exp_f32 v76, v76\n"
"v_mfma_f32_16x16x32_bf16 v[20:23], a[152:155], v[48:51], v[20:23]\n"
"v_exp_f32 v77, v77\n"
"v_mfma_f32_16x16x32_bf16 v[20:23], a[156:159], v[80:83], v[20:23]\n"
"v_exp_f32 v78, v78\n"
"v_mfma_f32_16x16x32_bf16 v[20:23], a[160:163], v[112:115], v[20:23]\n"
"v_exp_f32 v79, v79\n"
"v_mfma_f32_16x16x32_bf16 v[52:55], a[164:167], v[16:19], v[168:171]\n"
"v_exp_f32 v108, v108\n"
"v_mfma_f32_16x16x32_bf16 v[52:55], a[168:171], v[48:51], v[52:55]\n"
"v_exp_f32 v109, v109\n"
"v_mfma_f32_16x16x32_bf16 v[52:55], a[172:175], v[80:83], v[52:55]\n"
"v_exp_f32 v110, v110\n"
"v_mfma_f32_16x16x32_bf16 v[52:55], a[176:179], v[112:115], v[52:55]\n"
"v_exp_f32 v111, v111\n"
"v_mfma_f32_16x16x32_bf16 v[28:31], a[148:151], v[24:27], v[172:175]\n"
"v_cvt_pk_bf16_f32 v76, v76, v77\n"
"v_cvt_pk_bf16_f32 v77, v78, v79\n"
"v_mfma_f32_16x16x32_bf16 v[28:31], a[152:155], v[56:59], v[28:31]\n"
"v_cvt_pk_bf16_f32 v78, v108, v109\n"
"v_cvt_pk_bf16_f32 v79, v110, v111\n"
"v_mfma_f32_16x16x32_bf16 v[28:31], a[156:159], v[88:91], v[28:31]\n"
"v_exp_f32 v84, v84\n"
"v_mfma_f32_16x16x32_bf16 v[28:31], a[160:163], v[120:123], v[28:31]\n"
"v_exp_f32 v85, v85\n"
"v_mfma_f32_16x16x32_bf16 v[60:63], a[164:167], v[24:27], v[172:175]\n"
"v_exp_f32 v86, v86\n"
"v_mfma_f32_16x16x32_bf16 v[60:63], a[168:171], v[56:59], v[60:63]\n"
"v_exp_f32 v87, v87\n"
"v_mfma_f32_16x16x32_bf16 v[60:63], a[172:175], v[88:91], v[60:63]\n"
"v_exp_f32 v116, v116\n"
"v_mfma_f32_16x16x32_bf16 v[60:63], a[176:179], v[120:123], v[60:63]\n"
"s_add_u32 m0, s77, 98400\n"
"v_exp_f32 v117, v117\n"
"v_mfma_f32_16x16x32_bf16 a[128:131], a[144:147], v[68:71], a[128:131]\n"
"s_waitcnt lgkmcnt(0)\n"
"v_exp_f32 v118, v118\n"
"v_mfma_f32_16x16x32_bf16 a[0:3], a[180:183], v[68:71], a[0:3]\n"
"s_mov_b32 s82, 1105920\n"
"buffer_load_dwordx4 v227, s[36:39], s82 offen lds\n"
"v_mfma_f32_16x16x32_bf16 a[16:19], a[184:187], v[68:71], a[16:19]\n"
"s_addk_i32 m0, 1024\n"
"v_exp_f32 v119, v119\n"
"v_mfma_f32_16x16x32_bf16 a[32:35], a[188:191], v[68:71], a[32:35]\n"
"ds_read_b128 a[148:151], v222 offset:20496\n"
"v_cvt_pk_bf16_f32 v84, v84, v85\n"
"v_mfma_f32_16x16x32_bf16 a[48:51], a[192:195], v[68:71], a[48:51]\n"
"ds_read_b128 a[152:155], v223 offset:20496\n"
"v_cvt_pk_bf16_f32 v85, v86, v87\n"
"v_mfma_f32_16x16x32_bf16 a[64:67], a[196:199], v[68:71], a[64:67]\n"
"ds_read_b128 a[156:159], v222 offset:28688\n"
"v_cvt_pk_bf16_f32 v86, v116, v117\n"
"v_mfma_f32_16x16x32_bf16 a[80:83], a[200:203], v[68:71], a[80:83]\n"
"buffer_load_dwordx4 v227, s[40:43], s82 offen lds\n"
"v_mfma_f32_16x16x32_bf16 a[96:99], a[204:207], v[68:71], a[96:99]\n"
"s_addk_i32 m0, 7176\n"
"ds_read_b128 a[160:163], v223 offset:28688\n"
"v_cvt_pk_bf16_f32 v87, v118, v119\n"
"v_mfma_f32_16x16x32_bf16 a[112:115], a[208:211], v[68:71], a[112:115]\n"
"s_addk_i32 s82, 27648\n"
"v_exp_f32 v92, v92\n"
"v_mfma_f32_16x16x32_bf16 a[132:135], a[144:147], v[76:79], a[132:135]\n"
"v_exp_f32 v93, v93\n"
"v_mfma_f32_16x16x32_bf16 a[116:119], a[208:211], v[76:79], a[116:119]\n"
"v_exp_f32 v94, v94\n"
"v_mfma_f32_16x16x32_bf16 a[100:103], a[204:207], v[76:79], a[100:103]\n"
"buffer_load_dwordx4 v227, s[36:39], s82 offen lds\n"
"v_mfma_f32_16x16x32_bf16 a[84:87], a[200:203], v[76:79], a[84:87]\n"
"s_addk_i32 m0, 1024\n"
"v_exp_f32 v95, v95\n"
"v_mfma_f32_16x16x32_bf16 a[68:71], a[196:199], v[76:79], a[68:71]\n"
"v_exp_f32 v124, v124\n"
"v_mfma_f32_16x16x32_bf16 a[52:55], a[192:195], v[76:79], a[52:55]\n"
"v_exp_f32 v125, v125\n"
"v_mfma_f32_16x16x32_bf16 a[36:39], a[188:191], v[76:79], a[36:39]\n"
"v_exp_f32 v126, v126\n"
"v_mfma_f32_16x16x32_bf16 a[20:23], a[184:187], v[76:79], a[20:23]\n"
"buffer_load_dwordx4 v227, s[40:43], s82 offen lds\n"
"v_mfma_f32_16x16x32_bf16 a[4:7], a[180:183], v[76:79], a[4:7]\n"
"v_exp_f32 v127, v127\n"
"v_mfma_f32_16x16x32_bf16 a[136:139], a[144:147], v[84:87], a[136:139]\n"
"ds_read_b128 a[164:167], v222 offset:21008\n"
"v_cvt_pk_bf16_f32 v92, v92, v93\n"
"v_mfma_f32_16x16x32_bf16 a[8:11], a[180:183], v[84:87], a[8:11]\n"
"ds_read_b128 a[168:171], v223 offset:21008\n"
"v_cvt_pk_bf16_f32 v93, v94, v95\n"
"v_mfma_f32_16x16x32_bf16 a[24:27], a[184:187], v[84:87], a[24:27]\n"
"ds_read_b128 a[172:175], v222 offset:29200\n"
"v_cvt_pk_bf16_f32 v94, v124, v125\n"
"v_mfma_f32_16x16x32_bf16 a[40:43], a[188:191], v[84:87], a[40:43]\n"
"buffer_load_dwordx4 v[180:183], v216, s[68:71], s84 offen offset:3072 sc1\n"
"v_mfma_f32_16x16x32_bf16 a[56:59], a[192:195], v[84:87], a[56:59]\n"
"s_addk_i32 s84, 4096\n"
"ds_read_b128 a[176:179], v223 offset:29200\n"
"v_cvt_pk_bf16_f32 v95, v126, v127\n"
"v_mfma_f32_16x16x32_bf16 a[72:75], a[196:199], v[84:87], a[72:75]\n"
"v_exp_f32 v4, v4\n"
"v_mfma_f32_16x16x32_bf16 a[88:91], a[200:203], v[84:87], a[88:91]\n"
"v_exp_f32 v5, v5\n"
"v_mfma_f32_16x16x32_bf16 a[104:107], a[204:207], v[84:87], a[104:107]\n"
"v_exp_f32 v6, v6\n"
"v_mfma_f32_16x16x32_bf16 a[120:123], a[208:211], v[84:87], a[120:123]\n"
"buffer_load_dwordx4 v[184:187], v216, s[68:71], s85 offen offset:3072 sc1\n"
"v_mfma_f32_16x16x32_bf16 a[140:143], a[144:147], v[92:95], a[140:143]\n"
"s_addk_i32 s85, 4096\n"
"v_exp_f32 v7, v7\n"
"v_mfma_f32_16x16x32_bf16 a[124:127], a[208:211], v[92:95], a[124:127]\n"
"v_exp_f32 v36, v36\n"
"v_mfma_f32_16x16x32_bf16 a[108:111], a[204:207], v[92:95], a[108:111]\n"
"v_exp_f32 v37, v37\n"
"v_mfma_f32_16x16x32_bf16 a[92:95], a[200:203], v[92:95], a[92:95]\n"
"v_exp_f32 v38, v38\n"
"v_mfma_f32_16x16x32_bf16 a[76:79], a[196:199], v[92:95], a[76:79]\n"
"buffer_load_dwordx4 v[188:191], v216, s[68:71], s86 offen offset:3072 sc1\n"
"v_mfma_f32_16x16x32_bf16 a[60:63], a[192:195], v[92:95], a[60:63]\n"
"s_addk_i32 s86, 4096\n"
"v_exp_f32 v39, v39\n"
"v_mfma_f32_16x16x32_bf16 a[44:47], a[188:191], v[92:95], a[44:47]\n"
"v_cvt_pk_bf16_f32 v4, v4, v5\n"
"v_cvt_pk_bf16_f32 v5, v6, v7\n"
"v_mfma_f32_16x16x32_bf16 a[28:31], a[184:187], v[92:95], a[28:31]\n"
"v_cvt_pk_bf16_f32 v6, v36, v37\n"
"v_cvt_pk_bf16_f32 v7, v38, v39\n"
"v_mfma_f32_16x16x32_bf16 a[12:15], a[180:183], v[92:95], a[12:15]\n"
"s_waitcnt vmcnt(27) lgkmcnt(0)\n"
"s_barrier\n"
"ds_read_b64_tr_b16 a[180:181], v221 offset:32800\n"
"v_mfma_f32_16x16x32_bf16 v[68:71], a[148:151], v[0:3], v[160:163]\n"
"buffer_load_dwordx4 v[192:195], v216, s[68:71], s87 offen offset:3072 sc1\n"
"v_mfma_f32_16x16x32_bf16 v[68:71], a[152:155], v[32:35], v[68:71]\n"
"s_addk_i32 s87, 4096\n"
"ds_read_b64_tr_b16 a[182:183], v221 offset:33056\n"
"v_mfma_f32_16x16x32_bf16 v[68:71], a[156:159], v[64:67], v[68:71]\n"
"ds_read_b64_tr_b16 a[184:185], v221 offset:32808\n"
"v_mfma_f32_16x16x32_bf16 v[68:71], a[160:163], v[96:99], v[68:71]\n"
"ds_read_b64_tr_b16 a[186:187], v221 offset:33064\n"
"v_mfma_f32_16x16x32_bf16 v[100:103], a[164:167], v[0:3], v[160:163]\n"
"ds_read_b64_tr_b16 a[188:189], v221 offset:33312\n"
"v_mfma_f32_16x16x32_bf16 v[100:103], a[168:171], v[32:35], v[100:103]\n"
"buffer_load_dwordx4 v[196:199], v216, s[68:71], s84 offen sc1\n"
"v_mfma_f32_16x16x32_bf16 v[100:103], a[172:175], v[64:67], v[100:103]\n"
"ds_read_b64_tr_b16 a[190:191], v221 offset:33568\n"
"v_mfma_f32_16x16x32_bf16 v[100:103], a[176:179], v[96:99], v[100:103]\n"
"ds_read_b64_tr_b16 a[192:193], v221 offset:33320\n"
"v_mfma_f32_16x16x32_bf16 v[76:79], a[148:151], v[8:11], v[164:167]\n"
"ds_read_b64_tr_b16 a[194:195], v221 offset:33576\n"
"v_mfma_f32_16x16x32_bf16 v[76:79], a[152:155], v[40:43], v[76:79]\n"
"ds_read_b64_tr_b16 a[196:197], v221 offset:33824\n"
"v_mfma_f32_16x16x32_bf16 v[76:79], a[156:159], v[72:75], v[76:79]\n"
"buffer_load_dwordx4 v[200:203], v216, s[68:71], s85 offen sc1\n"
"v_mfma_f32_16x16x32_bf16 v[76:79], a[160:163], v[104:107], v[76:79]\n"
"ds_read_b64_tr_b16 a[198:199], v221 offset:34080\n"
"v_mfma_f32_16x16x32_bf16 v[108:111], a[164:167], v[8:11], v[164:167]\n"
"ds_read_b64_tr_b16 a[200:201], v221 offset:33832\n"
"v_mfma_f32_16x16x32_bf16 v[108:111], a[168:171], v[40:43], v[108:111]\n"
"ds_read_b64_tr_b16 a[202:203], v221 offset:34088\n"
"v_mfma_f32_16x16x32_bf16 v[108:111], a[172:175], v[72:75], v[108:111]\n"
"ds_read_b64_tr_b16 a[204:205], v221 offset:34336\n"
"v_mfma_f32_16x16x32_bf16 v[108:111], a[176:179], v[104:107], v[108:111]\n"
"buffer_load_dwordx4 v[204:207], v216, s[68:71], s86 offen sc1\n"
"v_mfma_f32_16x16x32_bf16 v[84:87], a[148:151], v[16:19], v[168:171]\n"
"ds_read_b64_tr_b16 a[206:207], v221 offset:34592\n"
"v_mfma_f32_16x16x32_bf16 v[84:87], a[152:155], v[48:51], v[84:87]\n"
"ds_read_b64_tr_b16 a[208:209], v221 offset:34344\n"
"v_mfma_f32_16x16x32_bf16 v[84:87], a[156:159], v[80:83], v[84:87]\n"
"ds_read_b64_tr_b16 a[210:211], v221 offset:34600\n"
"v_mfma_f32_16x16x32_bf16 v[84:87], a[160:163], v[112:115], v[84:87]\n"
"v_exp_f32 v12, v12\n"
"v_mfma_f32_16x16x32_bf16 v[116:119], a[164:167], v[16:19], v[168:171]\n"
"buffer_load_dwordx4 v[208:211], v216, s[68:71], s87 offen sc1\n"
"v_mfma_f32_16x16x32_bf16 v[116:119], a[168:171], v[48:51], v[116:119]\n"
"v_exp_f32 v13, v13\n"
"v_mfma_f32_16x16x32_bf16 v[116:119], a[172:175], v[80:83], v[116:119]\n"
"v_exp_f32 v14, v14\n"
"v_mfma_f32_16x16x32_bf16 v[116:119], a[176:179], v[112:115], v[116:119]\n"
"v_exp_f32 v15, v15\n"
"v_mfma_f32_16x16x32_bf16 v[92:95], a[148:151], v[24:27], v[172:175]\n"
"v_exp_f32 v44, v44\n"
"v_mfma_f32_16x16x32_bf16 v[92:95], a[152:155], v[56:59], v[92:95]\n"
"v_exp_f32 v45, v45\n"
"v_mfma_f32_16x16x32_bf16 v[92:95], a[156:159], v[88:91], v[92:95]\n"
"v_exp_f32 v46, v46\n"
"v_mfma_f32_16x16x32_bf16 v[92:95], a[160:163], v[120:123], v[92:95]\n"
"v_exp_f32 v47, v47\n"
"v_mfma_f32_16x16x32_bf16 v[124:127], a[164:167], v[24:27], v[172:175]\n"
"v_cvt_pk_bf16_f32 v12, v12, v13\n"
"v_cvt_pk_bf16_f32 v13, v14, v15\n"
"v_mfma_f32_16x16x32_bf16 v[124:127], a[168:171], v[56:59], v[124:127]\n"
"v_cvt_pk_bf16_f32 v14, v44, v45\n"
"v_cvt_pk_bf16_f32 v15, v46, v47\n"
"v_mfma_f32_16x16x32_bf16 v[124:127], a[172:175], v[88:91], v[124:127]\n"
"v_exp_f32 v20, v20\n"
"v_mfma_f32_16x16x32_bf16 v[124:127], a[176:179], v[120:123], v[124:127]\n"
"s_add_u32 m0, s77, 16400\n"
"v_exp_f32 v21, v21\n"
"v_mfma_f32_16x16x32_bf16 a[128:131], a[144:147], v[4:7], a[128:131]\n"
"s_waitcnt lgkmcnt(0)\n"
"v_exp_f32 v22, v22\n"
"v_mfma_f32_16x16x32_bf16 a[0:3], a[180:183], v[4:7], a[0:3]\n"
"s_mov_b32 s82, 1327104\n"
"buffer_load_dwordx4 v227, s[36:39], s82 offen lds\n"
"v_mfma_f32_16x16x32_bf16 a[16:19], a[184:187], v[4:7], a[16:19]\n"
"s_addk_i32 m0, 1024\n"
"v_exp_f32 v23, v23\n"
"v_mfma_f32_16x16x32_bf16 a[32:35], a[188:191], v[4:7], a[32:35]\n"
"v_exp_f32 v52, v52\n"
"v_mfma_f32_16x16x32_bf16 a[48:51], a[192:195], v[4:7], a[48:51]\n"
"v_exp_f32 v53, v53\n"
"v_mfma_f32_16x16x32_bf16 a[64:67], a[196:199], v[4:7], a[64:67]\n"
"v_exp_f32 v54, v54\n"
"v_mfma_f32_16x16x32_bf16 a[80:83], a[200:203], v[4:7], a[80:83]\n"
"buffer_load_dwordx4 v227, s[40:43], s82 offen lds\n"
"v_mfma_f32_16x16x32_bf16 a[96:99], a[204:207], v[4:7], a[96:99]\n"
"s_addk_i32 m0, 7176\n"
"v_exp_f32 v55, v55\n"
"v_mfma_f32_16x16x32_bf16 a[112:115], a[208:211], v[4:7], a[112:115]\n"
"s_addk_i32 s82, 27648\n"
"ds_read_b64_tr_b16 a[148:149], v221 offset:36896\n"
"v_cvt_pk_bf16_f32 v20, v20, v21\n"
"v_mfma_f32_16x16x32_bf16 a[132:135], a[144:147], v[12:15], a[132:135]\n"
"ds_read_b64_tr_b16 a[150:151], v221 offset:37152\n"
"v_cvt_pk_bf16_f32 v21, v22, v23\n"
"v_mfma_f32_16x16x32_bf16 a[116:119], a[208:211], v[12:15], a[116:119]\n"
"ds_read_b64_tr_b16 a[152:153], v221 offset:36904\n"
"v_cvt_pk_bf16_f32 v22, v52, v53\n"
"v_mfma_f32_16x16x32_bf16 a[100:103], a[204:207], v[12:15], a[100:103]\n"
"buffer_load_dwordx4 v227, s[36:39], s82 offen lds\n"
"v_mfma_f32_16x16x32_bf16 a[84:87], a[200:203], v[12:15], a[84:87]\n"
"s_addk_i32 m0, 1024\n"
"ds_read_b64_tr_b16 a[154:155], v221 offset:37160\n"
"v_cvt_pk_bf16_f32 v23, v54, v55\n"
"v_mfma_f32_16x16x32_bf16 a[68:71], a[196:199], v[12:15], a[68:71]\n"
"v_exp_f32 v28, v28\n"
"v_mfma_f32_16x16x32_bf16 a[52:55], a[192:195], v[12:15], a[52:55]\n"
"v_exp_f32 v29, v29\n"
"v_mfma_f32_16x16x32_bf16 a[36:39], a[188:191], v[12:15], a[36:39]\n"
"v_exp_f32 v30, v30\n"
"v_mfma_f32_16x16x32_bf16 a[20:23], a[184:187], v[12:15], a[20:23]\n"
"buffer_load_dwordx4 v227, s[40:43], s82 offen lds\n"
"v_mfma_f32_16x16x32_bf16 a[4:7], a[180:183], v[12:15], a[4:7]\n"
"v_exp_f32 v31, v31\n"
"v_mfma_f32_16x16x32_bf16 a[136:139], a[144:147], v[20:23], a[136:139]\n"
"v_exp_f32 v60, v60\n"
"v_mfma_f32_16x16x32_bf16 a[8:11], a[180:183], v[20:23], a[8:11]\n"
"v_exp_f32 v61, v61\n"
"v_mfma_f32_16x16x32_bf16 a[24:27], a[184:187], v[20:23], a[24:27]\n"
"v_exp_f32 v62, v62\n"
"v_mfma_f32_16x16x32_bf16 a[40:43], a[188:191], v[20:23], a[40:43]\n"
"v_exp_f32 v63, v63\n"
"v_mfma_f32_16x16x32_bf16 a[56:59], a[192:195], v[20:23], a[56:59]\n"
"buffer_load_dwordx4 v[128:131], v216, s[68:71], s84 offen offset:1024 sc1\n"
"v_mfma_f32_16x16x32_bf16 a[72:75], a[196:199], v[20:23], a[72:75]\n"
"ds_read_b64_tr_b16 a[156:157], v221 offset:37408\n"
"v_cvt_pk_bf16_f32 v28, v28, v29\n"
"v_mfma_f32_16x16x32_bf16 a[88:91], a[200:203], v[20:23], a[88:91]\n"
"ds_read_b64_tr_b16 a[158:159], v221 offset:37664\n"
"v_cvt_pk_bf16_f32 v29, v30, v31\n"
"v_cvt_pk_bf16_f32 v30, v60, v61\n"
"v_cvt_pk_bf16_f32 v31, v62, v63\n"
"v_mfma_f32_16x16x32_bf16 a[104:107], a[204:207], v[20:23], a[104:107]\n"
"ds_read_b64_tr_b16 a[160:161], v221 offset:37416\n"
"v_mfma_f32_16x16x32_bf16 a[120:123], a[208:211], v[20:23], a[120:123]\n"
"ds_read_b64_tr_b16 a[162:163], v221 offset:37672\n"
"v_mfma_f32_16x16x32_bf16 a[140:143], a[144:147], v[28:31], a[140:143]\n"
"buffer_load_dwordx4 v[132:135], v216, s[68:71], s85 offen offset:1024 sc1\n"
"v_mfma_f32_16x16x32_bf16 a[124:127], a[208:211], v[28:31], a[124:127]\n"
"ds_read_b64_tr_b16 a[164:165], v221 offset:37920\n"
"v_mfma_f32_16x16x32_bf16 a[108:111], a[204:207], v[28:31], a[108:111]\n"
"ds_read_b64_tr_b16 a[166:167], v221 offset:38176\n"
"v_mfma_f32_16x16x32_bf16 a[92:95], a[200:203], v[28:31], a[92:95]\n"
"v_exp_f32 v68, v68\n"
"v_mfma_f32_16x16x32_bf16 a[76:79], a[196:199], v[28:31], a[76:79]\n"
"v_exp_f32 v69, v69\n"
"v_mfma_f32_16x16x32_bf16 a[60:63], a[192:195], v[28:31], a[60:63]\n"
"ds_read_b64_tr_b16 a[168:169], v221 offset:37928\n"
"v_exp_f32 v70, v70\n"
"v_exp_f32 v71, v71\n"
"v_exp_f32 v100, v100\n"
"v_exp_f32 v101, v101\n"
"v_exp_f32 v102, v102\n"
"v_exp_f32 v103, v103\n"
"ds_read_b64_tr_b16 a[170:171], v221 offset:38184\n"
"v_mfma_f32_16x16x32_bf16 a[44:47], a[188:191], v[28:31], a[44:47]\n"
"ds_read_b64_tr_b16 a[172:173], v221 offset:38432\n"
"v_cvt_pk_bf16_f32 v68, v68, v69\n"
"v_cvt_pk_bf16_f32 v69, v70, v71\n"
"v_cvt_pk_bf16_f32 v70, v100, v101\n"
"v_cvt_pk_bf16_f32 v71, v102, v103\n"
"ds_read_b64_tr_b16 a[174:175], v221 offset:38688\n"
"v_mfma_f32_16x16x32_bf16 a[28:31], a[184:187], v[28:31], a[28:31]\n"
"ds_read_b64_tr_b16 a[176:177], v221 offset:38440\n"
"ds_read_b64_tr_b16 a[178:179], v221 offset:38696\n"
"v_mfma_f32_16x16x32_bf16 a[12:15], a[180:183], v[28:31], a[12:15]\n"
"s_add_u32 m0, s77, 114800\n"
"buffer_load_dwordx4 v[136:139], v216, s[68:71], s86 offen offset:1024 sc1\n"
"v_mfma_f32_16x16x32_bf16 a[128:131], a[144:147], v[68:71], a[128:131]\n"
"s_waitcnt lgkmcnt(10)\n"
"v_exp_f32 v76, v76\n"
"v_mfma_f32_16x16x32_bf16 a[0:3], a[148:151], v[68:71], a[0:3]\n"
"s_mov_b32 s82, 1548288\n"
"v_exp_f32 v77, v77\n"
"v_mfma_f32_16x16x32_bf16 a[16:19], a[152:155], v[68:71], a[16:19]\n"
"v_exp_f32 v78, v78\n"
"v_mfma_f32_16x16x32_bf16 a[32:35], a[156:159], v[68:71], a[32:35]\n"
"s_waitcnt lgkmcnt(8)\n"
"v_exp_f32 v79, v79\n"
"v_mfma_f32_16x16x32_bf16 a[48:51], a[160:163], v[68:71], a[48:51]\n"
"s_waitcnt lgkmcnt(6)\n"
"v_exp_f32 v108, v108\n"
"v_mfma_f32_16x16x32_bf16 a[64:67], a[164:167], v[68:71], a[64:67]\n"
"s_waitcnt lgkmcnt(4)\n"
"v_exp_f32 v109, v109\n"
"v_exp_f32 v110, v110\n"
"v_exp_f32 v111, v111\n"
"v_mfma_f32_16x16x32_bf16 a[80:83], a[168:171], v[68:71], a[80:83]\n"
"s_waitcnt lgkmcnt(2)\n"
"buffer_load_dwordx4 v[140:143], v216, s[68:71], s87 offen offset:1024 sc1\n"
"v_cvt_pk_bf16_f32 v76, v76, v77\n"
"v_cvt_pk_bf16_f32 v77, v78, v79\n"
"v_cvt_pk_bf16_f32 v78, v108, v109\n"
"v_cvt_pk_bf16_f32 v79, v110, v111\n"
"v_mfma_f32_16x16x32_bf16 a[96:99], a[172:175], v[68:71], a[96:99]\n"
"s_waitcnt lgkmcnt(0)\n"
"v_exp_f32 v84, v84\n"
"v_mfma_f32_16x16x32_bf16 a[112:115], a[176:179], v[68:71], a[112:115]\n"
"v_exp_f32 v85, v85\n"
"v_mfma_f32_16x16x32_bf16 a[132:135], a[144:147], v[76:79], a[132:135]\n"
"v_exp_f32 v86, v86\n"
"v_mfma_f32_16x16x32_bf16 a[116:119], a[176:179], v[76:79], a[116:119]\n"
"v_exp_f32 v87, v87\n"
"v_mfma_f32_16x16x32_bf16 a[100:103], a[172:175], v[76:79], a[100:103]\n"
"buffer_load_dwordx4 v227, s[36:39], s82 offen lds\n"
"v_mfma_f32_16x16x32_bf16 a[84:87], a[168:171], v[76:79], a[84:87]\n"
"s_addk_i32 m0, 1024\n"
"v_exp_f32 v116, v116\n"
"v_mfma_f32_16x16x32_bf16 a[68:71], a[164:167], v[76:79], a[68:71]\n"
"v_exp_f32 v117, v117\n"
"v_mfma_f32_16x16x32_bf16 a[52:55], a[160:163], v[76:79], a[52:55]\n"
"v_exp_f32 v118, v118\n"
"v_exp_f32 v119, v119\n"
"v_mfma_f32_16x16x32_bf16 a[36:39], a[156:159], v[76:79], a[36:39]\n"
"ds_read_b64_tr_b16 a[180:181], v220 offset:32800\n"
"v_cvt_pk_bf16_f32 v84, v84, v85\n"
"v_cvt_pk_bf16_f32 v85, v86, v87\n"
"v_cvt_pk_bf16_f32 v86, v116, v117\n"
"v_cvt_pk_bf16_f32 v87, v118, v119\n"
"v_mfma_f32_16x16x32_bf16 a[20:23], a[152:155], v[76:79], a[20:23]\n"
"buffer_load_dwordx4 v227, s[40:43], s82 offen lds\n"
"v_mfma_f32_16x16x32_bf16 a[4:7], a[148:151], v[76:79], a[4:7]\n"
"s_addk_i32 m0, 7176\n"
"v_exp_f32 v92, v92\n"
"v_mfma_f32_16x16x32_bf16 a[136:139], a[144:147], v[84:87], a[136:139]\n"
"s_addk_i32 s82, 27648\n"
"v_exp_f32 v93, v93\n"
"v_mfma_f32_16x16x32_bf16 a[8:11], a[148:151], v[84:87], a[8:11]\n"
"v_exp_f32 v94, v94\n"
"v_mfma_f32_16x16x32_bf16 a[24:27], a[152:155], v[84:87], a[24:27]\n"
"v_exp_f32 v95, v95\n"
"v_mfma_f32_16x16x32_bf16 a[40:43], a[156:159], v[84:87], a[40:43]\n"
"v_exp_f32 v124, v124\n"
"v_mfma_f32_16x16x32_bf16 a[56:59], a[160:163], v[84:87], a[56:59]\n"
"v_exp_f32 v125, v125\n"
"v_mfma_f32_16x16x32_bf16 a[72:75], a[164:167], v[84:87], a[72:75]\n"
"buffer_load_dwordx4 v227, s[36:39], s82 offen lds\n"
"v_exp_f32 v126, v126\n"
"v_exp_f32 v127, v127\n"
"v_mfma_f32_16x16x32_bf16 a[88:91], a[168:171], v[84:87], a[88:91]\n"
"s_addk_i32 m0, 1024\n"
"ds_read_b64_tr_b16 a[182:183], v220 offset:33056\n"
"v_cvt_pk_bf16_f32 v92, v92, v93\n"
"v_cvt_pk_bf16_f32 v93, v94, v95\n"
"v_cvt_pk_bf16_f32 v94, v124, v125\n"
"v_cvt_pk_bf16_f32 v95, v126, v127\n"
"v_mfma_f32_16x16x32_bf16 a[104:107], a[172:175], v[84:87], a[104:107]\n"
"ds_read_b64_tr_b16 a[184:185], v220 offset:36896\n"
"v_accvgpr_read_b32 v234, a128\n"
"v_mfma_f32_16x16x32_bf16 a[120:123], a[176:179], v[84:87], a[120:123]\n"
"ds_read_b64_tr_b16 a[186:187], v220 offset:37152\n"
"v_accvgpr_read_b32 v235, a132\n"
"v_mfma_f32_16x16x32_bf16 a[140:143], a[144:147], v[92:95], a[140:143]\n"
"ds_read_b64_tr_b16 a[188:189], v220 offset:32808\n"
"v_add_f32 v248, v234, v235\n"
"v_mfma_f32_16x16x32_bf16 a[124:127], a[176:179], v[92:95], a[124:127]\n"
"buffer_load_dwordx4 v227, s[40:43], s82 offen lds\n"
"v_mfma_f32_16x16x32_bf16 a[108:111], a[172:175], v[92:95], a[108:111]\n"
"ds_read_b64_tr_b16 a[190:191], v220 offset:33064\n"
"v_accvgpr_read_b32 v236, a136\n"
"v_mfma_f32_16x16x32_bf16 a[92:95], a[168:171], v[92:95], a[92:95]\n"
"ds_read_b64_tr_b16 a[192:193], v220 offset:36904\n"
"v_add_f32 v248, v248, v236\n"
"v_mfma_f32_16x16x32_bf16 a[76:79], a[164:167], v[92:95], a[76:79]\n"
"ds_read_b64_tr_b16 a[194:195], v220 offset:37160\n"
"v_accvgpr_read_b32 v237, a140\n"
"v_mfma_f32_16x16x32_bf16 a[60:63], a[160:163], v[92:95], a[60:63]\n"
"ds_read_b64_tr_b16 a[196:197], v220 offset:33824\n"
"v_add_f32 v248, v248, v237\n"
"v_mfma_f32_16x16x32_bf16 a[44:47], a[156:159], v[92:95], a[44:47]\n"
"buffer_load_dwordx4 v[144:147], v216, s[68:71], s84 offen offset:2048 sc1\n"
"v_mfma_f32_16x16x32_bf16 a[28:31], a[152:155], v[92:95], a[28:31]\n"
"ds_read_b64_tr_b16 a[198:199], v220 offset:34080\n"
"v_cmp_nge_f32 vcc, 0x53800000, v248\n"
"v_mfma_f32_16x16x32_bf16 a[12:15], a[148:151], v[92:95], a[12:15]\n"
"s_waitcnt vmcnt(44) lgkmcnt(8)\n"
"s_cbranch_vccnz L_slow_%=\n"
"L_slowret_%=:\n"
"v_mfma_f32_16x16x32_bf16 v[0:3], a[180:183], a[212:215], 0\n"
"s_waitcnt vmcnt(36) lgkmcnt(6)\n"
"ds_read_b64_tr_b16 a[200:201], v220 offset:37920\n"
"s_xor_b32 s92, s26, 0\n"
"v_mfma_f32_16x16x32_bf16 v[0:3], a[184:187], a[228:231], v[0:3]\n"
"s_waitcnt vmcnt(35)\n"
"ds_read_b64_tr_b16 a[202:203], v220 offset:38176\n"
"s_mul_i32 s92, s92, 133\n"
"v_mfma_f32_16x16x32_bf16 v[8:11], a[184:187], a[232:235], 0\n"
"s_lshl_b32 s93, s13, 2\n"
"ds_read_b64_tr_b16 a[204:205], v220 offset:33832\n"
"s_add_u32 s92, s92, s93\n"
"v_mfma_f32_16x16x32_bf16 v[8:11], a[180:183], a[216:219], v[8:11]\n"
"s_lshl_b32 s92, s92, 10\n"
"buffer_load_dwordx4 v[148:151], v216, s[68:71], s85 offen offset:2048 sc1\n"
"v_mfma_f32_16x16x32_bf16 v[16:19], a[180:183], a[220:223], 0\n"
"s_waitcnt vmcnt(35) lgkmcnt(8)\n"
"ds_read_b64_tr_b16 a[206:207], v220 offset:34088\n"
"s_add_u32 s88, s92, 3932160\n"
"v_mfma_f32_16x16x32_bf16 v[16:19], a[184:187], a[236:239], v[16:19]\n"
"s_waitcnt vmcnt(34) lgkmcnt(8)\n"
"ds_read_b64_tr_b16 a[208:209], v220 offset:37928\n"
"v_accvgpr_read_b32 v160, a0\n"
"v_mfma_f32_16x16x32_bf16 v[24:27], a[184:187], a[240:243], 0\n"
"s_xor_b32 s92, s26, 1\n"
"ds_read_b64_tr_b16 a[210:211], v220 offset:38184\n"
"v_accvgpr_read_b32 v161, a1\n"
"v_mfma_f32_16x16x32_bf16 v[24:27], a[180:183], a[224:227], v[24:27]\n"
"s_mul_i32 s92, s92, 133\n"
"ds_read_b64_tr_b16 a[148:149], v220 offset:33312\n"
"v_accvgpr_read_b32 v162, a2\n"
"v_mfma_f32_16x16x32_bf16 v[28:31], a[188:191], a[224:227], 0\n"
"s_waitcnt lgkmcnt(8)\n"
"ds_read_b64_tr_b16 a[150:151], v220 offset:33568\n"
"v_accvgpr_read_b32 v163, a3\n"
"v_mfma_f32_16x16x32_bf16 v[28:31], a[192:195], a[240:243], v[28:31]\n"
"s_lshl_b32 s93, s13, 2\n"
"buffer_load_dwordx4 v[152:155], v216, s[68:71], s86 offen offset:2048 sc1\n"
"v_mfma_f32_16x16x32_bf16 v[20:23], a[192:195], a[236:239], 0\n"
"s_add_u32 s92, s92, s93\n"
"ds_read_b64_tr_b16 a[152:153], v220 offset:37408\n"
"v_accvgpr_read_b32 v164, a16\n"
"v_mfma_f32_16x16x32_bf16 v[20:23], a[188:191], a[220:223], v[20:23]\n"
"s_lshl_b32 s92, s92, 10\n"
"ds_read_b64_tr_b16 a[154:155], v220 offset:37664\n"
"v_accvgpr_read_b32 v165, a17\n"
"v_mfma_f32_16x16x32_bf16 v[12:15], a[188:191], a[216:219], 0\n"
"s_add_u32 s89, s92, 3932160\n"
"v_rcp_f32 v234, v234\n"
"v_mfma_f32_16x16x32_bf16 v[12:15], a[192:195], a[232:235], v[12:15]\n"
"s_xor_b32 s92, s26, 2\n"
"ds_read_b64_tr_b16 a[156:157], v220 offset:33320\n"
"v_accvgpr_read_b32 v166, a18\n"
"v_mfma_f32_16x16x32_bf16 v[4:7], a[192:195], a[228:231], 0\n"
"s_mul_i32 s92, s92, 133\n"
"ds_read_b64_tr_b16 a[158:159], v220 offset:33576\n"
"v_accvgpr_read_b32 v167, a19\n"
"v_mfma_f32_16x16x32_bf16 v[4:7], a[188:191], a[212:215], v[4:7]\n"
"s_waitcnt lgkmcnt(9)\n"
"buffer_load_dwordx4 v[156:159], v216, s[68:71], s87 offen offset:2048 sc1\n"
"v_mfma_f32_16x16x32_bf16 v[64:67], a[196:199], a[212:215], 0\n"
"s_lshl_b32 s93, s13, 2\n"
"ds_read_b64_tr_b16 a[160:161], v220 offset:37416\n"
"v_mul_f32 v160, v234, v160\n"
"v_mfma_f32_16x16x32_bf16 v[64:67], a[200:203], a[228:231], v[64:67]\n"
"s_add_u32 s92, s92, s93\n"
"ds_read_b64_tr_b16 a[162:163], v220 offset:37672\n"
"v_mul_f32 v161, v234, v161\n"
"v_mfma_f32_16x16x32_bf16 v[72:75], a[200:203], a[232:235], 0\n"
"s_lshl_b32 s92, s92, 10\n"
"ds_read_b64_tr_b16 a[164:165], v220 offset:34336\n"
"v_mul_f32 v162, v234, v162\n"
"v_mfma_f32_16x16x32_bf16 v[72:75], a[196:199], a[216:219], v[72:75]\n"
"s_add_u32 s90, s92, 3932160\n"
"ds_read_b64_tr_b16 a[166:167], v220 offset:34592\n"
"v_mul_f32 v163, v234, v163\n"
"v_mfma_f32_16x16x32_bf16 v[80:83], a[196:199], a[220:223], 0\n"
"s_xor_b32 s92, s26, 3\n"
"ds_read_b64_tr_b16 a[168:169], v220 offset:38432\n"
"v_mul_f32 v164, v234, v164\n"
"v_mfma_f32_16x16x32_bf16 v[80:83], a[200:203], a[236:239], v[80:83]\n"
"s_mul_i32 s92, s92, 133\n"
"ds_read_b64_tr_b16 a[170:171], v220 offset:38688\n"
"v_mul_f32 v165, v234, v165\n"
"v_mfma_f32_16x16x32_bf16 v[88:91], a[200:203], a[240:243], 0\n"
"s_lshl_b32 s93, s13, 2\n"
"ds_read_b64_tr_b16 a[172:173], v220 offset:34344\n"
"v_mul_f32 v166, v234, v166\n"
"v_mfma_f32_16x16x32_bf16 v[88:91], a[196:199], a[224:227], v[88:91]\n"
"s_waitcnt lgkmcnt(9)\n"
"ds_read_b64_tr_b16 a[174:175], v220 offset:34600\n"
"v_mul_f32 v167, v234, v167\n"
"v_mfma_f32_16x16x32_bf16 v[92:95], a[204:207], a[224:227], 0\n"
"s_add_u32 s92, s92, s93\n"
"ds_read_b64_tr_b16 a[176:177], v220 offset:38440\n"
"v_cvt_pk_bf16_f32 v160, v160, v161\n"
"v_mfma_f32_16x16x32_bf16 v[92:95], a[208:211], a[240:243], v[92:95]\n"
"s_lshl_b32 s92, s92, 10\n"
"ds_read_b64_tr_b16 a[178:179], v220 offset:38696\n"
"v_cvt_pk_bf16_f32 v161, v162, v163\n"
"v_mfma_f32_16x16x32_bf16 v[84:87], a[208:211], a[236:239], 0\n"
"s_add_u32 s91, s92, 3932160\n"
"v_cvt_pk_bf16_f32 v162, v164, v165\n"
"v_cvt_pk_bf16_f32 v163, v166, v167\n"
"v_mfma_f32_16x16x32_bf16 v[84:87], a[204:207], a[220:223], v[84:87]\n"
"s_mov_b32 s0, 0xbfb8aa3b\n"
"v_accvgpr_write_b32 a0, v160\n"
"v_accvgpr_write_b32 a1, v161\n"
"v_mfma_f32_16x16x32_bf16 v[76:79], a[204:207], a[216:219], 0\n"
"s_mov_b32 s1, 0xffff0000\n"
"v_accvgpr_write_b32 a2, v162\n"
"v_accvgpr_write_b32 a3, v163\n"
"buffer_store_dwordx4 a[0:3], v216, s[60:63], s88 offen sc1\n"
"v_mfma_f32_16x16x32_bf16 v[76:79], a[208:211], a[232:235], v[76:79]\n"
"L_st9_%=:\n"
"s_cbranch_execz L_hd0_%=\n"
"v_accvgpr_read_b32 v168, a32\n"
"v_accvgpr_read_b32 v169, a33\n"
"v_mfma_f32_16x16x32_bf16 v[68:71], a[208:211], a[228:231], 0\n"
"v_accvgpr_read_b32 v170, a34\n"
"v_accvgpr_read_b32 v171, a35\n"
"v_mfma_f32_16x16x32_bf16 v[68:71], a[204:207], a[212:215], v[68:71]\n"
"s_addk_i32 m0, 7176\n"
"v_accvgpr_read_b32 v172, a48\n"
"v_accvgpr_read_b32 v173, a49\n"
"v_mfma_f32_16x16x32_bf16 v[32:35], a[148:151], a[212:215], 0\n"
"s_mov_b32 s82, 1769472\n"
"v_accvgpr_read_b32 v174, a50\n"
"v_accvgpr_read_b32 v175, a51\n"
"v_mfma_f32_16x16x32_bf16 v[32:35], a[152:155], a[228:231], v[32:35]\n"
"v_mul_f32 v168, v234, v168\n"
"v_mul_f32 v169, v234, v169\n"
"v_mfma_f32_16x16x32_bf16 v[40:43], a[152:155], a[232:235], 0\n"
"s_waitcnt lgkmcnt(0)\n"
"buffer_load_dwordx4 a[16:19], v216, s[68:71], s84 offen offset:3072 sc1\n"
"v_mfma_f32_16x16x32_bf16 v[40:43], a[148:151], a[216:219], v[40:43]\n"
"s_barrier\n"
"ds_read_b64_tr_b16 a[180:181], v220 offset:0\n"
"v_mul_f32 v170, v234, v170\n"
"v_mfma_f32_16x16x32_bf16 v[48:51], a[148:151], a[220:223], 0\n"
"s_addk_i32 s84, 4096\n"
"ds_read_b64_tr_b16 a[182:183], v220 offset:256\n"
"v_mul_f32 v171, v234, v171\n"
"v_mfma_f32_16x16x32_bf16 v[48:51], a[152:155], a[236:239], v[48:51]\n"
"ds_read_b64_tr_b16 a[184:185], v220 offset:4096\n"
"v_mul_f32 v172, v234, v172\n"
"v_mfma_f32_16x16x32_bf16 v[56:59], a[152:155], a[240:243], 0\n"
"ds_read_b64_tr_b16 a[186:187], v220 offset:4352\n"
"v_mul_f32 v173, v234, v173\n"
"v_mfma_f32_16x16x32_bf16 v[56:59], a[148:151], a[224:227], v[56:59]\n"
"ds_read_b64_tr_b16 a[188:189], v220 offset:8\n"
"v_mul_f32 v174, v234, v174\n"
"v_mfma_f32_16x16x32_bf16 v[60:63], a[156:159], a[224:227], 0\n"
"buffer_load_dwordx4 a[48:51], v216, s[68:71], s85 offen offset:3072 sc1\n"
"v_mfma_f32_16x16x32_bf16 v[60:63], a[160:163], a[240:243], v[60:63]\n"
"s_addk_i32 s85, 4096\n"
"ds_read_b64_tr_b16 a[190:191], v220 offset:264\n"
"v_mul_f32 v175, v234, v175\n"
"v_mfma_f32_16x16x32_bf16 v[52:55], a[160:163], a[236:239], 0\n"
"ds_read_b64_tr_b16 a[192:193], v220 offset:4104\n"
"v_cvt_pk_bf16_f32 v168, v168, v169\n"
"v_mfma_f32_16x16x32_bf16 v[52:55], a[156:159], a[220:223], v[52:55]\n"
"ds_read_b64_tr_b16 a[194:195], v220 offset:4360\n"
"v_cvt_pk_bf16_f32 v169, v170, v171\n"
"v_mfma_f32_16x16x32_bf16 v[44:47], a[156:159], a[216:219], 0\n"
"ds_read_b64_tr_b16 a[196:197], v220 offset:1024\n"
"v_cvt_pk_bf16_f32 v170, v172, v173\n"
"v_mfma_f32_16x16x32_bf16 v[44:47], a[160:163], a[232:235], v[44:47]\n"
"ds_read_b64_tr_b16 a[198:199], v220 offset:1280\n"
"v_cvt_pk_bf16_f32 v171, v174, v175\n"
"v_mfma_f32_16x16x32_bf16 v[36:39], a[160:163], a[228:231], 0\n"
"buffer_load_dwordx4 a[128:131], v216, s[68:71], s86 offen offset:3072 sc1\n"
"v_mfma_f32_16x16x32_bf16 v[36:39], a[156:159], a[212:215], v[36:39]\n"
"s_addk_i32 s86, 4096\n"
"ds_read_b64_tr_b16 a[200:201], v220 offset:5120\n"
"v_accvgpr_write_b32 a32, v168\n"
"v_mfma_f32_16x16x32_bf16 v[96:99], a[164:167], a[212:215], 0\n"
"ds_read_b64_tr_b16 a[202:203], v220 offset:5376\n"
"v_accvgpr_write_b32 a33, v169\n"
"v_mfma_f32_16x16x32_bf16 v[96:99], a[168:171], a[228:231], v[96:99]\n"
"ds_read_b64_tr_b16 a[204:205], v220 offset:1032\n"
"v_accvgpr_write_b32 a34, v170\n"
"v_mfma_f32_16x16x32_bf16 v[104:107], a[168:171], a[232:235], 0\n"
"ds_read_b64_tr_b16 a[206:207], v220 offset:1288\n"
"v_accvgpr_write_b32 a35, v171\n"
"v_mfma_f32_16x16x32_bf16 v[104:107], a[164:167], a[216:219], v[104:107]\n"
"buffer_store_dwordx4 a[32:35], v216, s[60:63], s88 offen offset:1024 sc1\n"
"v_mfma_f32_16x16x32_bf16 v[112:115], a[164:167], a[220:223], 0\n"
"ds_read_b64_tr_b16 a[208:209], v220 offset:5128\n"
"v_accvgpr_read_b32 v160, a64\n"
"v_mfma_f32_16x16x32_bf16 v[112:115], a[168:171], a[236:239], v[112:115]\n"
"ds_read_b64_tr_b16 a[210:211], v220 offset:5384\n"
"v_accvgpr_read_b32 v161, a65\n"
"v_mfma_f32_16x16x32_bf16 v[120:123], a[168:171], a[240:243], 0\n"
"v_accvgpr_read_b32 v162, a66\n"
"v_accvgpr_read_b32 v163, a67\n"
"v_mfma_f32_16x16x32_bf16 v[120:123], a[164:167], a[224:227], v[120:123]\n"
"v_accvgpr_read_b32 v164, a80\n"
"v_accvgpr_read_b32 v165, a81\n"
"v_mfma_f32_16x16x32_bf16 v[124:127], a[172:175], a[224:227], 0\n"
"v_accvgpr_read_b32 v166, a82\n"
"v_accvgpr_read_b32 v167, a83\n"
"v_mfma_f32_16x16x32_bf16 v[124:127], a[176:179], a[240:243], v[124:127]\n"
"buffer_load_dwordx4 a[132:135], v216, s[68:71], s87 offen offset:3072 sc1\n"
"v_mfma_f32_16x16x32_bf16 v[116:119], a[176:179], a[236:239], 0\n"
"s_addk_i32 s87, 4096\n"
"v_mul_f32 v160, v234, v160\n"
"v_mul_f32 v161, v234, v161\n"
"v_mfma_f32_16x16x32_bf16 v[116:119], a[172:175], a[220:223], v[116:119]\n"
"v_mul_f32 v162, v234, v162\n"
"v_mul_f32 v163, v234, v163\n"
"v_mfma_f32_16x16x32_bf16 v[108:111], a[172:175], a[216:219], 0\n"
"v_mul_f32 v164, v234, v164\n"
"v_mul_f32 v165, v234, v165\n"
"v_mfma_f32_16x16x32_bf16 v[108:111], a[176:179], a[232:235], v[108:111]\n"
"v_mul_f32 v166, v234, v166\n"
"v_mul_f32 v167, v234, v167\n"
"v_mfma_f32_16x16x32_bf16 v[100:103], a[176:179], a[228:231], 0\n"
"v_cvt_pk_bf16_f32 v160, v160, v161\n"
"v_cvt_pk_bf16_f32 v161, v162, v163\n"
"v_mfma_f32_16x16x32_bf16 v[100:103], a[172:175], a[212:215], v[100:103]\n"
"s_waitcnt vmcnt(37) lgkmcnt(2)\n"
"buffer_load_dwordx4 v227, s[36:39], s82 offen lds\n"
"v_mfma_f32_16x16x32_bf16 v[0:3], a[180:183], a[244:247], v[0:3]\n"
"s_waitcnt vmcnt(30) lgkmcnt(1)\n"
"ds_read_b64_tr_b16 a[148:149], v220 offset:512\n"
"s_mov_b32 s3, 32768\n"
"v_mfma_f32_16x16x32_bf16 v[0:3], a[184:187], v[180:183], v[0:3]\n"
"s_waitcnt vmcnt(29) lgkmcnt(1)\n"
"ds_read_b64_tr_b16 a[150:151], v220 offset:768\n"
"v_cvt_pk_bf16_f32 v162, v164, v165\n"
"v_mfma_f32_16x16x32_bf16 v[8:11], a[184:187], v[184:187], v[8:11]\n"
"s_addk_i32 m0, 1024\n"
"ds_read_b64_tr_b16 a[152:153], v220 offset:4608\n"
"v_cvt_pk_bf16_f32 v163, v166, v167\n"
"v_mfma_f32_16x16x32_bf16 v[8:11], a[180:183], a[248:251], v[8:11]\n"
"ds_read_b64_tr_b16 a[154:155], v220 offset:4864\n"
"v_accvgpr_write_b32 a64, v160\n"
"v_mfma_f32_16x16x32_bf16 v[16:19], a[180:183], a[252:255], v[16:19]\n"
"s_waitcnt vmcnt(28)\n"
"ds_read_b64_tr_b16 a[156:157], v220 offset:520\n"
"v_accvgpr_write_b32 a65, v161\n"
"v_mfma_f32_16x16x32_bf16 v[16:19], a[184:187], v[188:191], v[16:19]\n"
"s_waitcnt vmcnt(27)\n"
"buffer_load_dwordx4 v[240:243], v216, s[60:63], s3 offen sc1\n"
"v_mfma_f32_16x16x32_bf16 v[24:27], a[184:187], v[192:195], v[24:27]\n"
"ds_read_b64_tr_b16 a[158:159], v220 offset:776\n"
"v_accvgpr_write_b32 a66, v162\n"
"v_mfma_f32_16x16x32_bf16 v[24:27], a[180:183], v[176:179], v[24:27]\n"
"ds_read_b64_tr_b16 a[160:161], v220 offset:4616\n"
"v_accvgpr_write_b32 a67, v163\n"
"v_mfma_f32_16x16x32_bf16 v[28:31], a[188:191], v[176:179], v[28:31]\n"
"ds_read_b64_tr_b16 a[162:163], v220 offset:4872\n"
"v_accvgpr_read_b32 v168, a96\n"
"v_mfma_f32_16x16x32_bf16 v[28:31], a[192:195], v[192:195], v[28:31]\n"
"ds_read_b64_tr_b16 a[164:165], v220 offset:1536\n"
"v_accvgpr_read_b32 v169, a97\n"
"v_mfma_f32_16x16x32_bf16 v[20:23], a[192:195], v[188:191], v[20:23]\n"
"buffer_store_dwordx4 a[64:67], v216, s[60:63], s88 offen offset:2048 sc1\n"
"buffer_load_dwordx4 v[244:247], v216, s[60:63], s3 offen offset:1024 sc1\n"
"v_mfma_f32_16x16x32_bf16 v[20:23], a[188:191], a[252:255], v[20:23]\n"
"ds_read_b64_tr_b16 a[166:167], v220 offset:1792\n"
"v_accvgpr_read_b32 v170, a98\n"
"v_mfma_f32_16x16x32_bf16 v[12:15], a[188:191], a[248:251], v[12:15]\n"
"ds_read_b64_tr_b16 a[168:169], v220 offset:5632\n"
"v_accvgpr_read_b32 v171, a99\n"
"v_mfma_f32_16x16x32_bf16 v[12:15], a[192:195], v[184:187], v[12:15]\n"
"ds_read_b64_tr_b16 a[170:171], v220 offset:5888\n"
"v_accvgpr_read_b32 v172, a112\n"
"v_mfma_f32_16x16x32_bf16 v[4:7], a[192:195], v[180:183], v[4:7]\n"
"ds_read_b64_tr_b16 a[172:173], v220 offset:1544\n"
"v_accvgpr_read_b32 v173, a113\n"
"v_mfma_f32_16x16x32_bf16 v[4:7], a[188:191], a[244:247], v[4:7]\n"
"ds_read_b64_tr_b16 a[174:175], v220 offset:1800\n"
"v_accvgpr_read_b32 v174, a114\n"
"v_mfma_f32_16x16x32_bf16 v[64:67], a[196:199], a[244:247], v[64:67]\n"
"buffer_load_dwordx4 v227, s[40:43], s82 offen lds\n"
"v_mfma_f32_16x16x32_bf16 v[64:67], a[200:203], v[180:183], v[64:67]\n"
"s_addk_i32 m0, 7176\n"
"ds_read_b64_tr_b16 a[176:177], v220 offset:5640\n"
"v_accvgpr_read_b32 v175, a115\n"
"v_mfma_f32_16x16x32_bf16 v[72:75], a[200:203], v[184:187], v[72:75]\n"
"s_addk_i32 s82, 27648\n"
"ds_read_b64_tr_b16 a[178:179], v220 offset:5896\n"
"v_mul_f32 v168, v234, v168\n"
"v_mfma_f32_16x16x32_bf16 v[72:75], a[196:199], a[248:251], v[72:75]\n"
"v_mul_f32 v169, v234, v169\n"
"v_mul_f32 v170, v234, v170\n"
"v_mfma_f32_16x16x32_bf16 v[80:83], a[196:199], a[252:255], v[80:83]\n"
"v_mul_f32 v171, v234, v171\n"
"v_mul_f32 v172, v234, v172\n"
"v_mfma_f32_16x16x32_bf16 v[80:83], a[200:203], v[188:191], v[80:83]\n"
"v_mul_f32 v173, v234, v173\n"
"v_mul_f32 v174, v234, v174\n"
"v_mfma_f32_16x16x32_bf16 v[88:91], a[200:203], v[192:195], v[88:91]\n"
"buffer_load_dwordx4 v227, s[36:39], s82 offen lds\n"
"v_mfma_f32_16x16x32_bf16 v[88:91], a[196:199], v[176:179], v[88:91]\n"
"s_addk_i32 m0, 1024\n"
"v_mul_f32 v175, v234, v175\n"
"v_cvt_pk_bf16_f32 v168, v168, v169\n"
"v_mfma_f32_16x16x32_bf16 v[92:95], a[204:207], v[176:179], v[92:95]\n"
"v_cvt_pk_bf16_f32 v169, v170, v171\n"
"v_cvt_pk_bf16_f32 v170, v172, v173\n"
"v_mfma_f32_16x16x32_bf16 v[92:95], a[208:211], v[192:195], v[92:95]\n"
"v_cvt_pk_bf16_f32 v171, v174, v175\n"
"v_accvgpr_write_b32 a96, v168\n"
"v_mfma_f32_16x16x32_bf16 v[84:87], a[208:211], v[188:191], v[84:87]\n"
"v_accvgpr_write_b32 a97, v169\n"
"v_accvgpr_write_b32 a98, v170\n"
"v_mfma_f32_16x16x32_bf16 v[84:87], a[204:207], a[252:255], v[84:87]\n"
"v_accvgpr_write_b32 a99, v171\n"
"v_accvgpr_read_b32 v160, a4\n"
"v_mfma_f32_16x16x32_bf16 v[76:79], a[204:207], a[248:251], v[76:79]\n"
"buffer_store_dwordx4 a[96:99], v216, s[60:63], s88 offen offset:3072 sc1\n"
"v_mfma_f32_16x16x32_bf16 v[76:79], a[208:211], v[184:187], v[76:79]\n"
"v_rcp_f32 v235, v235\n"
"v_mfma_f32_16x16x32_bf16 v[68:71], a[208:211], v[180:183], v[68:71]\n"
"v_accvgpr_read_b32 v161, a5\n"
"v_accvgpr_read_b32 v162, a6\n"
"v_mfma_f32_16x16x32_bf16 v[68:71], a[204:207], a[244:247], v[68:71]\n"
"s_waitcnt lgkmcnt(0)\n"
"ds_read_b64_tr_b16 a[180:181], v220 offset:16400\n"
"v_accvgpr_read_b32 v163, a7\n"
"v_mfma_f32_16x16x32_bf16 v[32:35], a[148:151], a[244:247], v[32:35]\n"
"ds_read_b64_tr_b16 a[182:183], v220 offset:16656\n"
"v_accvgpr_read_b32 v164, a20\n"
"v_mfma_f32_16x16x32_bf16 v[32:35], a[152:155], v[180:183], v[32:35]\n"
"ds_read_b64_tr_b16 a[184:185], v220 offset:20496\n"
"v_accvgpr_read_b32 v165, a21\n"
"v_mfma_f32_16x16x32_bf16 v[40:43], a[152:155], v[184:187], v[40:43]\n"
"buffer_load_dwordx4 v227, s[40:43], s82 offen lds\n"
"v_mfma_f32_16x16x32_bf16 v[40:43], a[148:151], a[248:251], v[40:43]\n"
"s_add_u32 m0, s77, 82000\n"
"ds_read_b64_tr_b16 a[186:187], v220 offset:20752\n"
"v_accvgpr_read_b32 v166, a22\n"
"v_mfma_f32_16x16x32_bf16 v[48:51], a[148:151], a[252:255], v[48:51]\n"
"s_mov_b32 s82, 1990656\n"
"ds_read_b64_tr_b16 a[188:189], v220 offset:16408\n"
"v_accvgpr_read_b32 v167, a23\n"
"v_mfma_f32_16x16x32_bf16 v[48:51], a[152:155], v[188:191], v[48:51]\n"
"ds_read_b64_tr_b16 a[190:191], v220 offset:16664\n"
"v_mul_f32 v160, v235, v160\n"
"v_mfma_f32_16x16x32_bf16 v[56:59], a[152:155], v[192:195], v[56:59]\n"
"ds_read_b64_tr_b16 a[192:193], v220 offset:20504\n"
"v_mul_f32 v161, v235, v161\n"
"v_mfma_f32_16x16x32_bf16 v[56:59], a[148:151], v[176:179], v[56:59]\n"
"ds_read_b64_tr_b16 a[194:195], v220 offset:20760\n"
"v_mul_f32 v162, v235, v162\n"
"v_mfma_f32_16x16x32_bf16 v[60:63], a[156:159], v[176:179], v[60:63]\n"
"buffer_load_dwordx4 a[136:139], v216, s[68:71], s84 offen sc1\n"
"v_mfma_f32_16x16x32_bf16 v[60:63], a[160:163], v[192:195], v[60:63]\n"
"ds_read_b64_tr_b16 a[196:197], v220 offset:17424\n"
"v_mul_f32 v163, v235, v163\n"
"v_mfma_f32_16x16x32_bf16 v[52:55], a[160:163], v[188:191], v[52:55]\n"
"ds_read_b64_tr_b16 a[198:199], v220 offset:17680\n"
"v_mul_f32 v164, v235, v164\n"
"v_mfma_f32_16x16x32_bf16 v[52:55], a[156:159], a[252:255], v[52:55]\n"
"ds_read_b64_tr_b16 a[200:201], v220 offset:21520\n"
"v_mul_f32 v165, v235, v165\n"
"v_mfma_f32_16x16x32_bf16 v[44:47], a[156:159], a[248:251], v[44:47]\n"
"ds_read_b64_tr_b16 a[202:203], v220 offset:21776\n"
"v_mul_f32 v166, v235, v166\n"
"v_mfma_f32_16x16x32_bf16 v[44:47], a[160:163], v[184:187], v[44:47]\n"
"ds_read_b64_tr_b16 a[204:205], v220 offset:17432\n"
"v_mul_f32 v167, v235, v167\n"
"v_mfma_f32_16x16x32_bf16 v[36:39], a[160:163], v[180:183], v[36:39]\n"
"buffer_load_dwordx4 a[140:143], v216, s[68:71], s85 offen sc1\n"
"v_mfma_f32_16x16x32_bf16 v[36:39], a[156:159], a[244:247], v[36:39]\n"
"ds_read_b64_tr_b16 a[206:207], v220 offset:17688\n"
"v_cvt_pk_bf16_f32 v160, v160, v161\n"
"v_mfma_f32_16x16x32_bf16 v[96:99], a[164:167], a[244:247], v[96:99]\n"
"ds_read_b64_tr_b16 a[208:209], v220 offset:21528\n"
"v_cvt_pk_bf16_f32 v161, v162, v163\n"
"v_mfma_f32_16x16x32_bf16 v[96:99], a[168:171], v[180:183], v[96:99]\n"
"ds_read_b64_tr_b16 a[210:211], v220 offset:21784\n"
"v_cvt_pk_bf16_f32 v162, v164, v165\n"
"v_mfma_f32_16x16x32_bf16 v[104:107], a[168:171], v[184:187], v[104:107]\n"
"v_cvt_pk_bf16_f32 v163, v166, v167\n"
"v_accvgpr_write_b32 a4, v160\n"
"v_mfma_f32_16x16x32_bf16 v[104:107], a[164:167], a[248:251], v[104:107]\n"
"v_accvgpr_write_b32 a5, v161\n"
"v_accvgpr_write_b32 a6, v162\n"
"v_mfma_f32_16x16x32_bf16 v[112:115], a[164:167], a[252:255], v[112:115]\n"
"buffer_load_dwordx4 a[212:215], v216, s[68:71], s86 offen sc1\n"
"v_mfma_f32_16x16x32_bf16 v[112:115], a[168:171], v[188:191], v[112:115]\n"
"v_accvgpr_write_b32 a7, v163\n"
"v_accvgpr_read_b32 v168, a36\n"
"v_mfma_f32_16x16x32_bf16 v[120:123], a[168:171], v[192:195], v[120:123]\n"
"v_accvgpr_read_b32 v169, a37\n"
"v_accvgpr_read_b32 v170, a38\n"
"v_mfma_f32_16x16x32_bf16 v[120:123], a[164:167], v[176:179], v[120:123]\n"
"v_accvgpr_read_b32 v171, a39\n"
"v_accvgpr_read_b32 v172, a52\n"
"v_mfma_f32_16x16x32_bf16 v[124:127], a[172:175], v[176:179], v[124:127]\n"
"buffer_store_dwordx4 a[4:7], v216, s[60:63], s89 offen sc1\n"
"v_mfma_f32_16x16x32_bf16 v[124:127], a[176:179], v[192:195], v[124:127]\n"
"v_accvgpr_read_b32 v173, a53\n"
"v_accvgpr_read_b32 v174, a54\n"
"v_mfma_f32_16x16x32_bf16 v[116:119], a[176:179], v[188:191], v[116:119]\n"
"v_accvgpr_read_b32 v175, a55\n"
"v_mul_f32 v168, v235, v168\n"
"v_mfma_f32_16x16x32_bf16 v[116:119], a[172:175], a[252:255], v[116:119]\n"
"v_mul_f32 v169, v235, v169\n"
"v_mul_f32 v170, v235, v170\n"
"v_mfma_f32_16x16x32_bf16 v[108:111], a[172:175], a[248:251], v[108:111]\n"
"v_mul_f32 v171, v235, v171\n"
"v_mul_f32 v172, v235, v172\n"
"v_mfma_f32_16x16x32_bf16 v[108:111], a[176:179], v[184:187], v[108:111]\n"
"v_mul_f32 v173, v235, v173\n"
"v_mul_f32 v174, v235, v174\n"
"v_mfma_f32_16x16x32_bf16 v[100:103], a[176:179], v[180:183], v[100:103]\n"
"buffer_load_dwordx4 a[216:219], v216, s[68:71], s87 offen sc1\n"
"v_mfma_f32_16x16x32_bf16 v[100:103], a[172:175], a[244:247], v[100:103]\n"
"s_waitcnt vmcnt(38) lgkmcnt(0)\n"
"ds_read_b64_tr_b16 a[148:149], v220 offset:16912\n"
"v_mul_f32 v175, v235, v175\n"
"v_mfma_f32_16x16x32_bf16 v[0:3], a[180:183], v[196:199], v[0:3]\n"
"s_waitcnt vmcnt(30)\n"
"ds_read_b64_tr_b16 a[150:151], v220 offset:17168\n"
"v_cvt_pk_bf16_f32 v168, v168, v169\n"
"v_mfma_f32_16x16x32_bf16 v[0:3], a[184:187], v[128:131], v[0:3]\n"
"s_waitcnt vmcnt(29)\n"
"ds_read_b64_tr_b16 a[152:153], v220 offset:21008\n"
"v_cvt_pk_bf16_f32 v169, v170, v171\n"
"v_mfma_f32_16x16x32_bf16 v[8:11], a[184:187], v[132:135], v[8:11]\n"
"ds_read_b64_tr_b16 a[154:155], v220 offset:21264\n"
"v_cvt_pk_bf16_f32 v170, v172, v173\n"
"v_mfma_f32_16x16x32_bf16 v[8:11], a[180:183], v[200:203], v[8:11]\n"
"ds_read_b64_tr_b16 a[156:157], v220 offset:16920\n"
"v_cvt_pk_bf16_f32 v171, v174, v175\n"
"v_mfma_f32_16x16x32_bf16 v[16:19], a[180:183], v[204:207], v[16:19]\n"
"s_waitcnt vmcnt(28)\n"
"buffer_load_dwordx4 a[220:223], v216, s[68:71], s84 offen offset:1024 sc1\n"
"v_mfma_f32_16x16x32_bf16 v[16:19], a[184:187], v[136:139], v[16:19]\n"
"s_waitcnt vmcnt(28)\n"
"ds_read_b64_tr_b16 a[158:159], v220 offset:17176\n"
"v_accvgpr_write_b32 a36, v168\n"
"v_mfma_f32_16x16x32_bf16 v[24:27], a[184:187], v[140:143], v[24:27]\n"
"ds_read_b64_tr_b16 a[160:161], v220 offset:21016\n"
"v_accvgpr_write_b32 a37, v169\n"
"v_mfma_f32_16x16x32_bf16 v[24:27], a[180:183], v[208:211], v[24:27]\n"
"ds_read_b64_tr_b16 a[162:163], v220 offset:21272\n"
"v_accvgpr_write_b32 a38, v170\n"
"v_mfma_f32_16x16x32_bf16 v[28:31], a[188:191], v[208:211], v[28:31]\n"
"ds_read_b64_tr_b16 a[164:165], v220 offset:17936\n"
"v_accvgpr_write_b32 a39, v171\n"
"v_mfma_f32_16x16x32_bf16 v[28:31], a[192:195], v[140:143], v[28:31]\n"
"ds_read_b64_tr_b16 a[166:167], v220 offset:18192\n"
"v_accvgpr_read_b32 v160, a68\n"
"v_mfma_f32_16x16x32_bf16 v[20:23], a[192:195], v[136:139], v[20:23]\n"
"buffer_store_dwordx4 a[36:39], v216, s[60:63], s89 offen offset:1024 sc1\n"
"v_mfma_f32_16x16x32_bf16 v[20:23], a[188:191], v[204:207], v[20:23]\n"
"ds_read_b64_tr_b16 a[168:169], v220 offset:22032\n"
"v_accvgpr_read_b32 v161, a69\n"
"v_mfma_f32_16x16x32_bf16 v[12:15], a[188:191], v[200:203], v[12:15]\n"
"ds_read_b64_tr_b16 a[170:171], v220 offset:22288\n"
"v_accvgpr_read_b32 v162, a70\n"
"v_mfma_f32_16x16x32_bf16 v[12:15], a[192:195], v[132:135], v[12:15]\n"
"ds_read_b64_tr_b16 a[172:173], v220 offset:17944\n"
"v_accvgpr_read_b32 v163, a71\n"
"v_mfma_f32_16x16x32_bf16 v[4:7], a[192:195], v[128:131], v[4:7]\n"
"ds_read_b64_tr_b16 a[174:175], v220 offset:18200\n"
"v_accvgpr_read_b32 v164, a84\n"
"v_mfma_f32_16x16x32_bf16 v[4:7], a[188:191], v[196:199], v[4:7]\n"
"ds_read_b64_tr_b16 a[176:177], v220 offset:22040\n"
"v_accvgpr_read_b32 v165, a85\n"
"v_mfma_f32_16x16x32_bf16 v[64:67], a[196:199], v[196:199], v[64:67]\n"
"buffer_load_dwordx4 a[224:227], v216, s[68:71], s85 offen offset:1024 sc1\n"
"v_mfma_f32_16x16x32_bf16 v[64:67], a[200:203], v[128:131], v[64:67]\n"
"ds_read_b64_tr_b16 a[178:179], v220 offset:22296\n"
"v_accvgpr_read_b32 v166, a86\n"
"v_mfma_f32_16x16x32_bf16 v[72:75], a[200:203], v[132:135], v[72:75]\n"
"v_accvgpr_read_b32 v167, a87\n"
"v_mul_f32 v160, v235, v160\n"
"v_mfma_f32_16x16x32_bf16 v[72:75], a[196:199], v[200:203], v[72:75]\n"
"v_mul_f32 v161, v235, v161\n"
"v_mul_f32 v162, v235, v162\n"
"v_mfma_f32_16x16x32_bf16 v[80:83], a[196:199], v[204:207], v[80:83]\n"
"v_mul_f32 v163, v235, v163\n"
"v_mul_f32 v164, v235, v164\n"
"v_mfma_f32_16x16x32_bf16 v[80:83], a[200:203], v[136:139], v[80:83]\n"
"v_mul_f32 v165, v235, v165\n"
"v_mul_f32 v166, v235, v166\n"
"v_mfma_f32_16x16x32_bf16 v[88:91], a[200:203], v[140:143], v[88:91]\n"
"buffer_load_dwordx4 a[228:231], v216, s[68:71], s86 offen offset:1024 sc1\n"
"v_mfma_f32_16x16x32_bf16 v[88:91], a[196:199], v[208:211], v[88:91]\n"
"v_mul_f32 v167, v235, v167\n"
"v_cvt_pk_bf16_f32 v160, v160, v161\n"
"v_mfma_f32_16x16x32_bf16 v[92:95], a[204:207], v[208:211], v[92:95]\n"
"v_cvt_pk_bf16_f32 v161, v162, v163\n"
"v_cvt_pk_bf16_f32 v162, v164, v165\n"
"v_mfma_f32_16x16x32_bf16 v[92:95], a[208:211], v[140:143], v[92:95]\n"
"v_cvt_pk_bf16_f32 v163, v166, v167\n"
"v_accvgpr_write_b32 a68, v160\n"
"v_mfma_f32_16x16x32_bf16 v[84:87], a[208:211], v[136:139], v[84:87]\n"
"v_accvgpr_write_b32 a69, v161\n"
"v_accvgpr_write_b32 a70, v162\n"
"v_mfma_f32_16x16x32_bf16 v[84:87], a[204:207], v[204:207], v[84:87]\n"
"v_accvgpr_write_b32 a71, v163\n"
"v_mfma_f32_16x16x32_bf16 v[76:79], a[204:207], v[200:203], v[76:79]\n"
"buffer_store_dwordx4 a[68:71], v216, s[60:63], s89 offen offset:2048 sc1\n"
"v_mfma_f32_16x16x32_bf16 v[76:79], a[208:211], v[132:135], v[76:79]\n"
"v_rcp_f32 v236, v236\n"
"v_mfma_f32_16x16x32_bf16 v[68:71], a[208:211], v[128:131], v[68:71]\n"
"v_rcp_f32 v237, v237\n"
"v_mfma_f32_16x16x32_bf16 v[68:71], a[204:207], v[196:199], v[68:71]\n"
"s_waitcnt lgkmcnt(0)\n"
"ds_read_b64_tr_b16 a[180:181], v219 offset:32800\n"
"v_accvgpr_read_b32 v168, a100\n"
"v_mfma_f32_16x16x32_bf16 v[32:35], a[148:151], v[196:199], v[32:35]\n"
"ds_read_b64_tr_b16 a[182:183], v219 offset:33056\n"
"v_accvgpr_read_b32 v169, a101\n"
"v_mfma_f32_16x16x32_bf16 v[32:35], a[152:155], v[128:131], v[32:35]\n"
"ds_read_b64_tr_b16 a[184:185], v219 offset:36896\n"
"v_accvgpr_read_b32 v170, a102\n"
"v_mfma_f32_16x16x32_bf16 v[40:43], a[152:155], v[132:135], v[40:43]\n"
"buffer_load_dwordx4 a[232:235], v216, s[68:71], s87 offen offset:1024 sc1\n"
"v_mfma_f32_16x16x32_bf16 v[40:43], a[148:151], v[200:203], v[40:43]\n"
"ds_read_b64_tr_b16 a[186:187], v219 offset:37152\n"
"v_accvgpr_read_b32 v171, a103\n"
"v_mfma_f32_16x16x32_bf16 v[48:51], a[148:151], v[204:207], v[48:51]\n"
"ds_read_b64_tr_b16 a[188:189], v219 offset:32808\n"
"v_accvgpr_read_b32 v172, a116\n"
"v_mfma_f32_16x16x32_bf16 v[48:51], a[152:155], v[136:139], v[48:51]\n"
"ds_read_b64_tr_b16 a[190:191], v219 offset:33064\n"
"v_accvgpr_read_b32 v173, a117\n"
"v_mfma_f32_16x16x32_bf16 v[56:59], a[152:155], v[140:143], v[56:59]\n"
"ds_read_b64_tr_b16 a[192:193], v219 offset:36904\n"
"v_accvgpr_read_b32 v174, a118\n"
"v_mfma_f32_16x16x32_bf16 v[56:59], a[148:151], v[208:211], v[56:59]\n"
"ds_read_b64_tr_b16 a[194:195], v219 offset:37160\n"
"v_accvgpr_read_b32 v175, a119\n"
"v_mfma_f32_16x16x32_bf16 v[60:63], a[156:159], v[208:211], v[60:63]\n"
"buffer_load_dwordx4 v227, s[36:39], s82 offen lds\n"
"v_mfma_f32_16x16x32_bf16 v[60:63], a[160:163], v[140:143], v[60:63]\n"
"s_addk_i32 m0, 1024\n"
"ds_read_b64_tr_b16 a[196:197], v219 offset:33824\n"
"v_mul_f32 v168, v235, v168\n"
"v_mfma_f32_16x16x32_bf16 v[52:55], a[160:163], v[136:139], v[52:55]\n"
"ds_read_b64_tr_b16 a[198:199], v219 offset:34080\n"
"v_mul_f32 v169, v235, v169\n"
"v_mfma_f32_16x16x32_bf16 v[52:55], a[156:159], v[204:207], v[52:55]\n"
"ds_read_b64_tr_b16 a[200:201], v219 offset:37920\n"
"v_mul_f32 v170, v235, v170\n"
"v_mfma_f32_16x16x32_bf16 v[44:47], a[156:159], v[200:203], v[44:47]\n"
"ds_read_b64_tr_b16 a[202:203], v219 offset:38176\n"
"v_mul_f32 v171, v235, v171\n"
"v_mfma_f32_16x16x32_bf16 v[44:47], a[160:163], v[132:135], v[44:47]\n"
"ds_read_b64_tr_b16 a[204:205], v219 offset:33832\n"
"v_mul_f32 v172, v235, v172\n"
"v_mfma_f32_16x16x32_bf16 v[36:39], a[160:163], v[128:131], v[36:39]\n"
"buffer_load_dwordx4 v227, s[40:43], s82 offen lds\n"
"v_mfma_f32_16x16x32_bf16 v[36:39], a[156:159], v[196:199], v[36:39]\n"
"s_addk_i32 m0, 7176\n"
"ds_read_b64_tr_b16 a[206:207], v219 offset:34088\n"
"v_mul_f32 v173, v235, v173\n"
"v_mfma_f32_16x16x32_bf16 v[96:99], a[164:167], v[196:199], v[96:99]\n"
"s_addk_i32 s82, 27648\n"
"ds_read_b64_tr_b16 a[208:209], v219 offset:37928\n"
"v_mul_f32 v174, v235, v174\n"
"v_mfma_f32_16x16x32_bf16 v[96:99], a[168:171], v[128:131], v[96:99]\n"
"ds_read_b64_tr_b16 a[210:211], v219 offset:38184\n"
"v_mul_f32 v175, v235, v175\n"
"v_mfma_f32_16x16x32_bf16 v[104:107], a[168:171], v[132:135], v[104:107]\n"
"v_cvt_pk_bf16_f32 v168, v168, v169\n"
"v_cvt_pk_bf16_f32 v169, v170, v171\n"
"v_mfma_f32_16x16x32_bf16 v[104:107], a[164:167], v[200:203], v[104:107]\n"
"v_cvt_pk_bf16_f32 v170, v172, v173\n"
"v_cvt_pk_bf16_f32 v171, v174, v175\n"
"v_mfma_f32_16x16x32_bf16 v[112:115], a[164:167], v[204:207], v[112:115]\n"
"buffer_load_dwordx4 v227, s[36:39], s82 offen lds\n"
"v_mfma_f32_16x16x32_bf16 v[112:115], a[168:171], v[136:139], v[112:115]\n"
"s_addk_i32 m0, 1024\n"
"v_accvgpr_write_b32 a100, v168\n"
"v_accvgpr_write_b32 a101, v169\n"
"v_mfma_f32_16x16x32_bf16 v[120:123], a[168:171], v[140:143], v[120:123]\n"
"v_accvgpr_write_b32 a102, v170\n"
"v_accvgpr_write_b32 a103, v171\n"
"v_mfma_f32_16x16x32_bf16 v[120:123], a[164:167], v[208:211], v[120:123]\n"
"v_accvgpr_read_b32 v160, a8\n"
"v_accvgpr_read_b32 v161, a9\n"
"v_mfma_f32_16x16x32_bf16 v[124:127], a[172:175], v[208:211], v[124:127]\n"
"v_accvgpr_read_b32 v162, a10\n"
"v_accvgpr_read_b32 v163, a11\n"
"v_mfma_f32_16x16x32_bf16 v[124:127], a[176:179], v[140:143], v[124:127]\n"
"v_accvgpr_read_b32 v164, a24\n"
"v_accvgpr_read_b32 v165, a25\n"
"v_mfma_f32_16x16x32_bf16 v[116:119], a[176:179], v[136:139], v[116:119]\n"
"buffer_store_dwordx4 a[100:103], v216, s[60:63], s89 offen offset:3072 sc1\n"
"v_mfma_f32_16x16x32_bf16 v[116:119], a[172:175], v[204:207], v[116:119]\n"
"v_accvgpr_read_b32 v166, a26\n"
"v_accvgpr_read_b32 v167, a27\n"
"v_mfma_f32_16x16x32_bf16 v[108:111], a[172:175], v[200:203], v[108:111]\n"
"v_mul_f32 v160, v236, v160\n"
"v_mul_f32 v161, v236, v161\n"
"v_mfma_f32_16x16x32_bf16 v[108:111], a[176:179], v[132:135], v[108:111]\n"
"v_mul_f32 v162, v236, v162\n"
"v_mul_f32 v163, v236, v163\n"
"v_mfma_f32_16x16x32_bf16 v[100:103], a[176:179], v[128:131], v[100:103]\n"
"v_mul_f32 v164, v236, v164\n"
"v_mul_f32 v165, v236, v165\n"
"v_mfma_f32_16x16x32_bf16 v[100:103], a[172:175], v[196:199], v[100:103]\n"
"s_waitcnt vmcnt(19) lgkmcnt(0)\n"
"v_cmp_eq_u64_e64 s[4:5], v[240:241], s[80:81]\n"
"v_cmp_eq_u64_e64 s[6:7], v[242:243], s[80:81]\n"
"s_nop 1\n"
"s_and_b64 s[4:5], s[4:5], s[6:7]\n"
"v_cmp_eq_u64_e64 s[6:7], v[244:245], s[80:81]\n"
"s_nop 1\n"
"s_and_b64 s[4:5], s[4:5], s[6:7]\n"
"v_cmp_eq_u64_e64 s[6:7], v[246:247], s[80:81]\n"
"s_nop 1\n"
"s_and_b64 s[4:5], s[4:5], s[6:7]\n"
"s_cmp_eq_u64 s[4:5], -1\n"
"s_cbranch_scc0 L_spinR_%=\n"
"L_retR_%=:\n"
"v_mfma_f32_16x16x32_bf16 v[0:3], a[180:183], v[144:147], v[0:3]\n"
"s_cmp_eq_u32 s73, 0\n"
"s_cselect_b32 s97, 0, 0x40000000\n"
"buffer_load_dwordx4 v227, s[40:43], s82 offen lds\n"
"v_mfma_f32_16x16x32_bf16 v[0:3], a[184:187], a[16:19], v[0:3]\n"
"s_and_b32 s93, s73, 3\n"
"ds_read_b64_tr_b16 a[148:149], v219 offset:33312\n"
"v_mul_f32 v166, v236, v166\n"
"v_mfma_f32_16x16x32_bf16 v[8:11], a[184:187], a[48:51], v[8:11]\n"
"s_lshl_b32 s93, s93, 5\n"
"ds_read_b64_tr_b16 a[150:151], v219 offset:33568\n"
"v_mul_f32 v167, v236, v167\n"
"v_mfma_f32_16x16x32_bf16 v[8:11], a[180:183], v[148:151], v[8:11]\n"
"s_xor_b32 s92, s12, 1\n"
"ds_read_b64_tr_b16 a[152:153], v219 offset:37408\n"
"v_cvt_pk_bf16_f32 v160, v160, v161\n"
"v_mfma_f32_16x16x32_bf16 v[16:19], a[180:183], v[152:155], v[16:19]\n"
"s_lshl_b32 s92, s92, 7\n"
"ds_read_b64_tr_b16 a[154:155], v219 offset:37664\n"
"v_cvt_pk_bf16_f32 v161, v162, v163\n"
"v_mfma_f32_16x16x32_bf16 v[16:19], a[184:187], a[128:131], v[16:19]\n"
"s_add_u32 s92, s92, s93\n"
"ds_read_b64_tr_b16 a[156:157], v219 offset:33320\n"
"v_cvt_pk_bf16_f32 v162, v164, v165\n"
"v_mfma_f32_16x16x32_bf16 v[24:27], a[184:187], a[132:135], v[24:27]\n"
"s_add_u32 s92, s92, s72\n"
"buffer_load_dwordx4 a[236:239], v216, s[68:71], s84 offen offset:2048 sc1\n"
"v_mfma_f32_16x16x32_bf16 v[24:27], a[180:183], v[156:159], v[24:27]\n"
"s_mul_i32 s92, s92, 5120\n"
"ds_read_b64_tr_b16 a[158:159], v219 offset:33576\n"
"v_cvt_pk_bf16_f32 v163, v166, v167\n"
"v_mfma_f32_16x16x32_bf16 v[28:31], a[188:191], v[156:159], v[28:31]\n"
"s_add_u32 s24, s92, 28573696\n"
"ds_read_b64_tr_b16 a[160:161], v219 offset:37416\n"
"v_accvgpr_write_b32 a8, v160\n"
"v_mfma_f32_16x16x32_bf16 v[28:31], a[192:195], a[132:135], v[28:31]\n"
"s_xor_b32 s92, s12, 0\n"
"ds_read_b64_tr_b16 a[162:163], v219 offset:37672\n"
"v_accvgpr_write_b32 a9, v161\n"
"v_mfma_f32_16x16x32_bf16 v[20:23], a[192:195], a[128:131], v[20:23]\n"
"s_lshl_b32 s92, s92, 7\n"
"ds_read_b64_tr_b16 a[164:165], v219 offset:34336\n"
"v_accvgpr_write_b32 a10, v162\n"
"v_mfma_f32_16x16x32_bf16 v[20:23], a[188:191], v[152:155], v[20:23]\n"
"s_add_u32 s92, s92, s93\n"
"ds_read_b64_tr_b16 a[166:167], v219 offset:34592\n"
"v_accvgpr_write_b32 a11, v163\n"
"v_mfma_f32_16x16x32_bf16 v[12:15], a[188:191], v[148:151], v[12:15]\n"
"s_add_u32 s92, s92, s72\n"
"buffer_store_dwordx4 a[8:11], v216, s[60:63], s90 offen sc1\n"
"v_mfma_f32_16x16x32_bf16 v[12:15], a[192:195], a[48:51], v[12:15]\n"
"s_mul_i32 s92, s92, 5120\n"
"ds_read_b64_tr_b16 a[168:169], v219 offset:38432\n"
"v_accvgpr_read_b32 v168, a40\n"
"v_mfma_f32_16x16x32_bf16 v[4:7], a[192:195], a[16:19], v[4:7]\n"
"s_add_u32 s25, s92, 28573696\n"
"ds_read_b64_tr_b16 a[170:171], v219 offset:38688\n"
"v_accvgpr_read_b32 v169, a41\n"
"v_mfma_f32_16x16x32_bf16 v[4:7], a[188:191], v[144:147], v[4:7]\n"
"s_xor_b32 s92, s26, 0\n"
"ds_read_b64_tr_b16 a[172:173], v219 offset:34344\n"
"v_accvgpr_read_b32 v170, a42\n"
"v_mfma_f32_16x16x32_bf16 v[64:67], a[196:199], v[144:147], v[64:67]\n"
"s_mul_i32 s92, s92, 133\n"
"ds_read_b64_tr_b16 a[174:175], v219 offset:34600\n"
"v_accvgpr_read_b32 v171, a43\n"
"v_mfma_f32_16x16x32_bf16 v[64:67], a[200:203], a[16:19], v[64:67]\n"
"s_add_u32 s92, s92, s93\n"
"ds_read_b64_tr_b16 a[176:177], v219 offset:38440\n"
"v_accvgpr_read_b32 v172, a56\n"
"v_mfma_f32_16x16x32_bf16 v[72:75], a[200:203], a[48:51], v[72:75]\n"
"s_lshl_b32 s92, s92, 10\n"
"buffer_load_dwordx4 a[240:243], v216, s[68:71], s85 offen offset:2048 sc1\n"
"v_mfma_f32_16x16x32_bf16 v[72:75], a[196:199], v[148:151], v[72:75]\n"
"L_st10_%=:\n"
"s_cbranch_execz L_hd0_%=\n"
"s_add_u32 s16, s92, 3932160\n"
"ds_read_b64_tr_b16 a[178:179], v219 offset:38696\n"
"v_accvgpr_read_b32 v173, a57\n"
"v_mfma_f32_16x16x32_bf16 v[80:83], a[196:199], v[152:155], v[80:83]\n"
"s_xor_b32 s92, s26, 1\n"
"v_accvgpr_read_b32 v174, a58\n"
"v_accvgpr_read_b32 v175, a59\n"
"v_mfma_f32_16x16x32_bf16 v[80:83], a[200:203], a[128:131], v[80:83]\n"
"s_mul_i32 s92, s92, 133\n"
"v_mul_f32 v168, v236, v168\n"
"v_mul_f32 v169, v236, v169\n"
"v_mfma_f32_16x16x32_bf16 v[88:91], a[200:203], a[132:135], v[88:91]\n"
"s_add_u32 s92, s92, s93\n"
"v_mul_f32 v170, v236, v170\n"
"v_mul_f32 v171, v236, v171\n"
"v_mfma_f32_16x16x32_bf16 v[88:91], a[196:199], v[156:159], v[88:91]\n"
"s_lshl_b32 s92, s92, 10\n"
"v_mul_f32 v172, v236, v172\n"
"v_mul_f32 v173, v236, v173\n"
"v_mfma_f32_16x16x32_bf16 v[92:95], a[204:207], v[156:159], v[92:95]\n"
"s_add_u32 s17, s92, 3932160\n"
"buffer_load_dwordx4 a[80:83], v216, s[68:71], s86 offen offset:2048 sc1\n"
"v_mfma_f32_16x16x32_bf16 v[92:95], a[208:211], a[132:135], v[92:95]\n"
"s_xor_b32 s92, s26, 2\n"
"v_mul_f32 v174, v236, v174\n"
"v_mul_f32 v175, v236, v175\n"
"v_mfma_f32_16x16x32_bf16 v[84:87], a[208:211], a[128:131], v[84:87]\n"
"s_mul_i32 s92, s92, 133\n"
"v_cvt_pk_bf16_f32 v168, v168, v169\n"
"v_cvt_pk_bf16_f32 v169, v170, v171\n"
"v_mfma_f32_16x16x32_bf16 v[84:87], a[204:207], v[152:155], v[84:87]\n"
"s_add_u32 s92, s92, s93\n"
"v_cvt_pk_bf16_f32 v170, v172, v173\n"
"v_cvt_pk_bf16_f32 v171, v174, v175\n"
"v_mfma_f32_16x16x32_bf16 v[76:79], a[204:207], v[148:151], v[76:79]\n"
"s_lshl_b32 s92, s92, 10\n"
"v_accvgpr_write_b32 a40, v168\n"
"v_accvgpr_write_b32 a41, v169\n"
"v_mfma_f32_16x16x32_bf16 v[76:79], a[208:211], a[48:51], v[76:79]\n"
"s_add_u32 s18, s92, 3932160\n"
"v_accvgpr_write_b32 a42, v170\n"
"v_accvgpr_write_b32 a43, v171\n"
"v_mfma_f32_16x16x32_bf16 v[68:71], a[208:211], a[16:19], v[68:71]\n"
"s_xor_b32 s92, s26, 3\n"
"buffer_load_dwordx4 a[112:115], v216, s[68:71], s87 offen offset:2048 sc1\n"
"v_mfma_f32_16x16x32_bf16 v[68:71], a[204:207], v[144:147], v[68:71]\n"
"s_waitcnt lgkmcnt(0)\n"
"s_getpc_b64 s[4:5]\n"
"L_pc3_%=:\n"
"s_mov_b32 s6, L_end_%=-L_pc3_%=\n"
"s_and_b32 s5, s5, 0xffff\n"
"s_mov_b32 s7, 0x20000\n"
"v_mfma_f32_16x16x32_bf16 v[32:35], a[148:151], v[144:147], v[32:35]\n"
"s_barrier\n"
"ds_read_b64_tr_b16 a[180:181], v219 offset:0\n"
"v_accvgpr_read_b32 v160, a72\n"
"v_mfma_f32_16x16x32_bf16 v[32:35], a[152:155], a[16:19], v[32:35]\n"
"s_add_u32 m0, s78, 61440\n"
"ds_read_b64_tr_b16 a[182:183], v219 offset:256\n"
"v_accvgpr_read_b32 v161, a73\n"
"v_mfma_f32_16x16x32_bf16 v[40:43], a[152:155], a[48:51], v[40:43]\n"
"s_mov_b32 s83, s11\n"
"ds_read_b64_tr_b16 a[184:185], v219 offset:4096\n"
"v_accvgpr_read_b32 v162, a74\n"
"v_mfma_f32_16x16x32_bf16 v[40:43], a[148:151], v[148:151], v[40:43]\n"
"s_mul_i32 s83, s83, 20480\n"
"ds_read_b64_tr_b16 a[186:187], v219 offset:4352\n"
"v_accvgpr_read_b32 v163, a75\n"
"v_mfma_f32_16x16x32_bf16 v[48:51], a[148:151], v[152:155], v[48:51]\n"
"s_add_u32 s83, s83, s25\n"
"buffer_load_dword v247, v252, s[4:7], 0 offen\n"
"v_mfma_f32_16x16x32_bf16 v[48:51], a[152:155], a[128:131], v[48:51]\n"
"s_add_u32 s82, s83, 1024\n"
"ds_read_b64_tr_b16 a[188:189], v219 offset:8\n"
"v_accvgpr_read_b32 v164, a88\n"
"v_mfma_f32_16x16x32_bf16 v[56:59], a[152:155], a[132:135], v[56:59]\n"
"s_mul_i32 s92, s92, 133\n"
"ds_read_b64_tr_b16 a[190:191], v219 offset:264\n"
"v_accvgpr_read_b32 v165, a89\n"
"v_mfma_f32_16x16x32_bf16 v[56:59], a[148:151], v[156:159], v[56:59]\n"
"s_add_u32 s92, s92, s93\n"
"ds_read_b64_tr_b16 a[192:193], v219 offset:4104\n"
"v_accvgpr_read_b32 v166, a90\n"
"v_mfma_f32_16x16x32_bf16 v[60:63], a[156:159], v[156:159], v[60:63]\n"
"s_lshl_b32 s92, s92, 10\n"
"ds_read_b64_tr_b16 a[194:195], v219 offset:4360\n"
"v_accvgpr_read_b32 v167, a91\n"
"v_mfma_f32_16x16x32_bf16 v[60:63], a[160:163], a[132:135], v[60:63]\n"
"s_add_u32 s19, s92, 3932160\n"
"ds_read_b64_tr_b16 a[196:197], v219 offset:1024\n"
"v_mul_f32 v160, v236, v160\n"
"v_mfma_f32_16x16x32_bf16 v[52:55], a[160:163], a[128:131], v[52:55]\n"
"s_add_u32 s92, s27, 0\n"
"buffer_store_dwordx4 a[40:43], v216, s[60:63], s90 offen offset:1024 sc1\n"
"v_mfma_f32_16x16x32_bf16 v[52:55], a[156:159], v[152:155], v[52:55]\n"
"s_mul_i32 s92, s92, 55\n"
"ds_read_b64_tr_b16 a[198:199], v219 offset:1280\n"
"v_mul_f32 v161, v236, v161\n"
"v_mfma_f32_16x16x32_bf16 v[44:47], a[156:159], v[148:151], v[44:47]\n"
"s_lshl_b32 s93, s79, 1\n"
"ds_read_b64_tr_b16 a[200:201], v219 offset:5120\n"
"v_mul_f32 v162, v236, v162\n"
"v_mfma_f32_16x16x32_bf16 v[44:47], a[160:163], a[48:51], v[44:47]\n"
"s_add_u32 s92, s92, s93\n"
"ds_read_b64_tr_b16 a[202:203], v219 offset:5376\n"
"v_mul_f32 v163, v236, v163\n"
"v_mfma_f32_16x16x32_bf16 v[36:39], a[160:163], a[16:19], v[36:39]\n"
"s_lshl_b32 s92, s92, 10\n"
"ds_read_b64_tr_b16 a[204:205], v219 offset:1032\n"
"v_mul_f32 v164, v236, v164\n"
"v_mfma_f32_16x16x32_bf16 v[36:39], a[156:159], v[144:147], v[36:39]\n"
"s_add_u32 s20, s92, 21364736\n"
"ds_read_b64_tr_b16 a[206:207], v219 offset:1288\n"
"v_mul_f32 v165, v236, v165\n"
"v_mfma_f32_16x16x32_bf16 v[96:99], a[164:167], v[144:147], v[96:99]\n"
"s_add_u32 s92, s27, 4\n"
"buffer_load_dwordx4 a[20:23], v216, s[68:71], s84 offen offset:3072 sc1\n"
"v_mfma_f32_16x16x32_bf16 v[96:99], a[168:171], a[16:19], v[96:99]\n"
"s_addk_i32 s84, 4096\n"
"ds_read_b64_tr_b16 a[208:209], v219 offset:5128\n"
"v_mul_f32 v166, v236, v166\n"
"v_mfma_f32_16x16x32_bf16 v[104:107], a[168:171], a[48:51], v[104:107]\n"
"s_mul_i32 s92, s92, 55\n"
"ds_read_b64_tr_b16 a[210:211], v219 offset:5384\n"
"v_mul_f32 v167, v236, v167\n"
"v_mfma_f32_16x16x32_bf16 v[104:107], a[164:167], v[148:151], v[104:107]\n"
"s_lshl_b32 s93, s79, 1\n"
"v_cvt_pk_bf16_f32 v160, v160, v161\n"
"v_cvt_pk_bf16_f32 v161, v162, v163\n"
"v_mfma_f32_16x16x32_bf16 v[112:115], a[164:167], v[152:155], v[112:115]\n"
"s_add_u32 s92, s92, s93\n"
"v_cvt_pk_bf16_f32 v162, v164, v165\n"
"v_cvt_pk_bf16_f32 v163, v166, v167\n"
"v_mfma_f32_16x16x32_bf16 v[112:115], a[168:171], a[128:131], v[112:115]\n"
"s_lshl_b32 s92, s92, 10\n"
"v_accvgpr_write_b32 a72, v160\n"
"v_accvgpr_write_b32 a73, v161\n"
"v_mfma_f32_16x16x32_bf16 v[120:123], a[168:171], a[132:135], v[120:123]\n"
"s_add_u32 s21, s92, 21364736\n"
"buffer_load_dwordx4 a[244:247], v216, s[68:71], s85 offen offset:3072 sc1\n"
"v_mfma_f32_16x16x32_bf16 v[120:123], a[164:167], v[156:159], v[120:123]\n"
"s_addk_i32 s85, 4096\n"
"v_accvgpr_write_b32 a74, v162\n"
"v_accvgpr_write_b32 a75, v163\n"
"v_mfma_f32_16x16x32_bf16 v[124:127], a[172:175], v[156:159], v[124:127]\n"
"s_add_u32 s92, s27, 8\n"
"v_accvgpr_read_b32 v168, a104\n"
"v_accvgpr_read_b32 v169, a105\n"
"v_mfma_f32_16x16x32_bf16 v[124:127], a[176:179], a[132:135], v[124:127]\n"
"s_mul_i32 s92, s92, 55\n"
"v_accvgpr_read_b32 v170, a106\n"
"v_accvgpr_read_b32 v171, a107\n"
"v_mfma_f32_16x16x32_bf16 v[116:119], a[176:179], a[128:131], v[116:119]\n"
"s_lshl_b32 s93, s79, 1\n"
"v_accvgpr_read_b32 v172, a120\n"
"v_accvgpr_read_b32 v173, a121\n"
"v_mfma_f32_16x16x32_bf16 v[116:119], a[172:175], v[152:155], v[116:119]\n"
"s_add_u32 s92, s92, s93\n"
"v_accvgpr_read_b32 v174, a122\n"
"v_accvgpr_read_b32 v175, a123\n"
"v_mfma_f32_16x16x32_bf16 v[108:111], a[172:175], v[148:151], v[108:111]\n"
"s_lshl_b32 s92, s92, 10\n"
"buffer_store_dwordx4 a[72:75], v216, s[60:63], s90 offen offset:2048 sc1\n"
"v_mfma_f32_16x16x32_bf16 v[108:111], a[176:179], a[48:51], v[108:111]\n"
"s_add_u32 s22, s92, 21364736\n"
"v_mul_f32 v168, v236, v168\n"
"v_mul_f32 v169, v236, v169\n"
"v_mfma_f32_16x16x32_bf16 v[100:103], a[176:179], a[16:19], v[100:103]\n"
"s_add_u32 s92, s27, 12\n"
"v_mul_f32 v170, v236, v170\n"
"v_mul_f32 v171, v236, v171\n"
"v_mfma_f32_16x16x32_bf16 v[100:103], a[172:175], v[144:147], v[100:103]\n"
"s_waitcnt vmcnt(25) lgkmcnt(0)\n"
"ds_read_b64_tr_b16 a[148:149], v219 offset:512\n"
"v_mul_f32 v172, v236, v172\n"
"v_mfma_f32_16x16x32_bf16 v[0:3], a[180:183], a[136:139], v[0:3]\n"
"s_waitcnt vmcnt(20)\n"
"ds_read_b64_tr_b16 a[150:151], v219 offset:768\n"
"v_mul_f32 v173, v236, v173\n"
"v_mfma_f32_16x16x32_bf16 v[0:3], a[184:187], a[220:223], v[0:3]\n"
"s_waitcnt vmcnt(18)\n"
"ds_read_b64_tr_b16 a[152:153], v219 offset:4608\n"
"v_mul_f32 v174, v236, v174\n"
"v_mfma_f32_16x16x32_bf16 v[8:11], a[184:187], a[224:227], v[8:11]\n"
"s_mul_i32 s92, s92, 55\n"
"buffer_load_dwordx4 a[248:251], v216, s[68:71], s86 offen offset:3072 sc1\n"
"v_mfma_f32_16x16x32_bf16 v[8:11], a[180:183], a[140:143], v[8:11]\n"
"s_addk_i32 s86, 4096\n"
"ds_read_b64_tr_b16 a[154:155], v219 offset:4864\n"
"v_mul_f32 v175, v236, v175\n"
"v_mfma_f32_16x16x32_bf16 v[16:19], a[180:183], a[212:215], v[16:19]\n"
"s_waitcnt vmcnt(18)\n"
"ds_read_b64_tr_b16 a[156:157], v219 offset:520\n"
"v_cvt_pk_bf16_f32 v168, v168, v169\n"
"v_mfma_f32_16x16x32_bf16 v[16:19], a[184:187], a[228:231], v[16:19]\n"
"s_waitcnt vmcnt(16)\n"
"ds_read_b64_tr_b16 a[158:159], v219 offset:776\n"
"v_cvt_pk_bf16_f32 v169, v170, v171\n"
"v_mfma_f32_16x16x32_bf16 v[24:27], a[184:187], a[232:235], v[24:27]\n"
"s_lshl_b32 s93, s79, 1\n"
"ds_read_b64_tr_b16 a[160:161], v219 offset:4616\n"
"v_cvt_pk_bf16_f32 v170, v172, v173\n"
"v_mfma_f32_16x16x32_bf16 v[24:27], a[180:183], a[216:219], v[24:27]\n"
"s_add_u32 s92, s92, s93\n"
"ds_read_b64_tr_b16 a[162:163], v219 offset:4872\n"
"v_cvt_pk_bf16_f32 v171, v174, v175\n"
"v_mfma_f32_16x16x32_bf16 v[28:31], a[188:191], a[216:219], v[28:31]\n"
"s_lshl_b32 s92, s92, 10\n"
"buffer_load_dwordx4 a[252:255], v216, s[68:71], s87 offen offset:3072 sc1\n"
"v_mfma_f32_16x16x32_bf16 v[28:31], a[192:195], a[232:235], v[28:31]\n"
"s_addk_i32 s87, 4096\n"
"ds_read_b64_tr_b16 a[164:165], v219 offset:1536\n"
"v_accvgpr_write_b32 a104, v168\n"
"v_mfma_f32_16x16x32_bf16 v[20:23], a[192:195], a[228:231], v[20:23]\n"
"s_add_u32 s23, s92, 21364736\n"
"ds_read_b64_tr_b16 a[166:167], v219 offset:1792\n"
"v_accvgpr_write_b32 a105, v169\n"
"v_mfma_f32_16x16x32_bf16 v[20:23], a[188:191], a[212:215], v[20:23]\n"
"ds_read_b64_tr_b16 a[168:169], v219 offset:5632\n"
"v_accvgpr_write_b32 a106, v170\n"
"v_mfma_f32_16x16x32_bf16 v[12:15], a[188:191], a[140:143], v[12:15]\n"
"ds_read_b64_tr_b16 a[170:171], v219 offset:5888\n"
"v_accvgpr_write_b32 a107, v171\n"
"v_mfma_f32_16x16x32_bf16 v[12:15], a[192:195], a[224:227], v[12:15]\n"
"ds_read_b64_tr_b16 a[172:173], v219 offset:1544\n"
"v_accvgpr_read_b32 v160, a12\n"
"v_mfma_f32_16x16x32_bf16 v[4:7], a[192:195], a[220:223], v[4:7]\n"
"buffer_load_dwordx4 v[176:179], v216, s[68:71], s84 offen sc1\n"
"v_mfma_f32_16x16x32_bf16 v[4:7], a[188:191], a[136:139], v[4:7]\n"
"ds_read_b64_tr_b16 a[174:175], v219 offset:1800\n"
"v_accvgpr_read_b32 v161, a13\n"
"v_mfma_f32_16x16x32_bf16 v[64:67], a[196:199], a[136:139], v[64:67]\n"
"ds_read_b64_tr_b16 a[176:177], v219 offset:5640\n"
"v_accvgpr_read_b32 v162, a14\n"
"v_mfma_f32_16x16x32_bf16 v[64:67], a[200:203], a[220:223], v[64:67]\n"
"ds_read_b64_tr_b16 a[178:179], v219 offset:5896\n"
"v_accvgpr_read_b32 v163, a15\n"
"v_mfma_f32_16x16x32_bf16 v[72:75], a[200:203], a[224:227], v[72:75]\n"
"v_accvgpr_read_b32 v164, a28\n"
"v_accvgpr_read_b32 v165, a29\n"
"v_mfma_f32_16x16x32_bf16 v[72:75], a[196:199], a[140:143], v[72:75]\n"
"v_accvgpr_read_b32 v166, a30\n"
"v_accvgpr_read_b32 v167, a31\n"
"v_mfma_f32_16x16x32_bf16 v[80:83], a[196:199], a[212:215], v[80:83]\n"
"buffer_store_dwordx4 a[104:107], v216, s[60:63], s90 offen offset:3072 sc1\n"
"v_mfma_f32_16x16x32_bf16 v[80:83], a[200:203], a[228:231], v[80:83]\n"
"v_mul_f32 v160, v237, v160\n"
"v_mul_f32 v161, v237, v161\n"
"v_mfma_f32_16x16x32_bf16 v[88:91], a[200:203], a[232:235], v[88:91]\n"
"v_mul_f32 v162, v237, v162\n"
"v_mul_f32 v163, v237, v163\n"
"v_mfma_f32_16x16x32_bf16 v[88:91], a[196:199], a[216:219], v[88:91]\n"
"v_mul_f32 v164, v237, v164\n"
"v_mul_f32 v165, v237, v165\n"
"v_mfma_f32_16x16x32_bf16 v[92:95], a[204:207], a[216:219], v[92:95]\n"
"v_mul_f32 v166, v237, v166\n"
"v_mul_f32 v167, v237, v167\n"
"v_mfma_f32_16x16x32_bf16 v[92:95], a[208:211], a[232:235], v[92:95]\n"
"v_cvt_pk_bf16_f32 v160, v160, v161\n"
"v_cvt_pk_bf16_f32 v161, v162, v163\n"
"v_mfma_f32_16x16x32_bf16 v[84:87], a[208:211], a[228:231], v[84:87]\n"
"buffer_load_dwordx4 v[180:183], v216, s[68:71], s85 offen sc1\n"
"v_mfma_f32_16x16x32_bf16 v[84:87], a[204:207], a[212:215], v[84:87]\n"
"v_cvt_pk_bf16_f32 v162, v164, v165\n"
"v_cvt_pk_bf16_f32 v163, v166, v167\n"
"v_mfma_f32_16x16x32_bf16 v[76:79], a[204:207], a[140:143], v[76:79]\n"
"v_accvgpr_write_b32 a12, v160\n"
"v_accvgpr_write_b32 a13, v161\n"
"v_mfma_f32_16x16x32_bf16 v[76:79], a[208:211], a[224:227], v[76:79]\n"
"v_accvgpr_write_b32 a14, v162\n"
"v_accvgpr_write_b32 a15, v163\n"
"v_mfma_f32_16x16x32_bf16 v[68:71], a[208:211], a[220:223], v[68:71]\n"
"v_mfma_f32_16x16x32_bf16 v[68:71], a[204:207], a[136:139], v[68:71]\n"
"s_waitcnt lgkmcnt(0)\n"
"ds_read_b64_tr_b16 a[180:181], v221 offset:0\n"
"v_accvgpr_read_b32 v168, a44\n"
"v_mfma_f32_16x16x32_bf16 v[32:35], a[148:151], a[136:139], v[32:35]\n"
"buffer_load_dwordx4 v[184:187], v216, s[68:71], s86 offen sc1\n"
"v_mfma_f32_16x16x32_bf16 v[32:35], a[152:155], a[220:223], v[32:35]\n"
"ds_read_b64_tr_b16 a[182:183], v221 offset:256\n"
"v_accvgpr_read_b32 v169, a45\n"
"v_mfma_f32_16x16x32_bf16 v[40:43], a[152:155], a[224:227], v[40:43]\n"
"ds_read_b64_tr_b16 a[184:185], v221 offset:4096\n"
"v_accvgpr_read_b32 v170, a46\n"
"v_mfma_f32_16x16x32_bf16 v[40:43], a[148:151], a[140:143], v[40:43]\n"
"ds_read_b64_tr_b16 a[186:187], v221 offset:4352\n"
"v_accvgpr_read_b32 v171, a47\n"
"v_mfma_f32_16x16x32_bf16 v[48:51], a[148:151], a[212:215], v[48:51]\n"
"ds_read_b64_tr_b16 a[188:189], v221 offset:8\n"
"v_accvgpr_read_b32 v172, a60\n"
"v_mfma_f32_16x16x32_bf16 v[48:51], a[152:155], a[228:231], v[48:51]\n"
"ds_read_b64_tr_b16 a[190:191], v221 offset:264\n"
"v_accvgpr_read_b32 v173, a61\n"
"v_mfma_f32_16x16x32_bf16 v[56:59], a[152:155], a[232:235], v[56:59]\n"
"buffer_store_dwordx4 a[12:15], v216, s[60:63], s91 offen sc1\n"
"v_mfma_f32_16x16x32_bf16 v[56:59], a[148:151], a[216:219], v[56:59]\n"
"ds_read_b64_tr_b16 a[192:193], v221 offset:4104\n"
"v_accvgpr_read_b32 v174, a62\n"
"v_mfma_f32_16x16x32_bf16 v[60:63], a[156:159], a[216:219], v[60:63]\n"
"ds_read_b64_tr_b16 a[194:195], v221 offset:4360\n"
"v_accvgpr_read_b32 v175, a63\n"
"v_mfma_f32_16x16x32_bf16 v[60:63], a[160:163], a[232:235], v[60:63]\n"
"ds_read_b64_tr_b16 a[196:197], v221 offset:1024\n"
"v_mul_f32 v168, v237, v168\n"
"v_mfma_f32_16x16x32_bf16 v[52:55], a[160:163], a[228:231], v[52:55]\n"
"ds_read_b64_tr_b16 a[198:199], v221 offset:1280\n"
"v_mul_f32 v169, v237, v169\n"
"v_mfma_f32_16x16x32_bf16 v[52:55], a[156:159], a[212:215], v[52:55]\n"
"ds_read_b64_tr_b16 a[200:201], v221 offset:5120\n"
"v_mul_f32 v170, v237, v170\n"
"v_mfma_f32_16x16x32_bf16 v[44:47], a[156:159], a[140:143], v[44:47]\n"
"buffer_load_dwordx4 v[188:191], v216, s[68:71], s87 offen sc1\n"
"v_mfma_f32_16x16x32_bf16 v[44:47], a[160:163], a[224:227], v[44:47]\n"
"ds_read_b64_tr_b16 a[202:203], v221 offset:5376\n"
"v_mul_f32 v171, v237, v171\n"
"v_mfma_f32_16x16x32_bf16 v[36:39], a[160:163], a[220:223], v[36:39]\n"
"ds_read_b64_tr_b16 a[204:205], v221 offset:1032\n"
"v_mul_f32 v172, v237, v172\n"
"v_mfma_f32_16x16x32_bf16 v[36:39], a[156:159], a[136:139], v[36:39]\n"
"ds_read_b64_tr_b16 a[206:207], v221 offset:1288\n"
"v_mul_f32 v173, v237, v173\n"
"v_mfma_f32_16x16x32_bf16 v[96:99], a[164:167], a[136:139], v[96:99]\n"
"ds_read_b64_tr_b16 a[208:209], v221 offset:5128\n"
"v_mul_f32 v174, v237, v174\n"
"v_mfma_f32_16x16x32_bf16 v[96:99], a[168:171], a[220:223], v[96:99]\n"
"ds_read_b64_tr_b16 a[210:211], v221 offset:5384\n"
"v_mul_f32 v175, v237, v175\n"
"v_mfma_f32_16x16x32_bf16 v[104:107], a[168:171], a[224:227], v[104:107]\n"
"buffer_load_dwordx4 v[192:195], v216, s[68:71], s84 offen offset:1024 sc1\n"
"v_mfma_f32_16x16x32_bf16 v[104:107], a[164:167], a[140:143], v[104:107]\n"
"v_cvt_pk_bf16_f32 v168, v168, v169\n"
"v_cvt_pk_bf16_f32 v169, v170, v171\n"
"v_mfma_f32_16x16x32_bf16 v[112:115], a[164:167], a[212:215], v[112:115]\n"
"v_cvt_pk_bf16_f32 v170, v172, v173\n"
"v_cvt_pk_bf16_f32 v171, v174, v175\n"
"v_mfma_f32_16x16x32_bf16 v[112:115], a[168:171], a[228:231], v[112:115]\n"
"v_accvgpr_write_b32 a44, v168\n"
"v_accvgpr_write_b32 a45, v169\n"
"v_mfma_f32_16x16x32_bf16 v[120:123], a[168:171], a[232:235], v[120:123]\n"
"v_accvgpr_write_b32 a46, v170\n"
"v_accvgpr_write_b32 a47, v171\n"
"v_mfma_f32_16x16x32_bf16 v[120:123], a[164:167], a[216:219], v[120:123]\n"
"v_mfma_f32_16x16x32_bf16 v[124:127], a[172:175], a[216:219], v[124:127]\n"
"buffer_load_dwordx4 a[52:55], v216, s[68:71], s85 offen offset:1024 sc1\n"
"v_mfma_f32_16x16x32_bf16 v[124:127], a[176:179], a[232:235], v[124:127]\n"
"v_mfma_f32_16x16x32_bf16 v[116:119], a[176:179], a[228:231], v[116:119]\n"
"v_mfma_f32_16x16x32_bf16 v[116:119], a[172:175], a[212:215], v[116:119]\n"
"v_mfma_f32_16x16x32_bf16 v[108:111], a[172:175], a[140:143], v[108:111]\n"
"v_mfma_f32_16x16x32_bf16 v[108:111], a[176:179], a[224:227], v[108:111]\n"
"v_mfma_f32_16x16x32_bf16 v[100:103], a[176:179], a[220:223], v[100:103]\n"
"buffer_store_dwordx4 a[44:47], v216, s[60:63], s91 offen offset:1024 sc1\n"
"v_mfma_f32_16x16x32_bf16 v[100:103], a[172:175], a[136:139], v[100:103]\n"
"s_waitcnt vmcnt(20) lgkmcnt(0)\n"
"ds_read_b64_tr_b16 a[148:149], v221 offset:512\n"
"v_accvgpr_read_b32 v160, a76\n"
"v_mfma_f32_16x16x32_bf16 v[0:3], a[180:183], a[236:239], v[0:3]\n"
"s_waitcnt vmcnt(13)\n"
"ds_read_b64_tr_b16 a[150:151], v221 offset:768\n"
"v_accvgpr_read_b32 v161, a77\n"
"v_mfma_f32_16x16x32_bf16 v[0:3], a[184:187], a[20:23], v[0:3]\n"
"s_waitcnt vmcnt(12)\n"
"ds_read_b64_tr_b16 a[152:153], v221 offset:4608\n"
"v_accvgpr_read_b32 v162, a78\n"
"v_mfma_f32_16x16x32_bf16 v[8:11], a[184:187], a[244:247], v[8:11]\n"
"ds_read_b64_tr_b16 a[154:155], v221 offset:4864\n"
"v_accvgpr_read_b32 v163, a79\n"
"v_mfma_f32_16x16x32_bf16 v[8:11], a[180:183], a[240:243], v[8:11]\n"
"ds_read_b64_tr_b16 a[156:157], v221 offset:520\n"
"v_accvgpr_read_b32 v164, a92\n"
"v_mfma_f32_16x16x32_bf16 v[16:19], a[180:183], a[80:83], v[16:19]\n"
"s_waitcnt vmcnt(10)\n"
"buffer_load_dwordx4 a[84:87], v216, s[68:71], s86 offen offset:1024 sc1\n"
"v_mfma_f32_16x16x32_bf16 v[16:19], a[184:187], a[248:251], v[16:19]\n"
"s_waitcnt vmcnt(10)\n"
"ds_read_b64_tr_b16 a[158:159], v221 offset:776\n"
"v_accvgpr_read_b32 v165, a93\n"
"v_mfma_f32_16x16x32_bf16 v[24:27], a[184:187], a[252:255], v[24:27]\n"
"ds_read_b64_tr_b16 a[160:161], v221 offset:4616\n"
"v_accvgpr_read_b32 v166, a94\n"
"v_mfma_f32_16x16x32_bf16 v[24:27], a[180:183], a[112:115], v[24:27]\n"
"ds_read_b64_tr_b16 a[162:163], v221 offset:4872\n"
"v_accvgpr_read_b32 v167, a95\n"
"v_mfma_f32_16x16x32_bf16 v[28:31], a[188:191], a[112:115], v[28:31]\n"
"ds_read_b64_tr_b16 a[164:165], v221 offset:1536\n"
"v_mul_f32 v160, v237, v160\n"
"v_mfma_f32_16x16x32_bf16 v[28:31], a[192:195], a[252:255], v[28:31]\n"
"ds_read_b64_tr_b16 a[166:167], v221 offset:1792\n"
"v_mul_f32 v161, v237, v161\n"
"v_mfma_f32_16x16x32_bf16 v[20:23], a[192:195], a[248:251], v[20:23]\n"
"buffer_load_dwordx4 v[196:199], v216, s[68:71], s87 offen offset:1024 sc1\n"
"v_mfma_f32_16x16x32_bf16 v[20:23], a[188:191], a[80:83], v[20:23]\n"
"ds_read_b64_tr_b16 a[168:169], v221 offset:5632\n"
"v_mul_f32 v162, v237, v162\n"
"v_mfma_f32_16x16x32_bf16 v[12:15], a[188:191], a[240:243], v[12:15]\n"
"ds_read_b64_tr_b16 a[170:171], v221 offset:5888\n"
"v_mul_f32 v163, v237, v163\n"
"v_mfma_f32_16x16x32_bf16 v[12:15], a[192:195], a[244:247], v[12:15]\n"
"ds_read_b64_tr_b16 a[172:173], v221 offset:1544\n"
"v_mul_f32 v164, v237, v164\n"
"v_mfma_f32_16x16x32_bf16 v[4:7], a[192:195], a[20:23], v[4:7]\n"
"ds_read_b64_tr_b16 a[174:175], v221 offset:1800\n"
"v_mul_f32 v165, v237, v165\n"
"v_mfma_f32_16x16x32_bf16 v[4:7], a[188:191], a[236:239], v[4:7]\n"
"ds_read_b64_tr_b16 a[176:177], v221 offset:5640\n"
"v_mul_f32 v166, v237, v166\n"
"v_mfma_f32_16x16x32_bf16 v[64:67], a[196:199], a[236:239], v[64:67]\n"
"buffer_load_dwordx4 v[200:203], v216, s[68:71], s84 offen offset:2048 sc1\n"
"v_mfma_f32_16x16x32_bf16 v[64:67], a[200:203], a[20:23], v[64:67]\n"
"ds_read_b64_tr_b16 a[178:179], v221 offset:5896\n"
"v_mul_f32 v167, v237, v167\n"
"v_mfma_f32_16x16x32_bf16 v[72:75], a[200:203], a[244:247], v[72:75]\n"
"v_cvt_pk_bf16_f32 v160, v160, v161\n"
"v_cvt_pk_bf16_f32 v161, v162, v163\n"
"v_mfma_f32_16x16x32_bf16 v[72:75], a[196:199], a[240:243], v[72:75]\n"
"v_cvt_pk_bf16_f32 v162, v164, v165\n"
"v_cvt_pk_bf16_f32 v163, v166, v167\n"
"v_mfma_f32_16x16x32_bf16 v[80:83], a[196:199], a[80:83], v[80:83]\n"
"v_accvgpr_write_b32 a76, v160\n"
"v_accvgpr_write_b32 a77, v161\n"
"v_mfma_f32_16x16x32_bf16 v[80:83], a[200:203], a[248:251], v[80:83]\n"
"v_accvgpr_write_b32 a78, v162\n"
"v_accvgpr_write_b32 a79, v163\n"
"v_mfma_f32_16x16x32_bf16 v[88:91], a[200:203], a[252:255], v[88:91]\n"
"buffer_store_dwordx4 a[76:79], v216, s[60:63], s91 offen offset:2048 sc1\n"
"v_mfma_f32_16x16x32_bf16 v[88:91], a[196:199], a[112:115], v[88:91]\n"
"v_accvgpr_read_b32 v168, a108\n"
"v_accvgpr_read_b32 v169, a109\n"
"v_mfma_f32_16x16x32_bf16 v[92:95], a[204:207], a[112:115], v[92:95]\n"
"v_accvgpr_read_b32 v170, a110\n"
"v_accvgpr_read_b32 v171, a111\n"
"v_mfma_f32_16x16x32_bf16 v[92:95], a[208:211], a[252:255], v[92:95]\n"
"v_accvgpr_read_b32 v172, a124\n"
"v_accvgpr_read_b32 v173, a125\n"
"v_mfma_f32_16x16x32_bf16 v[84:87], a[208:211], a[248:251], v[84:87]\n"
"v_accvgpr_read_b32 v174, a126\n"
"v_accvgpr_read_b32 v175, a127\n"
"v_mfma_f32_16x16x32_bf16 v[84:87], a[204:207], a[80:83], v[84:87]\n"
"v_mul_f32 v168, v237, v168\n"
"v_mul_f32 v169, v237, v169\n"
"v_mfma_f32_16x16x32_bf16 v[76:79], a[204:207], a[240:243], v[76:79]\n"
"buffer_load_dwordx4 v[204:207], v216, s[68:71], s85 offen offset:2048 sc1\n"
"v_mfma_f32_16x16x32_bf16 v[76:79], a[208:211], a[244:247], v[76:79]\n"
"v_mul_f32 v170, v237, v170\n"
"v_mul_f32 v171, v237, v171\n"
"v_mfma_f32_16x16x32_bf16 v[68:71], a[208:211], a[20:23], v[68:71]\n"
"v_mul_f32 v172, v237, v172\n"
"v_mul_f32 v173, v237, v173\n"
"v_mfma_f32_16x16x32_bf16 v[68:71], a[204:207], a[236:239], v[68:71]\n"
"s_waitcnt lgkmcnt(0)\n"
"ds_read_b64_tr_b16 a[180:181], v219 offset:16400\n"
"v_mul_f32 v174, v237, v174\n"
"v_mfma_f32_16x16x32_bf16 v[32:35], a[148:151], a[236:239], v[32:35]\n"
"s_mov_b32 s99, s73\n"
"ds_read_b64_tr_b16 a[182:183], v219 offset:16656\n"
"v_mul_f32 v175, v237, v175\n"
"v_mfma_f32_16x16x32_bf16 v[32:35], a[152:155], a[20:23], v[32:35]\n"
"s_lshl_b32 s93, s72, 8\n"
"ds_read_b64_tr_b16 a[184:185], v219 offset:20496\n"
"v_cvt_pk_bf16_f32 v168, v168, v169\n"
"v_mfma_f32_16x16x32_bf16 v[40:43], a[152:155], a[244:247], v[40:43]\n"
"s_add_u32 s99, s99, s93\n"
"buffer_load_dwordx4 v[208:211], v216, s[68:71], s86 offen offset:2048 sc1\n"
"v_mfma_f32_16x16x32_bf16 v[40:43], a[148:151], a[240:243], v[40:43]\n"
"s_lshl_b32 s99, s99, 7\n"
"ds_read_b64_tr_b16 a[186:187], v219 offset:20752\n"
"v_cvt_pk_bf16_f32 v169, v170, v171\n"
"v_mfma_f32_16x16x32_bf16 v[48:51], a[148:151], a[80:83], v[48:51]\n"
"s_add_u32 s99, s99, 393216\n"
"ds_read_b64_tr_b16 a[188:189], v219 offset:16408\n"
"v_cvt_pk_bf16_f32 v170, v172, v173\n"
"v_mfma_f32_16x16x32_bf16 v[48:51], a[152:155], a[248:251], v[48:51]\n"
"ds_read_b64_tr_b16 a[190:191], v219 offset:16664\n"
"v_cvt_pk_bf16_f32 v171, v174, v175\n"
"v_mfma_f32_16x16x32_bf16 v[56:59], a[152:155], a[252:255], v[56:59]\n"
"ds_read_b64_tr_b16 a[192:193], v219 offset:20504\n"
"v_accvgpr_write_b32 a108, v168\n"
"v_mfma_f32_16x16x32_bf16 v[56:59], a[148:151], a[112:115], v[56:59]\n"
"ds_read_b64_tr_b16 a[194:195], v219 offset:20760\n"
"v_accvgpr_write_b32 a109, v169\n"
"v_mfma_f32_16x16x32_bf16 v[60:63], a[156:159], a[112:115], v[60:63]\n"
"buffer_load_dwordx4 v[128:131], v216, s[68:71], s87 offen offset:2048 sc1\n"
"v_mfma_f32_16x16x32_bf16 v[60:63], a[160:163], a[252:255], v[60:63]\n"
"ds_read_b64_tr_b16 a[196:197], v219 offset:17424\n"
"v_accvgpr_write_b32 a110, v170\n"
"v_mfma_f32_16x16x32_bf16 v[52:55], a[160:163], a[248:251], v[52:55]\n"
"ds_read_b64_tr_b16 a[198:199], v219 offset:17680\n"
"v_accvgpr_write_b32 a111, v171\n"
"v_mfma_f32_16x16x32_bf16 v[52:55], a[156:159], a[80:83], v[52:55]\n"
"ds_read_b64_tr_b16 a[200:201], v219 offset:21520\n"
"v_cmp_ne_u32 vcc, 0, v253\n"
"v_mfma_f32_16x16x32_bf16 v[44:47], a[156:159], a[240:243], v[44:47]\n"
"ds_read_b64_tr_b16 a[202:203], v219 offset:21776\n"
"v_mov_b32 v246, 0x40000000\n"
"v_mfma_f32_16x16x32_bf16 v[44:47], a[160:163], a[244:247], v[44:47]\n"
"ds_read_b64_tr_b16 a[204:205], v219 offset:17432\n"
"v_cndmask_b32 v246, 0, v246, vcc\n"
"v_mfma_f32_16x16x32_bf16 v[36:39], a[160:163], a[20:23], v[36:39]\n"
"buffer_store_dwordx4 a[108:111], v216, s[60:63], s91 offen offset:3072 sc1\n"
"v_mfma_f32_16x16x32_bf16 v[36:39], a[156:159], a[236:239], v[36:39]\n"
"ds_read_b64_tr_b16 a[206:207], v219 offset:17688\n"
"v_mfma_f32_16x16x32_bf16 v[96:99], a[164:167], a[236:239], v[96:99]\n"
"ds_read_b64_tr_b16 a[208:209], v219 offset:21528\n"
"v_mfma_f32_16x16x32_bf16 v[96:99], a[168:171], a[20:23], v[96:99]\n"
"ds_read_b64_tr_b16 a[210:211], v219 offset:21784\n"
"v_mfma_f32_16x16x32_bf16 v[104:107], a[168:171], a[244:247], v[104:107]\n"
"v_mfma_f32_16x16x32_bf16 v[104:107], a[164:167], a[240:243], v[104:107]\n"
"v_mfma_f32_16x16x32_bf16 v[112:115], a[164:167], a[80:83], v[112:115]\n"
"buffer_load_dwordx4 v216, s[60:63], s83 offen sc1 lds\n"
"v_mfma_f32_16x16x32_bf16 v[112:115], a[168:171], a[248:251], v[112:115]\n"
"v_mfma_f32_16x16x32_bf16 v[120:123], a[168:171], a[252:255], v[120:123]\n"
"v_mfma_f32_16x16x32_bf16 v[120:123], a[164:167], a[112:115], v[120:123]\n"
"v_mfma_f32_16x16x32_bf16 v[124:127], a[172:175], a[112:115], v[124:127]\n"
"v_mfma_f32_16x16x32_bf16 v[124:127], a[176:179], a[252:255], v[124:127]\n"
"v_mfma_f32_16x16x32_bf16 v[116:119], a[176:179], a[248:251], v[116:119]\n"
"buffer_load_dwordx4 v216, s[60:63], s83 offen offset:1024 sc1 lds\n"
"v_mfma_f32_16x16x32_bf16 v[116:119], a[172:175], a[80:83], v[116:119]\n"
"v_mfma_f32_16x16x32_bf16 v[108:111], a[172:175], a[240:243], v[108:111]\n"
"v_mfma_f32_16x16x32_bf16 v[108:111], a[176:179], a[244:247], v[108:111]\n"
"v_mfma_f32_16x16x32_bf16 v[100:103], a[176:179], a[20:23], v[100:103]\n"
"v_mfma_f32_16x16x32_bf16 v[100:103], a[172:175], a[236:239], v[100:103]\n"
"s_waitcnt vmcnt(18) lgkmcnt(0)\n"
"s_getpc_b64 s[4:5]\n"
"L_pc4_%=:\n"
"s_mov_b32 s6, L_end_%=-L_pc4_%=\n"
"s_and_b32 s5, s5, 0xffff\n"
"s_mov_b32 s7, 0x20000\n"
"v_mfma_f32_16x16x32_bf16 v[0:3], a[180:183], v[176:179], v[0:3]\n"
"s_waitcnt vmcnt(12)\n"
"buffer_load_dword v247, v252, s[4:7], 0 offen\n"
"v_mfma_f32_16x16x32_bf16 v[0:3], a[184:187], v[192:195], v[0:3]\n"
"s_waitcnt vmcnt(12)\n"
"ds_read_b64_tr_b16 a[148:149], v219 offset:16912\n"
"v_mfma_f32_16x16x32_bf16 v[8:11], a[184:187], a[52:55], v[8:11]\n"
"ds_read_b64_tr_b16 a[150:151], v219 offset:17168\n"
"v_mfma_f32_16x16x32_bf16 v[8:11], a[180:183], v[180:183], v[8:11]\n"
"ds_read_b64_tr_b16 a[152:153], v219 offset:21008\n"
"v_mfma_f32_16x16x32_bf16 v[16:19], a[180:183], v[184:187], v[16:19]\n"
"s_waitcnt vmcnt(10)\n"
"ds_read_b64_tr_b16 a[154:155], v219 offset:21264\n"
"v_mfma_f32_16x16x32_bf16 v[16:19], a[184:187], a[84:87], v[16:19]\n"
"s_waitcnt vmcnt(9)\n"
"ds_read_b64_tr_b16 a[156:157], v219 offset:16920\n"
"v_mfma_f32_16x16x32_bf16 v[24:27], a[184:187], v[196:199], v[24:27]\n"
"buffer_load_dwordx4 v216, s[60:63], s83 offen offset:2048 sc1 lds\n"
"v_mfma_f32_16x16x32_bf16 v[24:27], a[180:183], v[188:191], v[24:27]\n"
"ds_read_b64_tr_b16 a[158:159], v219 offset:17176\n"
"v_mfma_f32_16x16x32_bf16 v[28:31], a[188:191], v[188:191], v[28:31]\n"
"ds_read_b64_tr_b16 a[160:161], v219 offset:21016\n"
"v_mfma_f32_16x16x32_bf16 v[28:31], a[192:195], v[196:199], v[28:31]\n"
"ds_read_b64_tr_b16 a[162:163], v219 offset:21272\n"
"v_mfma_f32_16x16x32_bf16 v[20:23], a[192:195], a[84:87], v[20:23]\n"
"ds_read_b64_tr_b16 a[164:165], v219 offset:17936\n"
"v_mfma_f32_16x16x32_bf16 v[20:23], a[188:191], v[184:187], v[20:23]\n"
"ds_read_b64_tr_b16 a[166:167], v219 offset:18192\n"
"v_mfma_f32_16x16x32_bf16 v[12:15], a[188:191], v[180:183], v[12:15]\n"
"buffer_load_dwordx4 v216, s[60:63], s83 offen offset:3072 sc1 lds\n"
"v_mfma_f32_16x16x32_bf16 v[12:15], a[192:195], a[52:55], v[12:15]\n"
"s_addk_i32 m0, 1024\n"
"ds_read_b64_tr_b16 a[168:169], v219 offset:22032\n"
"s_cmp_gt_u32 s11, 0\n"
"s_cselect_b32 s83, 0, 1\n"
"v_mfma_f32_16x16x32_bf16 v[4:7], a[192:195], v[192:195], v[4:7]\n"
"s_mul_i32 s83, s83, 20480\n"
"ds_read_b64_tr_b16 a[170:171], v219 offset:22288\n"
"s_add_u32 s83, s83, s25\n"
"v_mfma_f32_16x16x32_bf16 v[4:7], a[188:191], v[176:179], v[4:7]\n"
"ds_read_b64_tr_b16 a[172:173], v219 offset:17944\n"
"v_mfma_f32_16x16x32_bf16 v[64:67], a[196:199], v[176:179], v[64:67]\n"
"L_st11_%=:\n"
"s_cbranch_execz L_hd0_%=\n"
"ds_read_b64_tr_b16 a[174:175], v219 offset:18200\n"
"v_mfma_f32_16x16x32_bf16 v[64:67], a[200:203], v[192:195], v[64:67]\n"
"ds_read_b64_tr_b16 a[176:177], v219 offset:22040\n"
"v_mfma_f32_16x16x32_bf16 v[72:75], a[200:203], a[52:55], v[72:75]\n"
"buffer_load_dwordx4 v216, s[60:63], s82 offen offset:3072 sc1 lds\n"
"v_mfma_f32_16x16x32_bf16 v[72:75], a[196:199], v[180:183], v[72:75]\n"
"s_addk_i32 m0, 44032\n"
"ds_read_b64_tr_b16 a[178:179], v219 offset:22296\n"
"s_add_u32 s82, s83, 1024\n"
"v_mfma_f32_16x16x32_bf16 v[80:83], a[196:199], v[184:187], v[80:83]\n"
"v_mfma_f32_16x16x32_bf16 v[80:83], a[200:203], a[84:87], v[80:83]\n"
"v_mfma_f32_16x16x32_bf16 v[88:91], a[200:203], v[196:199], v[88:91]\n"
"v_mfma_f32_16x16x32_bf16 v[88:91], a[196:199], v[188:191], v[88:91]\n"
"v_mfma_f32_16x16x32_bf16 v[92:95], a[204:207], v[188:191], v[92:95]\n"
"buffer_load_dwordx4 v[132:135], v216, s[68:71], s84 offen offset:3072 sc1\n"
"v_mfma_f32_16x16x32_bf16 v[92:95], a[208:211], v[196:199], v[92:95]\n"
"s_waitcnt vmcnt(7) lgkmcnt(5)\n"
"buffer_load_dwordx4 v[136:139], v216, s[68:71], s85 offen offset:3072 sc1\n"
"buffer_store_dwordx2 v[232:233], v246, s[60:63], s99 offen sc1\n"
"v_mfma_f32_16x16x32_bf16 v[84:87], a[208:211], a[84:87], v[84:87]\n"
"s_addk_i32 s84, 4096\n"
"s_addk_i32 s85, 4096\n"
"v_mfma_f32_16x16x32_bf16 v[84:87], a[204:207], v[184:187], v[84:87]\n"
"v_mfma_f32_16x16x32_bf16 v[76:79], a[204:207], v[180:183], v[76:79]\n"
"buffer_load_dwordx4 v[140:143], v216, s[68:71], s86 offen offset:3072 sc1\n"
"v_mfma_f32_16x16x32_bf16 v[76:79], a[208:211], a[52:55], v[76:79]\n"
"s_addk_i32 s86, 4096\n"
"buffer_load_dwordx4 a[116:119], v216, s[68:71], s87 offen offset:3072 sc1\n"
"v_mfma_f32_16x16x32_bf16 v[68:71], a[208:211], v[192:195], v[68:71]\n"
"s_addk_i32 s87, 4096\n"
"v_mfma_f32_16x16x32_bf16 v[68:71], a[204:207], v[176:179], v[68:71]\n"
"s_waitcnt lgkmcnt(0)\n"
"s_barrier\n"
"ds_read_b64_tr_b16 a[180:181], v221 offset:16400\n"
"v_mfma_f32_16x16x32_bf16 v[32:35], a[148:151], v[176:179], v[32:35]\n"
"ds_read_b64_tr_b16 a[182:183], v221 offset:16656\n"
"v_mfma_f32_16x16x32_bf16 v[32:35], a[152:155], v[192:195], v[32:35]\n"
"ds_read_b64_tr_b16 a[184:185], v221 offset:20496\n"
"v_mfma_f32_16x16x32_bf16 v[40:43], a[152:155], a[52:55], v[40:43]\n"
"ds_read_b64_tr_b16 a[186:187], v221 offset:20752\n"
"v_mfma_f32_16x16x32_bf16 v[40:43], a[148:151], v[180:183], v[40:43]\n"
"buffer_load_dwordx4 a[24:27], v216, s[68:71], s84 offen sc1\n"
"v_mfma_f32_16x16x32_bf16 v[48:51], a[148:151], v[184:187], v[48:51]\n"
"ds_read_b64_tr_b16 a[188:189], v221 offset:16408\n"
"v_mfma_f32_16x16x32_bf16 v[48:51], a[152:155], a[84:87], v[48:51]\n"
"ds_read_b64_tr_b16 a[190:191], v221 offset:16664\n"
"v_mfma_f32_16x16x32_bf16 v[56:59], a[152:155], v[196:199], v[56:59]\n"
"ds_read_b64_tr_b16 a[192:193], v221 offset:20504\n"
"v_mfma_f32_16x16x32_bf16 v[56:59], a[148:151], v[188:191], v[56:59]\n"
"ds_read_b64_tr_b16 a[194:195], v221 offset:20760\n"
"v_mfma_f32_16x16x32_bf16 v[60:63], a[156:159], v[188:191], v[60:63]\n"
"ds_read_b64_tr_b16 a[196:197], v221 offset:17424\n"
"v_mfma_f32_16x16x32_bf16 v[60:63], a[160:163], v[196:199], v[60:63]\n"
"buffer_load_dwordx4 a[56:59], v216, s[68:71], s85 offen sc1\n"
"v_mfma_f32_16x16x32_bf16 v[52:55], a[160:163], a[84:87], v[52:55]\n"
"ds_read_b64_tr_b16 a[198:199], v221 offset:17680\n"
"v_mfma_f32_16x16x32_bf16 v[52:55], a[156:159], v[184:187], v[52:55]\n"
"ds_read_b64_tr_b16 a[200:201], v221 offset:21520\n"
"v_mfma_f32_16x16x32_bf16 v[44:47], a[156:159], v[180:183], v[44:47]\n"
"ds_read_b64_tr_b16 a[202:203], v221 offset:21776\n"
"v_mfma_f32_16x16x32_bf16 v[44:47], a[160:163], a[52:55], v[44:47]\n"
"ds_read_b64_tr_b16 a[204:205], v221 offset:17432\n"
"v_mfma_f32_16x16x32_bf16 v[36:39], a[160:163], v[192:195], v[36:39]\n"
"ds_read_b64_tr_b16 a[206:207], v221 offset:17688\n"
"v_mfma_f32_16x16x32_bf16 v[36:39], a[156:159], v[176:179], v[36:39]\n"
"buffer_load_dwordx4 v[152:155], v216, s[68:71], s86 offen sc1\n"
"v_mfma_f32_16x16x32_bf16 v[96:99], a[164:167], v[176:179], v[96:99]\n"
"ds_read_b64_tr_b16 a[208:209], v221 offset:21528\n"
"v_mfma_f32_16x16x32_bf16 v[96:99], a[168:171], v[192:195], v[96:99]\n"
"ds_read_b64_tr_b16 a[210:211], v221 offset:21784\n"
"v_mfma_f32_16x16x32_bf16 v[104:107], a[168:171], a[52:55], v[104:107]\n"
"v_mfma_f32_16x16x32_bf16 v[104:107], a[164:167], v[180:183], v[104:107]\n"
"v_mfma_f32_16x16x32_bf16 v[112:115], a[164:167], v[184:187], v[112:115]\n"
"v_mfma_f32_16x16x32_bf16 v[112:115], a[168:171], a[84:87], v[112:115]\n"
"buffer_load_dwordx4 v[156:159], v216, s[68:71], s87 offen sc1\n"
"v_mfma_f32_16x16x32_bf16 v[120:123], a[168:171], v[196:199], v[120:123]\n"
"v_mfma_f32_16x16x32_bf16 v[120:123], a[164:167], v[188:191], v[120:123]\n"
"v_mfma_f32_16x16x32_bf16 v[124:127], a[172:175], v[188:191], v[124:127]\n"
"v_mfma_f32_16x16x32_bf16 v[124:127], a[176:179], v[196:199], v[124:127]\n"
"v_mfma_f32_16x16x32_bf16 v[116:119], a[176:179], a[84:87], v[116:119]\n"
"v_mfma_f32_16x16x32_bf16 v[116:119], a[172:175], v[184:187], v[116:119]\n"
"buffer_load_dwordx4 v216, s[60:63], s83 offen sc1 lds\n"
"v_mfma_f32_16x16x32_bf16 v[108:111], a[172:175], v[180:183], v[108:111]\n"
"v_mfma_f32_16x16x32_bf16 v[108:111], a[176:179], a[52:55], v[108:111]\n"
"v_mfma_f32_16x16x32_bf16 v[100:103], a[176:179], v[192:195], v[100:103]\n"
"v_mfma_f32_16x16x32_bf16 v[100:103], a[172:175], v[176:179], v[100:103]\n"
"s_waitcnt lgkmcnt(0)\n"
"ds_read_b64_tr_b16 a[148:149], v221 offset:16912\n"
"s_lshr_b32 s92, s12, 1\n"
"v_mfma_f32_16x16x32_bf16 v[0:3], a[180:183], v[200:203], v[0:3]\n"
"s_waitcnt vmcnt(9)\n"
"ds_read_b64_tr_b16 a[150:151], v221 offset:17168\n"
"s_mul_i32 s92, s92, 320\n"
"v_mfma_f32_16x16x32_bf16 v[0:3], a[184:187], v[132:135], v[0:3]\n"
"s_waitcnt vmcnt(8)\n"
"buffer_load_dwordx4 v216, s[60:63], s83 offen offset:1024 sc1 lds\n"
"v_mfma_f32_16x16x32_bf16 v[8:11], a[184:187], v[136:139], v[8:11]\n"
"s_mul_i32 s93, s26, 20480\n"
"ds_read_b64_tr_b16 a[152:153], v221 offset:21008\n"
"s_add_u32 s92, s92, s93\n"
"v_mfma_f32_16x16x32_bf16 v[8:11], a[180:183], v[204:207], v[8:11]\n"
"s_and_b32 s93, s12, 1\n"
"ds_read_b64_tr_b16 a[154:155], v221 offset:21264\n"
"s_mul_i32 s94, s93, 192\n"
"v_mfma_f32_16x16x32_bf16 v[16:19], a[180:183], v[208:211], v[16:19]\n"
"s_waitcnt vmcnt(7)\n"
"ds_read_b64_tr_b16 a[156:157], v221 offset:16920\n"
"s_add_u32 s45, s92, s94\n"
"v_mfma_f32_16x16x32_bf16 v[16:19], a[184:187], v[140:143], v[16:19]\n"
"s_waitcnt vmcnt(6)\n"
"ds_read_b64_tr_b16 a[158:159], v221 offset:17176\n"
"s_lshl_b32 s94, s93, 3\n"
"v_mfma_f32_16x16x32_bf16 v[24:27], a[184:187], a[116:119], v[24:27]\n"
"s_add_u32 s46, s92, s94\n"
"ds_read_b64_tr_b16 a[160:161], v221 offset:21016\n"
"v_mfma_f32_16x16x32_bf16 v[24:27], a[180:183], v[128:131], v[24:27]\n"
"buffer_load_dwordx4 v216, s[60:63], s83 offen offset:2048 sc1 lds\n"
"v_mfma_f32_16x16x32_bf16 v[28:31], a[188:191], v[128:131], v[28:31]\n"
"ds_read_b64_tr_b16 a[162:163], v221 offset:21272\n"
"v_mfma_f32_16x16x32_bf16 v[28:31], a[192:195], a[116:119], v[28:31]\n"
"ds_read_b64_tr_b16 a[164:165], v221 offset:17936\n"
"v_mfma_f32_16x16x32_bf16 v[20:23], a[192:195], v[140:143], v[20:23]\n"
"ds_read_b64_tr_b16 a[166:167], v221 offset:18192\n"
"v_mfma_f32_16x16x32_bf16 v[20:23], a[188:191], v[208:211], v[20:23]\n"
"ds_read_b64_tr_b16 a[168:169], v221 offset:22032\n"
"v_mfma_f32_16x16x32_bf16 v[12:15], a[188:191], v[204:207], v[12:15]\n"
"ds_read_b64_tr_b16 a[170:171], v221 offset:22288\n"
"v_mfma_f32_16x16x32_bf16 v[12:15], a[192:195], v[136:139], v[12:15]\n"
"buffer_load_dwordx4 v216, s[60:63], s83 offen offset:3072 sc1 lds\n"
"v_mfma_f32_16x16x32_bf16 v[4:7], a[192:195], v[132:135], v[4:7]\n"
"s_addk_i32 m0, 1024\n"
"ds_read_b64_tr_b16 a[172:173], v221 offset:17944\n"
"s_cmp_gt_u32 s11, 1\n"
"s_cselect_b32 s83, 1, 2\n"
"v_mfma_f32_16x16x32_bf16 v[4:7], a[188:191], v[200:203], v[4:7]\n"
"s_mul_i32 s83, s83, 20480\n"
"ds_read_b64_tr_b16 a[174:175], v221 offset:18200\n"
"s_add_u32 s83, s83, s25\n"
"v_mfma_f32_16x16x32_bf16 v[64:67], a[196:199], v[200:203], v[64:67]\n"
"ds_read_b64_tr_b16 a[176:177], v221 offset:22040\n"
"v_mfma_f32_16x16x32_bf16 v[64:67], a[200:203], v[132:135], v[64:67]\n"
"ds_read_b64_tr_b16 a[178:179], v221 offset:22296\n"
"v_mfma_f32_16x16x32_bf16 v[72:75], a[200:203], v[136:139], v[72:75]\n"
"v_mfma_f32_16x16x32_bf16 v[72:75], a[196:199], v[204:207], v[72:75]\n"
"buffer_load_dwordx4 v216, s[60:63], s82 offen offset:3072 sc1 lds\n"
"v_mfma_f32_16x16x32_bf16 v[80:83], a[196:199], v[208:211], v[80:83]\n"
"s_add_u32 m0, s78, 0\n"
"s_add_u32 s82, s83, 1024\n"
"v_mfma_f32_16x16x32_bf16 v[80:83], a[200:203], v[140:143], v[80:83]\n"
"v_mfma_f32_16x16x32_bf16 v[88:91], a[200:203], a[116:119], v[88:91]\n"
"v_mfma_f32_16x16x32_bf16 v[88:91], a[196:199], v[128:131], v[88:91]\n"
"v_mfma_f32_16x16x32_bf16 v[92:95], a[204:207], v[128:131], v[92:95]\n"
"buffer_load_dwordx4 a[16:19], v216, s[68:71], s84 offen offset:1024 sc1\n"
"v_mfma_f32_16x16x32_bf16 v[92:95], a[208:211], a[116:119], v[92:95]\n"
"buffer_load_dwordx4 a[48:51], v216, s[68:71], s85 offen offset:1024 sc1\n"
"v_mfma_f32_16x16x32_bf16 v[84:87], a[208:211], v[140:143], v[84:87]\n"
"v_mfma_f32_16x16x32_bf16 v[84:87], a[204:207], v[208:211], v[84:87]\n"
"v_mfma_f32_16x16x32_bf16 v[76:79], a[204:207], v[204:207], v[76:79]\n"
"buffer_load_dwordx4 a[128:131], v216, s[68:71], s86 offen offset:1024 sc1\n"
"v_mfma_f32_16x16x32_bf16 v[76:79], a[208:211], v[136:139], v[76:79]\n"
"buffer_load_dwordx4 a[132:135], v216, s[68:71], s87 offen offset:1024 sc1\n"
"v_mfma_f32_16x16x32_bf16 v[68:71], a[208:211], v[132:135], v[68:71]\n"
"v_mfma_f32_16x16x32_bf16 v[68:71], a[204:207], v[200:203], v[68:71]\n"
"s_waitcnt lgkmcnt(0)\n"
"s_and_b32 s3, s73, 199\n"
"s_lshl_b32 s93, s72, 8\n"
"v_mfma_f32_16x16x32_bf16 v[32:35], a[148:151], v[200:203], v[32:35]\n"
"s_barrier\n"
"ds_read_b64_tr_b16 a[180:181], v221 offset:32800\n"
"s_add_u32 s3, s3, s93\n"
"v_mfma_f32_16x16x32_bf16 v[32:35], a[152:155], v[132:135], v[32:35]\n"
"s_lshl_b32 s3, s3, 7\n"
"ds_read_b64_tr_b16 a[182:183], v221 offset:33056\n"
"s_add_u32 s3, s3, 393216\n"
"v_mfma_f32_16x16x32_bf16 v[40:43], a[152:155], v[136:139], v[40:43]\n"
"ds_read_b64_tr_b16 a[184:185], v221 offset:36896\n"
"v_and_b32 v239, 7, v253\n"
"v_mfma_f32_16x16x32_bf16 v[40:43], a[148:151], v[204:207], v[40:43]\n"
"buffer_load_dwordx4 a[88:91], v216, s[68:71], s84 offen offset:2048 sc1\n"
"v_mfma_f32_16x16x32_bf16 v[48:51], a[148:151], v[208:211], v[48:51]\n"
"ds_read_b64_tr_b16 a[186:187], v221 offset:37152\n"
"v_lshlrev_b32 v239, 10, v239\n"
"v_mfma_f32_16x16x32_bf16 v[48:51], a[152:155], v[140:143], v[48:51]\n"
"buffer_load_dwordx2 v[240:241], v239, s[60:63], s3 offen sc1\n"
"v_mfma_f32_16x16x32_bf16 v[56:59], a[152:155], a[116:119], v[56:59]\n"
"ds_read_b64_tr_b16 a[188:189], v221 offset:32808\n"
"v_mfma_f32_16x16x32_bf16 v[56:59], a[148:151], v[128:131], v[56:59]\n"
"ds_read_b64_tr_b16 a[190:191], v221 offset:33064\n"
"v_mfma_f32_16x16x32_bf16 v[60:63], a[156:159], v[128:131], v[60:63]\n"
"ds_read_b64_tr_b16 a[192:193], v221 offset:36904\n"
"v_mfma_f32_16x16x32_bf16 v[60:63], a[160:163], a[116:119], v[60:63]\n"
"ds_read_b64_tr_b16 a[194:195], v221 offset:37160\n"
"v_mfma_f32_16x16x32_bf16 v[52:55], a[160:163], v[140:143], v[52:55]\n"
"ds_read_b64_tr_b16 a[196:197], v221 offset:33824\n"
"v_mfma_f32_16x16x32_bf16 v[52:55], a[156:159], v[208:211], v[52:55]\n"
"buffer_load_dwordx4 a[120:123], v216, s[68:71], s85 offen offset:2048 sc1\n"
"v_mfma_f32_16x16x32_bf16 v[44:47], a[156:159], v[204:207], v[44:47]\n"
"ds_read_b64_tr_b16 a[198:199], v221 offset:34080\n"
"v_mfma_f32_16x16x32_bf16 v[44:47], a[160:163], v[136:139], v[44:47]\n"
"ds_read_b64_tr_b16 a[200:201], v221 offset:37920\n"
"v_mfma_f32_16x16x32_bf16 v[36:39], a[160:163], v[132:135], v[36:39]\n"
"ds_read_b64_tr_b16 a[202:203], v221 offset:38176\n"
"v_mfma_f32_16x16x32_bf16 v[36:39], a[156:159], v[200:203], v[36:39]\n"
"ds_read_b64_tr_b16 a[204:205], v221 offset:33832\n"
"v_mfma_f32_16x16x32_bf16 v[96:99], a[164:167], v[200:203], v[96:99]\n"
"ds_read_b64_tr_b16 a[206:207], v221 offset:34088\n"
"v_mfma_f32_16x16x32_bf16 v[96:99], a[168:171], v[132:135], v[96:99]\n"
"buffer_load_dwordx4 a[28:31], v216, s[68:71], s86 offen offset:2048 sc1\n"
"v_mfma_f32_16x16x32_bf16 v[104:107], a[168:171], v[136:139], v[104:107]\n"
"ds_read_b64_tr_b16 a[208:209], v221 offset:37928\n"
"v_mfma_f32_16x16x32_bf16 v[104:107], a[164:167], v[204:207], v[104:107]\n"
"ds_read_b64_tr_b16 a[210:211], v221 offset:38184\n"
"v_mfma_f32_16x16x32_bf16 v[112:115], a[164:167], v[208:211], v[112:115]\n"
"v_mfma_f32_16x16x32_bf16 v[112:115], a[168:171], v[140:143], v[112:115]\n"
"v_mfma_f32_16x16x32_bf16 v[120:123], a[168:171], a[116:119], v[120:123]\n"
"v_mfma_f32_16x16x32_bf16 v[120:123], a[164:167], v[128:131], v[120:123]\n"
"buffer_load_dwordx4 a[136:139], v216, s[68:71], s87 offen offset:2048 sc1\n"
"v_mfma_f32_16x16x32_bf16 v[124:127], a[172:175], v[128:131], v[124:127]\n"
"buffer_load_dwordx4 a[140:143], v216, s[68:71], s84 offen offset:3072 sc1\n"
"v_mfma_f32_16x16x32_bf16 v[124:127], a[176:179], a[116:119], v[124:127]\n"
"s_addk_i32 s84, 4096\n"
"v_mfma_f32_16x16x32_bf16 v[116:119], a[176:179], v[140:143], v[116:119]\n"
"v_mfma_f32_16x16x32_bf16 v[116:119], a[172:175], v[208:211], v[116:119]\n"
"v_mfma_f32_16x16x32_bf16 v[108:111], a[172:175], v[204:207], v[108:111]\n"
"v_mfma_f32_16x16x32_bf16 v[108:111], a[176:179], v[136:139], v[108:111]\n"
"v_mfma_f32_16x16x32_bf16 v[100:103], a[176:179], v[132:135], v[100:103]\n"
"buffer_load_dwordx4 a[212:215], v216, s[68:71], s85 offen offset:3072 sc1\n"
"v_mfma_f32_16x16x32_bf16 v[100:103], a[172:175], v[200:203], v[100:103]\n"
"s_waitcnt vmcnt(19) lgkmcnt(0)\n"
"ds_read_b64_tr_b16 a[148:149], v220 offset:32800\n"
"s_addk_i32 s85, 4096\n"
"v_mfma_f32_16x16x32_bf16 v[0:3], a[180:183], a[24:27], v[0:3]\n"
"s_waitcnt vmcnt(10)\n"
"ds_read_b64_tr_b16 a[150:151], v220 offset:33056\n"
"v_mfma_f32_16x16x32_bf16 v[0:3], a[184:187], a[16:19], v[0:3]\n"
"s_waitcnt vmcnt(9)\n"
"ds_read_b64_tr_b16 a[152:153], v220 offset:36896\n"
"v_mfma_f32_16x16x32_bf16 v[8:11], a[184:187], a[48:51], v[8:11]\n"
"ds_read_b64_tr_b16 a[154:155], v220 offset:37152\n"
"v_mfma_f32_16x16x32_bf16 v[8:11], a[180:183], a[56:59], v[8:11]\n"
"ds_read_b64_tr_b16 a[156:157], v220 offset:32808\n"
"v_mfma_f32_16x16x32_bf16 v[16:19], a[180:183], v[152:155], v[16:19]\n"
"s_waitcnt vmcnt(8)\n"
"buffer_load_dwordx4 a[216:219], v216, s[68:71], s86 offen offset:3072 sc1\n"
"v_mfma_f32_16x16x32_bf16 v[16:19], a[184:187], a[128:131], v[16:19]\n"
"s_waitcnt vmcnt(8)\n"
"ds_read_b64_tr_b16 a[158:159], v220 offset:33064\n"
"s_addk_i32 s86, 4096\n"
"v_mfma_f32_16x16x32_bf16 v[24:27], a[184:187], a[132:135], v[24:27]\n"
"ds_read_b64_tr_b16 a[160:161], v220 offset:36904\n"
"v_mfma_f32_16x16x32_bf16 v[24:27], a[180:183], v[156:159], v[24:27]\n"
"ds_read_b64_tr_b16 a[162:163], v220 offset:37160\n"
"v_mfma_f32_16x16x32_bf16 v[28:31], a[188:191], v[156:159], v[28:31]\n"
"ds_read_b64_tr_b16 a[164:165], v220 offset:33824\n"
"v_mfma_f32_16x16x32_bf16 v[28:31], a[192:195], a[132:135], v[28:31]\n"
"ds_read_b64_tr_b16 a[166:167], v220 offset:34080\n"
"v_mfma_f32_16x16x32_bf16 v[20:23], a[192:195], a[128:131], v[20:23]\n"
"buffer_load_dwordx4 a[220:223], v216, s[68:71], s87 offen offset:3072 sc1\n"
"v_mfma_f32_16x16x32_bf16 v[20:23], a[188:191], v[152:155], v[20:23]\n"
"s_addk_i32 s87, 4096\n"
"ds_read_b64_tr_b16 a[168:169], v220 offset:37920\n"
"v_mfma_f32_16x16x32_bf16 v[12:15], a[188:191], a[56:59], v[12:15]\n"
"ds_read_b64_tr_b16 a[170:171], v220 offset:38176\n"
"v_mfma_f32_16x16x32_bf16 v[12:15], a[192:195], a[48:51], v[12:15]\n"
"ds_read_b64_tr_b16 a[172:173], v220 offset:33832\n"
"v_mfma_f32_16x16x32_bf16 v[4:7], a[192:195], a[16:19], v[4:7]\n"
"ds_read_b64_tr_b16 a[174:175], v220 offset:34088\n"
"v_mfma_f32_16x16x32_bf16 v[4:7], a[188:191], a[24:27], v[4:7]\n"
"ds_read_b64_tr_b16 a[176:177], v220 offset:37928\n"
"v_mfma_f32_16x16x32_bf16 v[64:67], a[196:199], a[24:27], v[64:67]\n"
"buffer_load_dwordx4 v216, s[60:63], s83 offen sc1 lds\n"
"v_mfma_f32_16x16x32_bf16 v[64:67], a[200:203], a[16:19], v[64:67]\n"
"ds_read_b64_tr_b16 a[178:179], v220 offset:38184\n"
"v_mfma_f32_16x16x32_bf16 v[72:75], a[200:203], a[48:51], v[72:75]\n"
"v_mfma_f32_16x16x32_bf16 v[72:75], a[196:199], a[56:59], v[72:75]\n"
"v_mfma_f32_16x16x32_bf16 v[80:83], a[196:199], v[152:155], v[80:83]\n"
"v_mfma_f32_16x16x32_bf16 v[80:83], a[200:203], a[128:131], v[80:83]\n"
"v_mfma_f32_16x16x32_bf16 v[88:91], a[200:203], a[132:135], v[88:91]\n"
"buffer_load_dwordx4 v216, s[60:63], s83 offen offset:1024 sc1 lds\n"
"v_mfma_f32_16x16x32_bf16 v[88:91], a[196:199], v[156:159], v[88:91]\n"
"v_mfma_f32_16x16x32_bf16 v[92:95], a[204:207], v[156:159], v[92:95]\n"
"v_mfma_f32_16x16x32_bf16 v[92:95], a[208:211], a[132:135], v[92:95]\n"
"v_mfma_f32_16x16x32_bf16 v[84:87], a[208:211], a[128:131], v[84:87]\n"
"v_mfma_f32_16x16x32_bf16 v[84:87], a[204:207], v[152:155], v[84:87]\n"
"v_mfma_f32_16x16x32_bf16 v[76:79], a[204:207], a[56:59], v[76:79]\n"
"buffer_load_dwordx4 v216, s[60:63], s83 offen offset:2048 sc1 lds\n"
"v_mfma_f32_16x16x32_bf16 v[76:79], a[208:211], a[48:51], v[76:79]\n"
"v_mfma_f32_16x16x32_bf16 v[68:71], a[208:211], a[16:19], v[68:71]\n"
"v_mfma_f32_16x16x32_bf16 v[68:71], a[204:207], a[24:27], v[68:71]\n"
"s_waitcnt vmcnt(10) lgkmcnt(0)\n"
"v_cmp_eq_u64_e64 s[4:5], v[240:241], s[80:81]\n"
"s_nop 1\n"
"s_cmp_eq_u64 s[4:5], -1\n"
"s_cbranch_scc0 L_spinO_%=\n"
"L_retO_%=:\n"
"v_mfma_f32_16x16x32_bf16 v[0:3], a[148:151], a[88:91], v[0:3]\n"
"s_waitcnt vmcnt(6)\n"
"ds_read_b64_tr_b16 a[180:181], v221 offset:33312\n"
"s_cmp_gt_u32 s11, 0\n"
"s_cselect_b32 s92, 0, 1\n"
"v_mfma_f32_16x16x32_bf16 v[0:3], a[152:155], a[140:143], v[0:3]\n"
"s_lshl_b32 s92, s92, 12\n"
"ds_read_b64_tr_b16 a[182:183], v221 offset:33568\n"
"s_add_u32 s84, s16, s92\n"
"v_mfma_f32_16x16x32_bf16 v[4:7], a[156:159], a[88:91], v[4:7]\n"
"s_add_u32 s85, s17, s92\n"
"buffer_load_dwordx4 v216, s[60:63], s83 offen offset:3072 sc1 lds\n"
"v_mfma_f32_16x16x32_bf16 v[4:7], a[160:163], a[140:143], v[4:7]\n"
"s_addk_i32 m0, 1024\n"
"ds_read_b64_tr_b16 a[184:185], v221 offset:37408\n"
"s_add_u32 s86, s18, s92\n"
"v_mfma_f32_16x16x32_bf16 v[64:67], a[164:167], a[88:91], v[64:67]\n"
"s_add_u32 s87, s19, s92\n"
"ds_read_b64_tr_b16 a[186:187], v221 offset:37664\n"
"s_cmp_gt_u32 s11, 2\n"
"s_cselect_b32 s83, 2, 3\n"
"v_mfma_f32_16x16x32_bf16 v[64:67], a[168:171], a[140:143], v[64:67]\n"
"s_mul_i32 s83, s83, 20480\n"
"ds_read_b64_tr_b16 a[188:189], v221 offset:33320\n"
"s_add_u32 s83, s83, s25\n"
"v_mfma_f32_16x16x32_bf16 v[68:71], a[172:175], a[88:91], v[68:71]\n"
"ds_read_b64_tr_b16 a[190:191], v221 offset:33576\n"
"v_mfma_f32_16x16x32_bf16 v[68:71], a[176:179], a[140:143], v[68:71]\n"
"ds_read_b64_tr_b16 a[192:193], v221 offset:37416\n"
"v_mfma_f32_16x16x32_bf16 v[8:11], a[148:151], a[120:123], v[8:11]\n"
"s_waitcnt vmcnt(6)\n"
"buffer_load_dwordx4 v216, s[60:63], s82 offen offset:3072 sc1 lds\n"
"v_mfma_f32_16x16x32_bf16 v[8:11], a[152:155], a[212:215], v[8:11]\n"
"s_addk_i32 m0, 19456\n"
"ds_read_b64_tr_b16 a[194:195], v221 offset:37672\n"
"s_add_u32 s82, s83, 1024\n"
"v_mfma_f32_16x16x32_bf16 v[12:15], a[156:159], a[120:123], v[12:15]\n"
"ds_read_b64_tr_b16 a[196:197], v221 offset:34336\n"
"v_mfma_f32_16x16x32_bf16 v[12:15], a[160:163], a[212:215], v[12:15]\n"
"ds_read_b64_tr_b16 a[198:199], v221 offset:34592\n"
"v_mfma_f32_16x16x32_bf16 v[72:75], a[164:167], a[120:123], v[72:75]\n"
"ds_read_b64_tr_b16 a[200:201], v221 offset:38432\n"
"v_mfma_f32_16x16x32_bf16 v[72:75], a[168:171], a[212:215], v[72:75]\n"
"ds_read_b64_tr_b16 a[202:203], v221 offset:38688\n"
"v_mfma_f32_16x16x32_bf16 v[76:79], a[172:175], a[120:123], v[76:79]\n"
"buffer_load_dwordx4 v216, s[60:63], s83 offen sc1 lds\n"
"v_mfma_f32_16x16x32_bf16 v[76:79], a[176:179], a[212:215], v[76:79]\n"
"ds_read_b64_tr_b16 a[204:205], v221 offset:34344\n"
"v_mfma_f32_16x16x32_bf16 v[16:19], a[148:151], a[28:31], v[16:19]\n"
"s_waitcnt vmcnt(7) lgkmcnt(9)\n"
"ds_read_b64_tr_b16 a[206:207], v221 offset:34600\n"
"v_mfma_f32_16x16x32_bf16 v[16:19], a[152:155], a[216:219], v[16:19]\n"
"ds_read_b64_tr_b16 a[208:209], v221 offset:38440\n"
"v_mfma_f32_16x16x32_bf16 v[20:23], a[156:159], a[28:31], v[20:23]\n"
"ds_read_b64_tr_b16 a[210:211], v221 offset:38696\n"
"v_mfma_f32_16x16x32_bf16 v[20:23], a[160:163], a[216:219], v[20:23]\n"
"v_mfma_f32_16x16x32_bf16 v[80:83], a[164:167], a[28:31], v[80:83]\n"
"buffer_load_dwordx4 v216, s[60:63], s83 offen offset:1024 sc1 lds\n"
"v_mfma_f32_16x16x32_bf16 v[80:83], a[168:171], a[216:219], v[80:83]\n"
"v_mfma_f32_16x16x32_bf16 v[84:87], a[172:175], a[28:31], v[84:87]\n"
"v_mfma_f32_16x16x32_bf16 v[84:87], a[176:179], a[216:219], v[84:87]\n"
"v_mfma_f32_16x16x32_bf16 v[24:27], a[148:151], a[136:139], v[24:27]\n"
"s_waitcnt vmcnt(7) lgkmcnt(5)\n"
"v_mfma_f32_16x16x32_bf16 v[24:27], a[152:155], a[220:223], v[24:27]\n"
"v_mfma_f32_16x16x32_bf16 v[28:31], a[156:159], a[136:139], v[28:31]\n"
"buffer_load_dwordx4 v216, s[60:63], s83 offen offset:2048 sc1 lds\n"
"v_mfma_f32_16x16x32_bf16 v[28:31], a[160:163], a[220:223], v[28:31]\n"
"v_mfma_f32_16x16x32_bf16 v[88:91], a[164:167], a[136:139], v[88:91]\n"
"v_mfma_f32_16x16x32_bf16 v[88:91], a[168:171], a[220:223], v[88:91]\n"
"v_mfma_f32_16x16x32_bf16 v[92:95], a[172:175], a[136:139], v[92:95]\n"
"v_mfma_f32_16x16x32_bf16 v[92:95], a[176:179], a[220:223], v[92:95]\n"
"s_waitcnt lgkmcnt(0)\n"
"ds_read_b64_tr_b16 a[148:149], v220 offset:33312\n"
"v_mul_f32 v128, s0, v0\n"
"v_mfma_f32_16x16x32_bf16 v[32:35], a[180:183], a[24:27], v[32:35]\n"
"s_barrier\n"
"buffer_load_dwordx4 v216, s[60:63], s83 offen offset:3072 sc1 lds\n"
"v_mfma_f32_16x16x32_bf16 v[32:35], a[184:187], a[16:19], v[32:35]\n"
"s_addk_i32 m0, 1024\n"
"ds_read_b64_tr_b16 a[150:151], v220 offset:33568\n"
"v_mul_f32 v129, s0, v1\n"
"v_mfma_f32_16x16x32_bf16 v[40:43], a[184:187], a[48:51], v[40:43]\n"
"s_cmp_gt_u32 s11, 3\n"
"s_cselect_b32 s83, 3, 4\n"
"ds_read_b64_tr_b16 a[152:153], v220 offset:37408\n"
"v_mul_f32 v130, s0, v2\n"
"v_mfma_f32_16x16x32_bf16 v[40:43], a[180:183], a[56:59], v[40:43]\n"
"s_mul_i32 s83, s83, 20480\n"
"ds_read_b64_tr_b16 a[154:155], v220 offset:37664\n"
"v_mul_f32 v131, s0, v3\n"
"v_mfma_f32_16x16x32_bf16 v[48:51], a[180:183], v[152:155], v[48:51]\n"
"s_add_u32 s83, s83, s25\n"
"v_exp_f32 v128, v128\n"
"v_mfma_f32_16x16x32_bf16 v[48:51], a[184:187], a[128:131], v[48:51]\n"
"ds_read_b64_tr_b16 a[156:157], v220 offset:33320\n"
"v_mul_f32 v64, v0, v64\n"
"v_mfma_f32_16x16x32_bf16 v[56:59], a[184:187], a[132:135], v[56:59]\n"
"buffer_load_dwordx4 v216, s[60:63], s82 offen offset:3072 sc1 lds\n"
"v_mfma_f32_16x16x32_bf16 v[56:59], a[180:183], v[156:159], v[56:59]\n"
"s_add_u32 m0, s78, 102400\n"
"v_exp_f32 v129, v129\n"
"v_mfma_f32_16x16x32_bf16 v[60:63], a[188:191], v[156:159], v[60:63]\n"
"s_add_u32 s82, s83, 1024\n"
"ds_read_b64_tr_b16 a[158:159], v220 offset:33576\n"
"v_mul_f32 v65, v1, v65\n"
"v_mfma_f32_16x16x32_bf16 v[60:63], a[192:195], a[132:135], v[60:63]\n"
"v_exp_f32 v130, v130\n"
"v_mfma_f32_16x16x32_bf16 v[52:55], a[192:195], a[128:131], v[52:55]\n"
"ds_read_b64_tr_b16 a[160:161], v220 offset:37416\n"
"v_mul_f32 v66, v2, v66\n"
"v_mfma_f32_16x16x32_bf16 v[52:55], a[188:191], v[152:155], v[52:55]\n"
"v_exp_f32 v131, v131\n"
"v_mfma_f32_16x16x32_bf16 v[44:47], a[188:191], a[56:59], v[44:47]\n"
"buffer_load_dwordx4 v[242:245], v230, s[28:31], s45 offen\n"
"v_mfma_f32_16x16x32_bf16 v[44:47], a[192:195], a[48:51], v[44:47]\n"
"ds_read_b64_tr_b16 a[162:163], v220 offset:37672\n"
"v_mul_f32 v67, v3, v67\n"
"v_mfma_f32_16x16x32_bf16 v[36:39], a[192:195], a[16:19], v[36:39]\n"
"ds_read_b64_tr_b16 a[164:165], v220 offset:34336\n"
"v_add_f32 v128, 1.0, v128\n"
"v_mfma_f32_16x16x32_bf16 v[36:39], a[188:191], a[24:27], v[36:39]\n"
"ds_read_b64_tr_b16 a[166:167], v220 offset:34592\n"
"v_add_f32 v129, 1.0, v129\n"
"v_mfma_f32_16x16x32_bf16 v[96:99], a[196:199], a[24:27], v[96:99]\n"
"ds_read_b64_tr_b16 a[168:169], v220 offset:38432\n"
"v_add_f32 v130, 1.0, v130\n"
"v_mfma_f32_16x16x32_bf16 v[96:99], a[200:203], a[16:19], v[96:99]\n"
"ds_read_b64_tr_b16 a[170:171], v220 offset:38688\n"
"v_add_f32 v131, 1.0, v131\n"
"v_mfma_f32_16x16x32_bf16 v[104:107], a[200:203], a[48:51], v[104:107]\n"
"buffer_load_dwordx4 v[172:175], v230, s[28:31], s45 offen offset:64\n"
"v_mfma_f32_16x16x32_bf16 v[104:107], a[196:199], a[56:59], v[104:107]\n"
"v_rcp_f32 v128, v128\n"
"v_mfma_f32_16x16x32_bf16 v[112:115], a[196:199], v[152:155], v[112:115]\n"
"v_rcp_f32 v129, v129\n"
"v_mfma_f32_16x16x32_bf16 v[112:115], a[200:203], a[128:131], v[112:115]\n"
"v_rcp_f32 v130, v130\n"
"v_mfma_f32_16x16x32_bf16 v[120:123], a[200:203], a[132:135], v[120:123]\n"
"v_rcp_f32 v131, v131\n"
"v_mfma_f32_16x16x32_bf16 v[120:123], a[196:199], v[156:159], v[120:123]\n"
"ds_read_b64_tr_b16 a[172:173], v220 offset:34344\n"
"v_mul_f32 v128, v128, v64\n"
"v_mfma_f32_16x16x32_bf16 v[124:127], a[204:207], v[156:159], v[124:127]\n"
"buffer_load_dwordx2 v[236:237], v230, s[28:31], s46 offen offset:128\n"
"v_mfma_f32_16x16x32_bf16 v[124:127], a[208:211], a[132:135], v[124:127]\n"
"ds_read_b64_tr_b16 a[174:175], v220 offset:34600\n"
"v_mul_f32 v129, v129, v65\n"
"v_mfma_f32_16x16x32_bf16 v[116:119], a[208:211], a[128:131], v[116:119]\n"
"ds_read_b64_tr_b16 a[176:177], v220 offset:38440\n"
"v_mul_f32 v130, v130, v66\n"
"v_mfma_f32_16x16x32_bf16 v[116:119], a[204:207], v[152:155], v[116:119]\n"
"ds_read_b64_tr_b16 a[178:179], v220 offset:38696\n"
"v_mul_f32 v131, v131, v67\n"
"v_mfma_f32_16x16x32_bf16 v[108:111], a[204:207], a[56:59], v[108:111]\n"
"v_mul_f32 v132, s0, v4\n"
"v_mul_f32 v133, s0, v5\n"
"v_mfma_f32_16x16x32_bf16 v[108:111], a[208:211], a[48:51], v[108:111]\n"
"v_mul_f32 v134, s0, v6\n"
"v_mul_f32 v135, s0, v7\n"
"v_mfma_f32_16x16x32_bf16 v[100:103], a[208:211], a[16:19], v[100:103]\n"
"v_exp_f32 v132, v132\n"
"v_mfma_f32_16x16x32_bf16 v[100:103], a[204:207], a[24:27], v[100:103]\n"
"s_waitcnt lgkmcnt(4)\n"
"buffer_load_dwordx4 v216, s[60:63], s83 offen sc1 lds\n"
"v_mfma_f32_16x16x32_bf16 v[32:35], a[148:151], a[88:91], v[32:35]\n"
"s_cmp_gt_u32 s11, 1\n"
"s_cselect_b32 s92, 1, 2\n"
"v_mul_f32 v68, v4, v68\n"
"v_mul_f32 v69, v5, v69\n"
"v_mfma_f32_16x16x32_bf16 v[32:35], a[152:155], a[140:143], v[32:35]\n"
"s_lshl_b32 s92, s92, 12\n"
"v_exp_f32 v133, v133\n"
"v_mfma_f32_16x16x32_bf16 v[36:39], a[156:159], a[88:91], v[36:39]\n"
"v_exp_f32 v134, v134\n"
"v_mfma_f32_16x16x32_bf16 v[36:39], a[160:163], a[140:143], v[36:39]\n"
"v_mul_f32 v70, v6, v70\n"
"v_mul_f32 v71, v7, v71\n"
"v_mfma_f32_16x16x32_bf16 v[96:99], a[164:167], a[88:91], v[96:99]\n"
"s_waitcnt lgkmcnt(0)\n"
"v_exp_f32 v135, v135\n"
"v_mfma_f32_16x16x32_bf16 v[96:99], a[168:171], a[140:143], v[96:99]\n"
"s_barrier\n"
"buffer_load_dwordx4 v216, s[60:63], s83 offen offset:1024 sc1 lds\n"
"v_add_f32 v132, 1.0, v132\n"
"v_add_f32 v133, 1.0, v133\n"
"v_mfma_f32_16x16x32_bf16 v[100:103], a[172:175], a[88:91], v[100:103]\n"
"ds_read_b128 a[180:183], v217 offset:0\n"
"v_add_f32 v134, 1.0, v134\n"
"v_mfma_f32_16x16x32_bf16 v[100:103], a[176:179], a[140:143], v[100:103]\n"
"ds_read_b128 a[184:187], v217 offset:5120\n"
"v_add_f32 v135, 1.0, v135\n"
"v_rcp_f32 v132, v132\n"
"v_mfma_f32_16x16x32_bf16 v[40:43], a[148:151], a[120:123], v[40:43]\n"
"v_rcp_f32 v133, v133\n"
"v_rcp_f32 v134, v134\n"
"v_mfma_f32_16x16x32_bf16 v[40:43], a[152:155], a[212:215], v[40:43]\n"
"v_rcp_f32 v135, v135\n"
"v_mfma_f32_16x16x32_bf16 v[44:47], a[156:159], a[120:123], v[44:47]\n"
"ds_read_b128 a[188:191], v217 offset:1024\n"
"v_mul_f32 v132, v132, v68\n"
"v_mul_f32 v133, v133, v69\n"
"v_mfma_f32_16x16x32_bf16 v[44:47], a[160:163], a[212:215], v[44:47]\n"
"buffer_load_dwordx4 v216, s[60:63], s83 offen offset:2048 sc1 lds\n"
"v_mul_f32 v134, v134, v70\n"
"v_mul_f32 v135, v135, v71\n"
"v_mfma_f32_16x16x32_bf16 v[104:107], a[164:167], a[120:123], v[104:107]\n"
"ds_read_b128 a[192:195], v217 offset:6144\n"
"v_cvt_pk_bf16_f32 v128, v128, v129\n"
"v_mfma_f32_16x16x32_bf16 v[104:107], a[168:171], a[212:215], v[104:107]\n"
"ds_read_b128 a[196:199], v217 offset:2048\n"
"v_cvt_pk_bf16_f32 v129, v130, v131\n"
"v_cvt_pk_bf16_f32 v130, v132, v133\n"
"v_mfma_f32_16x16x32_bf16 v[108:111], a[172:175], a[120:123], v[108:111]\n"
"ds_read_b128 a[200:203], v217 offset:7168\n"
"v_cvt_pk_bf16_f32 v131, v134, v135\n"
"v_mfma_f32_16x16x32_bf16 v[108:111], a[176:179], a[212:215], v[108:111]\n"
"buffer_store_dwordx4 v[128:131], v216, s[68:71], s20 offen sc1\n"
"v_mfma_f32_16x16x32_bf16 v[48:51], a[148:151], a[28:31], v[48:51]\n"
"v_mul_f32 v136, s0, v8\n"
"v_mul_f32 v137, s0, v9\n"
"v_mfma_f32_16x16x32_bf16 v[48:51], a[152:155], a[216:219], v[48:51]\n"
"v_mul_f32 v138, s0, v10\n"
"v_mul_f32 v139, s0, v11\n"
"v_mfma_f32_16x16x32_bf16 v[52:55], a[156:159], a[28:31], v[52:55]\n"
"v_exp_f32 v136, v136\n"
"v_mfma_f32_16x16x32_bf16 v[52:55], a[160:163], a[216:219], v[52:55]\n"
"v_mul_f32 v72, v8, v72\n"
"v_mul_f32 v73, v9, v73\n"
"v_mfma_f32_16x16x32_bf16 v[112:115], a[164:167], a[28:31], v[112:115]\n"
"v_exp_f32 v137, v137\n"
"v_mfma_f32_16x16x32_bf16 v[112:115], a[168:171], a[216:219], v[112:115]\n"
"buffer_load_dwordx4 v216, s[60:63], s83 offen offset:3072 sc1 lds\n"
"v_mfma_f32_16x16x32_bf16 v[116:119], a[172:175], a[28:31], v[116:119]\n"
"s_addk_i32 m0, 1024\n"
"v_exp_f32 v138, v138\n"
"v_mfma_f32_16x16x32_bf16 v[116:119], a[176:179], a[216:219], v[116:119]\n"
"s_cmp_gt_u32 s11, 4\n"
"s_cselect_b32 s83, 4, 5\n"
"v_mul_f32 v74, v10, v74\n"
"v_mul_f32 v75, v11, v75\n"
"v_mfma_f32_16x16x32_bf16 v[56:59], a[148:151], a[136:139], v[56:59]\n"
"s_mul_i32 s83, s83, 20480\n"
"v_exp_f32 v139, v139\n"
"v_mfma_f32_16x16x32_bf16 v[56:59], a[152:155], a[220:223], v[56:59]\n"
"s_add_u32 s83, s83, s25\n"
"v_add_f32 v136, 1.0, v136\n"
"v_add_f32 v137, 1.0, v137\n"
"v_mfma_f32_16x16x32_bf16 v[60:63], a[156:159], a[136:139], v[60:63]\n"
"v_add_f32 v138, 1.0, v138\n"
"v_add_f32 v139, 1.0, v139\n"
"v_mfma_f32_16x16x32_bf16 v[60:63], a[160:163], a[220:223], v[60:63]\n"
"v_rcp_f32 v136, v136\n"
"v_mfma_f32_16x16x32_bf16 v[120:123], a[164:167], a[136:139], v[120:123]\n"
"v_rcp_f32 v137, v137\n"
"v_mfma_f32_16x16x32_bf16 v[120:123], a[168:171], a[220:223], v[120:123]\n"
"v_rcp_f32 v138, v138\n"
"v_mfma_f32_16x16x32_bf16 v[124:127], a[172:175], a[136:139], v[124:127]\n"
"v_rcp_f32 v139, v139\n"
"v_mfma_f32_16x16x32_bf16 v[124:127], a[176:179], a[220:223], v[124:127]\n"
"s_waitcnt lgkmcnt(0)\n"
"buffer_load_dwordx4 v216, s[60:63], s82 offen offset:3072 sc1 lds\n"
"v_mul_f32 v136, v136, v72\n"
"v_mfma_f32_16x16x32_bf16 v[152:155], a[180:183], a[0:3], 0\n"
"s_addk_i32 m0, 19456\n"
"ds_read_b128 a[148:151], v217 offset:3072\n"
"v_mul_f32 v137, v137, v73\n"
"v_mul_f32 v138, v138, v74\n"
"v_mfma_f32_16x16x32_bf16 v[152:155], a[184:187], a[32:35], v[152:155]\n"
"s_add_u32 s82, s83, 1024\n"
"ds_read_b128 a[152:155], v217 offset:8192\n"
"v_mul_f32 v139, v139, v75\n"
"v_mfma_f32_16x16x32_bf16 a[16:19], a[184:187], a[36:39], 0\n"
"ds_read_b128 a[156:159], v217 offset:4096\n"
"v_mul_f32 v140, s0, v12\n"
"v_mul_f32 v141, s0, v13\n"
"v_mfma_f32_16x16x32_bf16 a[16:19], a[180:183], a[4:7], a[16:19]\n"
"buffer_load_dwordx4 a[144:147], v216, s[60:63], s84 offen sc1\n"
"v_mul_f32 v142, s0, v14\n"
"v_mul_f32 v143, s0, v15\n"
"v_mfma_f32_16x16x32_bf16 a[52:55], a[180:183], a[8:11], 0\n"
"v_exp_f32 v140, v140\n"
"v_mfma_f32_16x16x32_bf16 a[52:55], a[184:187], a[40:43], a[52:55]\n"
"ds_read_b128 a[160:163], v217 offset:9216\n"
"v_mul_f32 v76, v12, v76\n"
"v_exp_f32 v141, v141\n"
"v_mfma_f32_16x16x32_bf16 a[88:91], a[184:187], a[44:47], 0\n"
"v_mul_f32 v77, v13, v77\n"
"v_exp_f32 v142, v142\n"
"v_mfma_f32_16x16x32_bf16 a[88:91], a[180:183], a[12:15], a[88:91]\n"
"buffer_load_dwordx4 a[224:227], v216, s[60:63], s85 offen sc1\n"
"v_mul_f32 v78, v14, v78\n"
"v_mfma_f32_16x16x32_bf16 a[92:95], a[188:191], a[12:15], 0\n"
"v_exp_f32 v143, v143\n"
"v_mul_f32 v79, v15, v79\n"
"v_mfma_f32_16x16x32_bf16 a[92:95], a[192:195], a[44:47], a[92:95]\n"
"v_add_f32 v140, 1.0, v140\n"
"v_add_f32 v141, 1.0, v141\n"
"v_mfma_f32_16x16x32_bf16 a[56:59], a[192:195], a[40:43], 0\n"
"v_add_f32 v142, 1.0, v142\n"
"v_add_f32 v143, 1.0, v143\n"
"v_mfma_f32_16x16x32_bf16 a[56:59], a[188:191], a[8:11], a[56:59]\n"
"L_st12_%=:\n"
"s_cbranch_execz L_hd0_%=\n"
"v_rcp_f32 v140, v140\n"
"v_mfma_f32_16x16x32_bf16 a[20:23], a[188:191], a[4:7], 0\n"
"v_rcp_f32 v141, v141\n"
"v_rcp_f32 v142, v142\n"
"v_mfma_f32_16x16x32_bf16 a[20:23], a[192:195], a[36:39], a[20:23]\n"
"v_rcp_f32 v143, v143\n"
"v_mfma_f32_16x16x32_bf16 v[156:159], a[192:195], a[32:35], 0\n"
"buffer_load_dwordx4 a[228:231], v216, s[60:63], s86 offen sc1\n"
"v_mul_f32 v140, v140, v76\n"
"v_mul_f32 v141, v141, v77\n"
"v_mfma_f32_16x16x32_bf16 v[156:159], a[188:191], a[0:3], v[156:159]\n"
"v_mul_f32 v142, v142, v78\n"
"v_mul_f32 v143, v143, v79\n"
"v_mfma_f32_16x16x32_bf16 v[160:163], a[196:199], a[0:3], 0\n"
"v_cvt_pk_bf16_f32 v136, v136, v137\n"
"v_cvt_pk_bf16_f32 v137, v138, v139\n"
"v_mfma_f32_16x16x32_bf16 v[160:163], a[200:203], a[32:35], v[160:163]\n"
"v_cvt_pk_bf16_f32 v138, v140, v141\n"
"v_cvt_pk_bf16_f32 v139, v142, v143\n"
"v_mfma_f32_16x16x32_bf16 a[24:27], a[200:203], a[36:39], 0\n"
"buffer_store_dwordx4 v[136:139], v216, s[68:71], s21 offen sc1\n"
"v_mfma_f32_16x16x32_bf16 a[24:27], a[196:199], a[4:7], a[24:27]\n"
"v_mul_f32 v144, s0, v16\n"
"v_mul_f32 v145, s0, v17\n"
"v_mfma_f32_16x16x32_bf16 a[60:63], a[196:199], a[8:11], 0\n"
"v_mul_f32 v146, s0, v18\n"
"v_mul_f32 v147, s0, v19\n"
"v_mfma_f32_16x16x32_bf16 a[60:63], a[200:203], a[40:43], a[60:63]\n"
"v_exp_f32 v144, v144\n"
"v_mfma_f32_16x16x32_bf16 a[112:115], a[200:203], a[44:47], 0\n"
"buffer_load_dwordx4 a[232:235], v216, s[60:63], s87 offen sc1\n"
"v_mfma_f32_16x16x32_bf16 a[112:115], a[196:199], a[12:15], a[112:115]\n"
"s_waitcnt lgkmcnt(0)\n"
"ds_read_b128 a[180:183], v217 offset:10240\n"
"v_mul_f32 v80, v16, v80\n"
"v_mfma_f32_16x16x32_bf16 v[164:167], a[148:151], a[0:3], 0\n"
"v_exp_f32 v145, v145\n"
"v_mfma_f32_16x16x32_bf16 v[164:167], a[152:155], a[32:35], v[164:167]\n"
"ds_read_b128 a[184:187], v217 offset:15360\n"
"v_mul_f32 v81, v17, v81\n"
"v_mfma_f32_16x16x32_bf16 a[28:31], a[152:155], a[36:39], 0\n"
"v_exp_f32 v146, v146\n"
"v_mfma_f32_16x16x32_bf16 a[28:31], a[148:151], a[4:7], a[28:31]\n"
"buffer_load_dwordx4 a[124:127], v216, s[60:63], s84 offen offset:1024 sc1\n"
"v_mfma_f32_16x16x32_bf16 a[80:83], a[148:151], a[8:11], 0\n"
"ds_read_b128 a[188:191], v217 offset:11264\n"
"v_mul_f32 v82, v18, v82\n"
"v_mfma_f32_16x16x32_bf16 a[80:83], a[152:155], a[40:43], a[80:83]\n"
"ds_read_b128 a[192:195], v217 offset:16384\n"
"v_exp_f32 v147, v147\n"
"v_mfma_f32_16x16x32_bf16 a[116:119], a[152:155], a[44:47], 0\n"
"ds_read_b128 a[196:199], v217 offset:12288\n"
"v_mul_f32 v83, v19, v83\n"
"v_add_f32 v144, 1.0, v144\n"
"v_mfma_f32_16x16x32_bf16 a[116:119], a[148:151], a[12:15], a[116:119]\n"
"buffer_load_dwordx4 a[236:239], v216, s[60:63], s85 offen offset:1024 sc1\n"
"v_add_f32 v145, 1.0, v145\n"
"v_mfma_f32_16x16x32_bf16 a[120:123], a[156:159], a[12:15], 0\n"
"ds_read_b128 a[200:203], v217 offset:17408\n"
"v_add_f32 v146, 1.0, v146\n"
"v_add_f32 v147, 1.0, v147\n"
"v_mfma_f32_16x16x32_bf16 a[120:123], a[160:163], a[44:47], a[120:123]\n"
"v_rcp_f32 v144, v144\n"
"v_rcp_f32 v145, v145\n"
"v_mfma_f32_16x16x32_bf16 a[84:87], a[160:163], a[40:43], 0\n"
"v_rcp_f32 v146, v146\n"
"v_mfma_f32_16x16x32_bf16 a[84:87], a[156:159], a[8:11], a[84:87]\n"
"buffer_load_dwordx4 a[240:243], v216, s[60:63], s86 offen offset:1024 sc1\n"
"v_rcp_f32 v147, v147\n"
"v_mul_f32 v144, v144, v80\n"
"v_mfma_f32_16x16x32_bf16 a[48:51], a[156:159], a[4:7], 0\n"
"v_mul_f32 v145, v145, v81\n"
"v_mul_f32 v146, v146, v82\n"
"v_mfma_f32_16x16x32_bf16 a[48:51], a[160:163], a[36:39], a[48:51]\n"
"v_mul_f32 v147, v147, v83\n"
"v_mul_f32 v148, s0, v20\n"
"v_mfma_f32_16x16x32_bf16 v[168:171], a[160:163], a[32:35], 0\n"
"v_mul_f32 v149, s0, v21\n"
"v_mul_f32 v150, s0, v22\n"
"v_mfma_f32_16x16x32_bf16 v[168:171], a[156:159], a[0:3], v[168:171]\n"
"s_waitcnt lgkmcnt(4)\n"
"buffer_load_dwordx4 a[244:247], v216, s[60:63], s87 offen offset:1024 sc1\n"
"v_mul_f32 v151, s0, v23\n"
"v_mfma_f32_16x16x32_bf16 v[152:155], a[180:183], a[64:67], v[152:155]\n"
"v_exp_f32 v148, v148\n"
"v_mfma_f32_16x16x32_bf16 v[152:155], a[184:187], a[96:99], v[152:155]\n"
"ds_read_b128 a[148:151], v217 offset:13312\n"
"v_mul_f32 v84, v20, v84\n"
"v_exp_f32 v149, v149\n"
"v_mfma_f32_16x16x32_bf16 a[16:19], a[184:187], a[100:103], a[16:19]\n"
"ds_read_b128 a[152:155], v217 offset:18432\n"
"v_mul_f32 v85, v21, v85\n"
"v_exp_f32 v150, v150\n"
"v_mfma_f32_16x16x32_bf16 a[16:19], a[180:183], a[68:71], a[16:19]\n"
"buffer_load_dwordx4 a[248:251], v216, s[60:63], s84 offen offset:2048 sc1\n"
"v_mul_f32 v86, v22, v86\n"
"v_mfma_f32_16x16x32_bf16 a[52:55], a[180:183], a[72:75], a[52:55]\n"
"v_exp_f32 v151, v151\n"
"v_mul_f32 v87, v23, v87\n"
"v_mfma_f32_16x16x32_bf16 a[52:55], a[184:187], a[104:107], a[52:55]\n"
"ds_read_b128 a[156:159], v217 offset:14336\n"
"v_add_f32 v148, 1.0, v148\n"
"v_add_f32 v149, 1.0, v149\n"
"v_mfma_f32_16x16x32_bf16 a[88:91], a[184:187], a[108:111], a[88:91]\n"
"ds_read_b128 a[160:163], v217 offset:19456\n"
"v_add_f32 v150, 1.0, v150\n"
"v_mfma_f32_16x16x32_bf16 a[88:91], a[180:183], a[76:79], a[88:91]\n"
"s_waitcnt lgkmcnt(4)\n"
"buffer_load_dwordx4 a[252:255], v216, s[60:63], s85 offen offset:2048 sc1\n"
"v_add_f32 v151, 1.0, v151\n"
"v_rcp_f32 v148, v148\n"
"v_mfma_f32_16x16x32_bf16 a[92:95], a[188:191], a[76:79], a[92:95]\n"
"v_rcp_f32 v149, v149\n"
"v_rcp_f32 v150, v150\n"
"v_mfma_f32_16x16x32_bf16 a[92:95], a[192:195], a[108:111], a[92:95]\n"
"v_rcp_f32 v151, v151\n"
"v_mfma_f32_16x16x32_bf16 a[56:59], a[192:195], a[104:107], a[56:59]\n"
"v_mul_f32 v148, v148, v84\n"
"v_mul_f32 v149, v149, v85\n"
"v_mfma_f32_16x16x32_bf16 a[56:59], a[188:191], a[72:75], a[56:59]\n"
"buffer_load_dwordx4 v[176:179], v216, s[60:63], s86 offen offset:2048 sc1\n"
"v_mul_f32 v150, v150, v86\n"
"v_mul_f32 v151, v151, v87\n"
"v_mfma_f32_16x16x32_bf16 a[20:23], a[188:191], a[68:71], a[20:23]\n"
"v_cvt_pk_bf16_f32 v144, v144, v145\n"
"v_cvt_pk_bf16_f32 v145, v146, v147\n"
"v_mfma_f32_16x16x32_bf16 a[20:23], a[192:195], a[100:103], a[20:23]\n"
"v_cvt_pk_bf16_f32 v146, v148, v149\n"
"v_cvt_pk_bf16_f32 v147, v150, v151\n"
"v_mfma_f32_16x16x32_bf16 v[156:159], a[192:195], a[96:99], v[156:159]\n"
"v_mul_f32 v128, s0, v24\n"
"v_mul_f32 v129, s0, v25\n"
"v_mfma_f32_16x16x32_bf16 v[156:159], a[188:191], a[64:67], v[156:159]\n"
"buffer_store_dwordx4 v[144:147], v216, s[68:71], s22 offen sc1\n"
"v_mfma_f32_16x16x32_bf16 v[160:163], a[196:199], a[64:67], v[160:163]\n"
"v_mul_f32 v130, s0, v26\n"
"v_mul_f32 v131, s0, v27\n"
"v_mfma_f32_16x16x32_bf16 v[160:163], a[200:203], a[96:99], v[160:163]\n"
"v_exp_f32 v128, v128\n"
"v_mfma_f32_16x16x32_bf16 a[24:27], a[200:203], a[100:103], a[24:27]\n"
"v_mul_f32 v88, v24, v88\n"
"v_mul_f32 v89, v25, v89\n"
"v_mfma_f32_16x16x32_bf16 a[24:27], a[196:199], a[68:71], a[24:27]\n"
"buffer_load_dwordx4 v[180:183], v216, s[60:63], s87 offen offset:2048 sc1\n"
"v_mfma_f32_16x16x32_bf16 a[60:63], a[196:199], a[72:75], a[60:63]\n"
"v_exp_f32 v129, v129\n"
"v_mfma_f32_16x16x32_bf16 a[60:63], a[200:203], a[104:107], a[60:63]\n"
"v_exp_f32 v130, v130\n"
"v_mfma_f32_16x16x32_bf16 a[112:115], a[200:203], a[108:111], a[112:115]\n"
"v_mul_f32 v90, v26, v90\n"
"v_mul_f32 v91, v27, v91\n"
"v_mfma_f32_16x16x32_bf16 a[112:115], a[196:199], a[76:79], a[112:115]\n"
"s_waitcnt lgkmcnt(0)\n"
"v_exp_f32 v131, v131\n"
"v_mfma_f32_16x16x32_bf16 v[164:167], a[148:151], a[64:67], v[164:167]\n"
"s_barrier\n"
"buffer_load_dwordx4 v[184:187], v216, s[60:63], s84 offen offset:3072 sc1\n"
"v_mfma_f32_16x16x32_bf16 v[164:167], a[152:155], a[96:99], v[164:167]\n"
"s_add_u32 s84, s16, s92\n"
"ds_read_b128 a[180:183], v216 offset:40960\n"
"v_add_f32 v128, 1.0, v128\n"
"v_mfma_f32_16x16x32_bf16 a[28:31], a[152:155], a[100:103], a[28:31]\n"
"ds_read_b128 a[184:187], v216 offset:46080\n"
"v_add_f32 v129, 1.0, v129\n"
"v_mfma_f32_16x16x32_bf16 a[28:31], a[148:151], a[68:71], a[28:31]\n"
"ds_read_b128 a[188:191], v216 offset:41984\n"
"v_add_f32 v130, 1.0, v130\n"
"v_mfma_f32_16x16x32_bf16 a[80:83], a[148:151], a[72:75], a[80:83]\n"
"buffer_load_dwordx4 v[188:191], v216, s[60:63], s85 offen offset:3072 sc1\n"
"v_add_f32 v131, 1.0, v131\n"
"v_mfma_f32_16x16x32_bf16 a[80:83], a[152:155], a[104:107], a[80:83]\n"
"s_add_u32 s85, s17, s92\n"
"ds_read_b128 a[192:195], v216 offset:47104\n"
"v_rcp_f32 v128, v128\n"
"v_rcp_f32 v129, v129\n"
"v_mfma_f32_16x16x32_bf16 a[116:119], a[152:155], a[108:111], a[116:119]\n"
"v_rcp_f32 v130, v130\n"
"v_mfma_f32_16x16x32_bf16 a[116:119], a[148:151], a[76:79], a[116:119]\n"
"v_rcp_f32 v131, v131\n"
"v_mul_f32 v128, v128, v88\n"
"v_mfma_f32_16x16x32_bf16 a[120:123], a[156:159], a[76:79], a[120:123]\n"
"buffer_load_dwordx4 v[192:195], v216, s[60:63], s86 offen offset:3072 sc1\n"
"v_mul_f32 v129, v129, v89\n"
"v_mul_f32 v130, v130, v90\n"
"v_mfma_f32_16x16x32_bf16 a[120:123], a[160:163], a[108:111], a[120:123]\n"
"s_add_u32 s86, s18, s92\n"
"ds_read_b128 a[196:199], v216 offset:43008\n"
"v_mul_f32 v131, v131, v91\n"
"v_mfma_f32_16x16x32_bf16 a[84:87], a[160:163], a[104:107], a[84:87]\n"
"ds_read_b128 a[200:203], v216 offset:48128\n"
"v_mul_f32 v132, s0, v28\n"
"v_mul_f32 v133, s0, v29\n"
"v_mfma_f32_16x16x32_bf16 a[84:87], a[156:159], a[72:75], a[84:87]\n"
"v_mul_f32 v134, s0, v30\n"
"v_mul_f32 v135, s0, v31\n"
"v_mfma_f32_16x16x32_bf16 a[48:51], a[156:159], a[68:71], a[48:51]\n"
"buffer_load_dwordx4 v[196:199], v216, s[60:63], s87 offen offset:3072 sc1\n"
"v_exp_f32 v132, v132\n"
"v_mfma_f32_16x16x32_bf16 a[48:51], a[160:163], a[100:103], a[48:51]\n"
"s_add_u32 s87, s19, s92\n"
"v_mul_f32 v92, v28, v92\n"
"v_exp_f32 v133, v133\n"
"v_mfma_f32_16x16x32_bf16 v[168:171], a[160:163], a[96:99], v[168:171]\n"
"s_cmp_gt_u32 s11, 2\n"
"s_cselect_b32 s92, 2, 3\n"
"v_mul_f32 v93, v29, v93\n"
"v_exp_f32 v134, v134\n"
"v_mfma_f32_16x16x32_bf16 v[168:171], a[156:159], a[64:67], v[168:171]\n"
"s_waitcnt vmcnt(17) lgkmcnt(3)\n"
"ds_read_b128 a[148:151], v216 offset:44032\n"
"v_mul_f32 v94, v30, v94\n"
"v_mfma_f32_16x16x32_bf16 v[152:155], a[180:183], a[144:147], v[152:155]\n"
"s_waitcnt vmcnt(12)\n"
"v_exp_f32 v135, v135\n"
"v_mul_f32 v95, v31, v95\n"
"v_mfma_f32_16x16x32_bf16 v[152:155], a[184:187], a[124:127], v[152:155]\n"
"s_waitcnt vmcnt(11) lgkmcnt(3)\n"
"buffer_load_dwordx4 v216, s[60:63], s83 offen sc1 lds\n"
"v_add_f32 v132, 1.0, v132\n"
"v_add_f32 v133, 1.0, v133\n"
"v_mfma_f32_16x16x32_bf16 a[16:19], a[184:187], a[236:239], a[16:19]\n"
"s_lshl_b32 s92, s92, 12\n"
"ds_read_b128 a[152:155], v216 offset:49152\n"
"v_add_f32 v134, 1.0, v134\n"
"v_mfma_f32_16x16x32_bf16 a[16:19], a[180:183], a[224:227], a[16:19]\n"
"ds_read_b128 a[156:159], v216 offset:45056\n"
"v_add_f32 v135, 1.0, v135\n"
"v_rcp_f32 v132, v132\n"
"v_mfma_f32_16x16x32_bf16 a[52:55], a[180:183], a[228:231], a[52:55]\n"
"s_waitcnt vmcnt(11)\n"
"v_rcp_f32 v133, v133\n"
"v_rcp_f32 v134, v134\n"
"v_mfma_f32_16x16x32_bf16 a[52:55], a[184:187], a[240:243], a[52:55]\n"
"s_waitcnt vmcnt(10) lgkmcnt(4)\n"
"v_rcp_f32 v135, v135\n"
"v_mfma_f32_16x16x32_bf16 a[88:91], a[184:187], a[244:247], a[88:91]\n"
"buffer_load_dwordx4 v216, s[60:63], s83 offen offset:1024 sc1 lds\n"
"v_mul_f32 v132, v132, v92\n"
"v_mul_f32 v133, v133, v93\n"
"v_mfma_f32_16x16x32_bf16 a[88:91], a[180:183], a[232:235], a[88:91]\n"
"ds_read_b128 a[160:163], v216 offset:50176\n"
"v_mul_f32 v134, v134, v94\n"
"v_mul_f32 v135, v135, v95\n"
"v_mfma_f32_16x16x32_bf16 a[92:95], a[188:191], a[232:235], a[92:95]\n"
"v_cvt_pk_bf16_f32 v128, v128, v129\n"
"v_cvt_pk_bf16_f32 v129, v130, v131\n"
"v_mfma_f32_16x16x32_bf16 a[92:95], a[192:195], a[244:247], a[92:95]\n"
"v_cvt_pk_bf16_f32 v130, v132, v133\n"
"v_cvt_pk_bf16_f32 v131, v134, v135\n"
"v_mfma_f32_16x16x32_bf16 a[56:59], a[192:195], a[240:243], a[56:59]\n"
"buffer_store_dwordx4 v[128:131], v216, s[68:71], s23 offen sc1\n"
"v_mfma_f32_16x16x32_bf16 a[56:59], a[188:191], a[228:231], a[56:59]\n"
"v_mul_f32 v136, s0, v32\n"
"v_mul_f32 v137, s0, v33\n"
"v_mfma_f32_16x16x32_bf16 a[20:23], a[188:191], a[224:227], a[20:23]\n"
"v_mul_f32 v138, s0, v34\n"
"v_mul_f32 v139, s0, v35\n"
"v_mfma_f32_16x16x32_bf16 a[20:23], a[192:195], a[236:239], a[20:23]\n"
"v_exp_f32 v136, v136\n"
"v_mfma_f32_16x16x32_bf16 v[156:159], a[192:195], a[124:127], v[156:159]\n"
"buffer_load_dwordx4 v216, s[60:63], s83 offen offset:2048 sc1 lds\n"
"v_mfma_f32_16x16x32_bf16 v[156:159], a[188:191], a[144:147], v[156:159]\n"
"v_mul_f32 v96, v32, v96\n"
"v_mul_f32 v97, v33, v97\n"
"v_mfma_f32_16x16x32_bf16 v[160:163], a[196:199], a[144:147], v[160:163]\n"
"s_waitcnt lgkmcnt(1)\n"
"v_exp_f32 v137, v137\n"
"v_mfma_f32_16x16x32_bf16 v[160:163], a[200:203], a[124:127], v[160:163]\n"
"v_exp_f32 v138, v138\n"
"v_mfma_f32_16x16x32_bf16 a[24:27], a[200:203], a[236:239], a[24:27]\n"
"buffer_load_dwordx4 v216, s[60:63], s83 offen offset:3072 sc1 lds\n"
"v_mfma_f32_16x16x32_bf16 a[24:27], a[196:199], a[224:227], a[24:27]\n"
"s_addk_i32 m0, 1024\n"
"v_mul_f32 v98, v34, v98\n"
"v_mul_f32 v99, v35, v99\n"
"v_mfma_f32_16x16x32_bf16 a[60:63], a[196:199], a[228:231], a[60:63]\n"
"s_cmp_gt_u32 s11, 5\n"
"s_cselect_b32 s83, 5, 6\n"
"v_exp_f32 v139, v139\n"
"v_mfma_f32_16x16x32_bf16 a[60:63], a[200:203], a[240:243], a[60:63]\n"
"s_mul_i32 s83, s83, 20480\n"
"v_add_f32 v136, 1.0, v136\n"
"v_add_f32 v137, 1.0, v137\n"
"v_mfma_f32_16x16x32_bf16 a[112:115], a[200:203], a[244:247], a[112:115]\n"
"s_add_u32 s83, s83, s25\n"
"buffer_load_dwordx4 v216, s[60:63], s82 offen offset:3072 sc1 lds\n"
"v_mfma_f32_16x16x32_bf16 a[112:115], a[196:199], a[232:235], a[112:115]\n"
"s_addk_i32 m0, 19456\n"
"ds_read_b128 a[180:183], v216 offset:51200\n"
"v_add_f32 v138, 1.0, v138\n"
"v_mfma_f32_16x16x32_bf16 v[164:167], a[148:151], a[144:147], v[164:167]\n"
"s_add_u32 s82, s83, 1024\n"
"ds_read_b128 a[184:187], v216 offset:56320\n"
"v_add_f32 v139, 1.0, v139\n"
"v_mfma_f32_16x16x32_bf16 v[164:167], a[152:155], a[124:127], v[164:167]\n"
"v_rcp_f32 v136, v136\n"
"v_rcp_f32 v137, v137\n"
"v_mfma_f32_16x16x32_bf16 a[28:31], a[152:155], a[236:239], a[28:31]\n"
"v_rcp_f32 v138, v138\n"
"v_mfma_f32_16x16x32_bf16 a[28:31], a[148:151], a[224:227], a[28:31]\n"
"v_rcp_f32 v139, v139\n"
"v_mul_f32 v136, v136, v96\n"
"v_mfma_f32_16x16x32_bf16 a[80:83], a[148:151], a[228:231], a[80:83]\n"
"buffer_load_dwordx4 a[128:131], v216, s[60:63], s84 offen sc1\n"
"v_mul_f32 v137, v137, v97\n"
"v_mul_f32 v138, v138, v98\n"
"v_mfma_f32_16x16x32_bf16 a[80:83], a[152:155], a[240:243], a[80:83]\n"
"ds_read_b128 a[188:191], v216 offset:52224\n"
"v_mul_f32 v139, v139, v99\n"
"v_mfma_f32_16x16x32_bf16 a[116:119], a[152:155], a[244:247], a[116:119]\n"
"ds_read_b128 a[192:195], v216 offset:57344\n"
"v_mul_f32 v140, s0, v36\n"
"v_mul_f32 v141, s0, v37\n"
"v_mfma_f32_16x16x32_bf16 a[116:119], a[148:151], a[232:235], a[116:119]\n"
"ds_read_b128 a[196:199], v216 offset:53248\n"
"v_mul_f32 v142, s0, v38\n"
"v_mul_f32 v143, s0, v39\n"
"v_mfma_f32_16x16x32_bf16 a[120:123], a[156:159], a[232:235], a[120:123]\n"
"s_waitcnt lgkmcnt(5)\n"
"buffer_load_dwordx4 a[132:135], v216, s[60:63], s85 offen sc1\n"
"v_exp_f32 v140, v140\n"
"v_mfma_f32_16x16x32_bf16 a[120:123], a[160:163], a[244:247], a[120:123]\n"
"ds_read_b128 a[200:203], v216 offset:58368\n"
"v_mul_f32 v100, v36, v100\n"
"v_exp_f32 v141, v141\n"
"v_mfma_f32_16x16x32_bf16 a[84:87], a[160:163], a[240:243], a[84:87]\n"
"v_mul_f32 v101, v37, v101\n"
"v_exp_f32 v142, v142\n"
"v_mfma_f32_16x16x32_bf16 a[84:87], a[156:159], a[228:231], a[84:87]\n"
"v_mul_f32 v102, v38, v102\n"
"v_mul_f32 v103, v39, v103\n"
"v_mfma_f32_16x16x32_bf16 a[48:51], a[156:159], a[224:227], a[48:51]\n"
"buffer_load_dwordx4 v[200:203], v216, s[60:63], s86 offen sc1\n"
"v_exp_f32 v143, v143\n"
"v_mfma_f32_16x16x32_bf16 a[48:51], a[160:163], a[236:239], a[48:51]\n"
"v_add_f32 v140, 1.0, v140\n"
"v_add_f32 v141, 1.0, v141\n"
"v_mfma_f32_16x16x32_bf16 v[168:171], a[160:163], a[124:127], v[168:171]\n"
"v_add_f32 v142, 1.0, v142\n"
"v_add_f32 v143, 1.0, v143\n"
"v_mfma_f32_16x16x32_bf16 v[168:171], a[156:159], a[144:147], v[168:171]\n"
"s_waitcnt vmcnt(17) lgkmcnt(4)\n"
"v_rcp_f32 v140, v140\n"
"v_mfma_f32_16x16x32_bf16 v[152:155], a[180:183], a[248:251], v[152:155]\n"
"s_waitcnt vmcnt(12)\n"
"buffer_load_dwordx4 v[204:207], v216, s[60:63], s87 offen sc1\n"
"buffer_load_dwordx4 a[136:139], v216, s[60:63], s84 offen offset:1024 sc1\n"
"v_rcp_f32 v141, v141\n"
"v_rcp_f32 v142, v142\n"
"v_mfma_f32_16x16x32_bf16 v[152:155], a[184:187], v[184:187], v[152:155]\n"
"s_waitcnt vmcnt(13) lgkmcnt(3)\n"
"buffer_load_dwordx4 a[140:143], v216, s[60:63], s85 offen offset:1024 sc1\n"
"v_rcp_f32 v143, v143\n"
"v_mfma_f32_16x16x32_bf16 a[16:19], a[184:187], v[188:191], a[16:19]\n"
"s_getpc_b64 s[4:5]\n"
"L_pc5_%=:\n"
"s_mov_b32 s6, L_end_%=-L_pc5_%=\n"
"s_and_b32 s5, s5, 0xffff\n"
"s_mov_b32 s7, 0x20000\n"
"ds_read_b128 a[148:151], v216 offset:54272\n"
"v_mul_f32 v140, v140, v100\n"
"v_mul_f32 v141, v141, v101\n"
"v_mfma_f32_16x16x32_bf16 a[16:19], a[180:183], a[252:255], a[16:19]\n"
"ds_read_b128 a[152:155], v216 offset:59392\n"
"v_mul_f32 v142, v142, v102\n"
"v_mul_f32 v143, v143, v103\n"
"v_mfma_f32_16x16x32_bf16 a[52:55], a[180:183], v[176:179], a[52:55]\n"
"s_waitcnt vmcnt(13) lgkmcnt(3)\n"
"buffer_load_dwordx4 a[212:215], v216, s[60:63], s86 offen offset:1024 sc1\n"
"v_cvt_pk_bf16_f32 v136, v136, v137\n"
"v_mfma_f32_16x16x32_bf16 a[52:55], a[184:187], v[192:195], a[52:55]\n"
"s_waitcnt vmcnt(13) lgkmcnt(2)\n"
"buffer_load_dwordx4 a[216:219], v216, s[60:63], s87 offen offset:1024 sc1\n"
"v_cvt_pk_bf16_f32 v137, v138, v139\n"
"v_cvt_pk_bf16_f32 v138, v140, v141\n"
"v_mfma_f32_16x16x32_bf16 a[88:91], a[184:187], v[196:199], a[88:91]\n"
"ds_read_b128 a[156:159], v216 offset:55296\n"
"v_cvt_pk_bf16_f32 v139, v142, v143\n"
"v_mfma_f32_16x16x32_bf16 a[88:91], a[180:183], v[180:183], a[88:91]\n"
"buffer_store_dwordx4 v[136:139], v216, s[68:71], s20 offen offset:1024 sc1\n"
"buffer_load_dword v247, v252, s[4:7], 0 offen\n"
"v_mfma_f32_16x16x32_bf16 a[92:95], a[188:191], v[180:183], a[92:95]\n"
"ds_read_b128 a[160:163], v216 offset:60416\n"
"v_mul_f32 v144, s0, v40\n"
"v_mfma_f32_16x16x32_bf16 a[92:95], a[192:195], v[196:199], a[92:95]\n"
"v_mul_f32 v145, s0, v41\n"
"v_mul_f32 v146, s0, v42\n"
"v_mfma_f32_16x16x32_bf16 a[56:59], a[192:195], v[192:195], a[56:59]\n"
"v_mul_f32 v147, s0, v43\n"
"v_mul_f32 v104, v40, v104\n"
"v_mfma_f32_16x16x32_bf16 a[56:59], a[188:191], v[176:179], a[56:59]\n"
"buffer_load_dwordx4 a[220:223], v216, s[60:63], s84 offen offset:2048 sc1\n"
"v_mfma_f32_16x16x32_bf16 a[20:23], a[188:191], a[252:255], a[20:23]\n"
"v_exp_f32 v144, v144\n"
"v_mfma_f32_16x16x32_bf16 a[20:23], a[192:195], v[188:191], a[20:23]\n"
"v_exp_f32 v145, v145\n"
"v_mfma_f32_16x16x32_bf16 v[156:159], a[192:195], v[184:187], v[156:159]\n"
"v_mul_f32 v105, v41, v105\n"
"v_mul_f32 v106, v42, v106\n"
"v_mfma_f32_16x16x32_bf16 v[156:159], a[188:191], a[248:251], v[156:159]\n"
"v_exp_f32 v146, v146\n"
"v_mfma_f32_16x16x32_bf16 v[160:163], a[196:199], a[248:251], v[160:163]\n"
"v_exp_f32 v147, v147\n"
"v_mfma_f32_16x16x32_bf16 v[160:163], a[200:203], v[184:187], v[160:163]\n"
"buffer_load_dwordx4 a[204:207], v216, s[60:63], s85 offen offset:2048 sc1\n"
"v_mfma_f32_16x16x32_bf16 a[24:27], a[200:203], v[188:191], a[24:27]\n"
"v_mul_f32 v107, v43, v107\n"
"v_add_f32 v144, 1.0, v144\n"
"v_mfma_f32_16x16x32_bf16 a[24:27], a[196:199], a[252:255], a[24:27]\n"
"v_add_f32 v145, 1.0, v145\n"
"v_add_f32 v146, 1.0, v146\n"
"v_mfma_f32_16x16x32_bf16 a[60:63], a[196:199], v[176:179], a[60:63]\n"
"v_add_f32 v147, 1.0, v147\n"
"v_mul_f32 v148, s0, v44\n"
"v_mfma_f32_16x16x32_bf16 a[60:63], a[200:203], v[192:195], a[60:63]\n"
"v_rcp_f32 v144, v144\n"
"v_rcp_f32 v145, v145\n"
"v_mfma_f32_16x16x32_bf16 a[112:115], a[200:203], v[196:199], a[112:115]\n"
"v_rcp_f32 v146, v146\n"
"v_mfma_f32_16x16x32_bf16 a[112:115], a[196:199], v[180:183], a[112:115]\n"
"s_waitcnt lgkmcnt(0)\n"
"v_rcp_f32 v147, v147\n"
"v_mul_f32 v144, v144, v104\n"
"v_mfma_f32_16x16x32_bf16 v[164:167], a[148:151], a[248:251], v[164:167]\n"
"s_barrier\n"
"buffer_load_dwordx4 a[208:211], v216, s[60:63], s86 offen offset:2048 sc1\n"
"v_mul_f32 v145, v145, v105\n"
"v_mul_f32 v146, v146, v106\n"
"v_mfma_f32_16x16x32_bf16 v[164:167], a[152:155], v[184:187], v[164:167]\n"
"s_mul_i32 s93, s11, 5\n"
"ds_read_b128 a[180:183], v216 offset:0\n"
"v_mul_f32 v147, v147, v107\n"
"v_mfma_f32_16x16x32_bf16 a[28:31], a[152:155], v[188:191], a[28:31]\n"
"ds_read_b128 a[184:187], v216 offset:5120\n"
"v_mul_f32 v149, s0, v45\n"
"v_mfma_f32_16x16x32_bf16 a[28:31], a[148:151], a[252:255], a[28:31]\n"
"ds_read_b128 a[188:191], v216 offset:1024\n"
"v_mul_f32 v150, s0, v46\n"
"v_mul_f32 v151, s0, v47\n"
"v_mfma_f32_16x16x32_bf16 a[80:83], a[148:151], v[176:179], a[80:83]\n"
"v_exp_f32 v148, v148\n"
"v_mfma_f32_16x16x32_bf16 a[80:83], a[152:155], v[192:195], a[80:83]\n"
"buffer_load_dwordx4 a[168:171], v216, s[60:63], s87 offen offset:2048 sc1\n"
"v_mul_f32 v108, v44, v108\n"
"v_exp_f32 v149, v149\n"
"v_mfma_f32_16x16x32_bf16 a[116:119], a[152:155], v[196:199], a[116:119]\n"
"ds_read_b128 a[192:195], v216 offset:6144\n"
"v_mul_f32 v109, v45, v109\n"
"v_exp_f32 v150, v150\n"
"v_mfma_f32_16x16x32_bf16 a[116:119], a[148:151], v[180:183], a[116:119]\n"
"ds_read_b128 a[196:199], v216 offset:2048\n"
"v_mul_f32 v110, v46, v110\n"
"v_mfma_f32_16x16x32_bf16 a[120:123], a[156:159], v[180:183], a[120:123]\n"
"v_exp_f32 v151, v151\n"
"v_mul_f32 v111, v47, v111\n"
"v_mfma_f32_16x16x32_bf16 a[120:123], a[160:163], v[196:199], a[120:123]\n"
"buffer_load_dwordx4 v216, s[60:63], s83 offen sc1 lds\n"
"v_add_f32 v148, 1.0, v148\n"
"v_add_f32 v149, 1.0, v149\n"
"v_mfma_f32_16x16x32_bf16 a[84:87], a[160:163], v[192:195], a[84:87]\n"
"ds_read_b128 a[200:203], v216 offset:7168\n"
"v_add_f32 v150, 1.0, v150\n"
"v_mfma_f32_16x16x32_bf16 a[84:87], a[156:159], v[176:179], a[84:87]\n"
"v_add_f32 v151, 1.0, v151\n"
"v_rcp_f32 v148, v148\n"
"v_mfma_f32_16x16x32_bf16 a[48:51], a[156:159], a[252:255], a[48:51]\n"
"v_rcp_f32 v149, v149\n"
"v_rcp_f32 v150, v150\n"
"v_mfma_f32_16x16x32_bf16 a[48:51], a[160:163], v[188:191], a[48:51]\n"
"buffer_load_dwordx4 v216, s[60:63], s83 offen offset:1024 sc1 lds\n"
"v_rcp_f32 v151, v151\n"
"v_mfma_f32_16x16x32_bf16 v[168:171], a[160:163], v[184:187], v[168:171]\n"
"v_mul_f32 v148, v148, v108\n"
"v_mul_f32 v149, v149, v109\n"
"v_mfma_f32_16x16x32_bf16 v[168:171], a[156:159], a[248:251], v[168:171]\n"
"s_waitcnt vmcnt(15) lgkmcnt(3)\n"
"buffer_load_dwordx4 v216, s[60:63], s83 offen offset:2048 sc1 lds\n"
"buffer_load_dwordx4 v216, s[60:63], s83 offen offset:3072 sc1 lds\n"
"s_addk_i32 m0, 1024\n"
"v_mul_f32 v150, v150, v110\n"
"v_mul_f32 v151, v151, v111\n"
"v_mfma_f32_16x16x32_bf16 v[152:155], a[180:183], a[128:131], v[152:155]\n"
"s_waitcnt vmcnt(13)\n"
"buffer_load_dwordx4 v216, s[60:63], s82 offen offset:3072 sc1 lds\n"
"buffer_load_dwordx4 a[172:175], v216, s[60:63], s84 offen offset:3072 sc1\n"
"v_cvt_pk_bf16_f32 v144, v144, v145\n"
"v_mfma_f32_16x16x32_bf16 v[152:155], a[184:187], a[136:139], v[152:155]\n"
"s_waitcnt vmcnt(14)\n"
"buffer_load_dwordx4 a[176:179], v216, s[60:63], s85 offen offset:3072 sc1\n"
"v_cvt_pk_bf16_f32 v145, v146, v147\n"
"v_cvt_pk_bf16_f32 v146, v148, v149\n"
"v_mfma_f32_16x16x32_bf16 a[16:19], a[184:187], a[140:143], a[16:19]\n"
"s_add_u32 m0, s78, 81920\n"
"ds_read_b128 a[148:151], v216 offset:3072\n"
"v_cvt_pk_bf16_f32 v147, v150, v151\n"
"v_mfma_f32_16x16x32_bf16 a[16:19], a[180:183], a[132:135], a[16:19]\n"
"s_add_u32 s84, s16, s92\n"
"buffer_store_dwordx4 v[144:147], v216, s[68:71], s21 offen offset:1024 sc1\n"
"v_mfma_f32_16x16x32_bf16 a[52:55], a[180:183], v[200:203], a[52:55]\n"
"s_waitcnt vmcnt(15) lgkmcnt(2)\n"
"buffer_load_dwordx4 v[208:211], v216, s[60:63], s86 offen offset:3072 sc1\n"
"v_mfma_f32_16x16x32_bf16 a[52:55], a[184:187], a[212:215], a[52:55]\n"
"s_waitcnt vmcnt(15)\n"
"buffer_load_dwordx4 v[0:3], v216, s[60:63], s87 offen offset:3072 sc1\n"
"v_mfma_f32_16x16x32_bf16 a[88:91], a[184:187], a[216:219], a[88:91]\n"
"s_add_u32 s85, s17, s92\n"
"ds_read_b128 a[152:155], v216 offset:8192\n"
"v_mul_f32 v128, s0, v48\n"
"v_mfma_f32_16x16x32_bf16 a[88:91], a[180:183], v[204:207], a[88:91]\n"
"s_add_u32 s86, s18, s92\n"
"ds_read_b128 a[156:159], v216 offset:4096\n"
"v_mul_f32 v129, s0, v49\n"
"v_mfma_f32_16x16x32_bf16 a[92:95], a[188:191], v[204:207], a[92:95]\n"
"s_add_u32 s87, s19, s92\n"
"ds_read_b128 a[160:163], v216 offset:9216\n"
"v_mul_f32 v130, s0, v50\n"
"v_mfma_f32_16x16x32_bf16 a[92:95], a[192:195], a[216:219], a[92:95]\n"
"s_cmp_gt_u32 s11, 6\n"
"s_cselect_b32 s83, 6, 7\n"
"buffer_store_dwordx2 v[232:233], v231, s[60:63], s97 offen sc1\n"
"v_mul_f32 v131, s0, v51\n"
"v_mfma_f32_16x16x32_bf16 a[56:59], a[192:195], a[212:215], a[56:59]\n"
"s_mul_i32 s83, s83, 20480\n"
"v_exp_f32 v128, v128\n"
"v_mul_f32 v112, v48, v112\n"
"v_mfma_f32_16x16x32_bf16 a[56:59], a[188:191], v[200:203], a[56:59]\n"
"s_add_u32 s83, s83, s25\n"
"v_exp_f32 v129, v129\n"
"v_mul_f32 v113, v49, v113\n"
"v_mfma_f32_16x16x32_bf16 a[20:23], a[188:191], a[132:135], a[20:23]\n"
"s_add_u32 s82, s83, 1024\n"
"v_exp_f32 v130, v130\n"
"v_mfma_f32_16x16x32_bf16 a[20:23], a[192:195], a[140:143], a[20:23]\n"
"s_cmp_gt_u32 s11, 3\n"
"s_cselect_b32 s92, 3, 4\n"
"buffer_load_dwordx4 v216, s[60:63], s83 offen sc1 lds\n"
"v_mul_f32 v114, v50, v114\n"
"v_exp_f32 v131, v131\n"
"v_mfma_f32_16x16x32_bf16 v[156:159], a[192:195], a[136:139], v[156:159]\n"
"s_lshl_b32 s92, s92, 12\n"
"v_mul_f32 v115, v51, v115\n"
"v_add_f32 v128, 1.0, v128\n"
"v_mfma_f32_16x16x32_bf16 v[156:159], a[188:191], a[128:131], v[156:159]\n"
"v_add_f32 v129, 1.0, v129\n"
"v_add_f32 v130, 1.0, v130\n"
"v_mfma_f32_16x16x32_bf16 v[160:163], a[196:199], a[128:131], v[160:163]\n"
"s_waitcnt lgkmcnt(3)\n"
"v_add_f32 v131, 1.0, v131\n"
"v_mul_f32 v132, s0, v52\n"
"v_mfma_f32_16x16x32_bf16 v[160:163], a[200:203], a[136:139], v[160:163]\n"
"v_rcp_f32 v128, v128\n"
"v_rcp_f32 v129, v129\n"
"v_mfma_f32_16x16x32_bf16 a[24:27], a[200:203], a[140:143], a[24:27]\n"
"v_rcp_f32 v130, v130\n"
"v_mfma_f32_16x16x32_bf16 a[24:27], a[196:199], a[132:135], a[24:27]\n"
"v_rcp_f32 v131, v131\n"
"v_mul_f32 v128, v128, v112\n"
"v_mfma_f32_16x16x32_bf16 a[60:63], a[196:199], v[200:203], a[60:63]\n"
"buffer_load_dwordx4 v216, s[60:63], s83 offen offset:1024 sc1 lds\n"
"v_mul_f32 v129, v129, v113\n"
"v_mul_f32 v130, v130, v114\n"
"v_mfma_f32_16x16x32_bf16 a[60:63], a[200:203], a[212:215], a[60:63]\n"
"v_mul_f32 v131, v131, v115\n"
"v_mul_f32 v133, s0, v53\n"
"v_mfma_f32_16x16x32_bf16 a[112:115], a[200:203], a[216:219], a[112:115]\n"
"v_mul_f32 v134, s0, v54\n"
"v_mul_f32 v135, s0, v55\n"
"v_mfma_f32_16x16x32_bf16 a[112:115], a[196:199], v[204:207], a[112:115]\n"
"v_exp_f32 v132, v132\n"
"v_mfma_f32_16x16x32_bf16 v[164:167], a[148:151], a[128:131], v[164:167]\n"
"s_waitcnt lgkmcnt(0)\n"
"buffer_load_dwordx4 v216, s[60:63], s83 offen offset:2048 sc1 lds\n"
"v_mfma_f32_16x16x32_bf16 v[164:167], a[152:155], a[136:139], v[164:167]\n"
"ds_read_b128 a[180:183], v216 offset:10240\n"
"v_mul_f32 v116, v52, v116\n"
"v_exp_f32 v133, v133\n"
"v_mfma_f32_16x16x32_bf16 a[28:31], a[152:155], a[140:143], a[28:31]\n"
"ds_read_b128 a[184:187], v216 offset:15360\n"
"v_mul_f32 v117, v53, v117\n"
"v_exp_f32 v134, v134\n"
"v_mfma_f32_16x16x32_bf16 a[28:31], a[148:151], a[132:135], a[28:31]\n"
"ds_read_b128 a[188:191], v216 offset:11264\n"
"v_mul_f32 v118, v54, v118\n"
"v_mfma_f32_16x16x32_bf16 a[80:83], a[148:151], v[200:203], a[80:83]\n"
"buffer_load_dwordx4 v216, s[60:63], s83 offen offset:3072 sc1 lds\n"
"v_exp_f32 v135, v135\n"
"v_mul_f32 v119, v55, v119\n"
"v_mfma_f32_16x16x32_bf16 a[80:83], a[152:155], a[212:215], a[80:83]\n"
"s_addk_i32 m0, 1024\n"
"ds_read_b128 a[192:195], v216 offset:16384\n"
"v_add_f32 v132, 1.0, v132\n"
"v_add_f32 v133, 1.0, v133\n"
"v_mfma_f32_16x16x32_bf16 a[116:119], a[152:155], a[216:219], a[116:119]\n"
"ds_read_b128 a[196:199], v216 offset:12288\n"
"v_add_f32 v134, 1.0, v134\n"
"v_mfma_f32_16x16x32_bf16 a[116:119], a[148:151], v[204:207], a[116:119]\n"
"ds_read_b128 a[200:203], v216 offset:17408\n"
"v_add_f32 v135, 1.0, v135\n"
"v_rcp_f32 v132, v132\n"
"v_mfma_f32_16x16x32_bf16 a[120:123], a[156:159], v[204:207], a[120:123]\n"
"buffer_load_dwordx4 v216, s[60:63], s82 offen offset:3072 sc1 lds\n"
"v_rcp_f32 v133, v133\n"
"v_rcp_f32 v134, v134\n"
"v_mfma_f32_16x16x32_bf16 a[120:123], a[160:163], a[216:219], a[120:123]\n"
"s_addk_i32 m0, 44032\n"
"v_rcp_f32 v135, v135\n"
"v_mfma_f32_16x16x32_bf16 a[84:87], a[160:163], a[212:215], a[84:87]\n"
"v_mul_f32 v132, v132, v116\n"
"v_mul_f32 v133, v133, v117\n"
"v_mfma_f32_16x16x32_bf16 a[84:87], a[156:159], v[200:203], a[84:87]\n"
"v_mul_f32 v134, v134, v118\n"
"v_mul_f32 v135, v135, v119\n"
"v_mfma_f32_16x16x32_bf16 a[48:51], a[156:159], a[132:135], a[48:51]\n"
"buffer_load_dwordx4 a[0:3], v216, s[60:63], s84 offen sc1\n"
"v_cvt_pk_bf16_f32 v128, v128, v129\n"
"v_mfma_f32_16x16x32_bf16 a[48:51], a[160:163], a[140:143], a[48:51]\n"
"v_cvt_pk_bf16_f32 v129, v130, v131\n"
"v_cvt_pk_bf16_f32 v130, v132, v133\n"
"v_mfma_f32_16x16x32_bf16 v[168:171], a[160:163], a[136:139], v[168:171]\n"
"v_cvt_pk_bf16_f32 v131, v134, v135\n"
"v_mul_f32 v136, s0, v56\n"
"v_mfma_f32_16x16x32_bf16 v[168:171], a[156:159], a[128:131], v[168:171]\n"
"s_waitcnt vmcnt(20) lgkmcnt(3)\n"
"buffer_store_dwordx4 v[128:131], v216, s[68:71], s22 offen offset:1024 sc1\n"
"v_mfma_f32_16x16x32_bf16 v[152:155], a[180:183], a[220:223], v[152:155]\n"
"s_waitcnt vmcnt(12)\n"
"buffer_load_dwordx4 a[4:7], v216, s[60:63], s85 offen sc1\n"
"buffer_load_dwordx4 a[8:11], v216, s[60:63], s86 offen sc1\n"
"buffer_load_dwordx4 a[12:15], v216, s[60:63], s87 offen sc1\n"
"buffer_load_dwordx4 a[32:35], v216, s[60:63], s84 offen offset:1024 sc1\n"
"v_mfma_f32_16x16x32_bf16 v[152:155], a[184:187], a[172:175], v[152:155]\n"
"s_waitcnt vmcnt(15) lgkmcnt(2)\n"
"buffer_load_dwordx4 a[36:39], v216, s[60:63], s85 offen offset:1024 sc1\n"
"v_mfma_f32_16x16x32_bf16 a[16:19], a[184:187], a[176:179], a[16:19]\n"
"ds_read_b128 a[148:151], v216 offset:13312\n"
"v_mul_f32 v137, s0, v57\n"
"v_mfma_f32_16x16x32_bf16 a[16:19], a[180:183], a[204:207], a[16:19]\n"
"ds_read_b128 a[152:155], v216 offset:18432\n"
"v_mul_f32 v138, s0, v58\n"
"v_mfma_f32_16x16x32_bf16 a[52:55], a[180:183], a[208:211], a[52:55]\n"
"s_waitcnt vmcnt(14) lgkmcnt(2)\n"
"buffer_load_dwordx4 a[40:43], v216, s[60:63], s86 offen offset:1024 sc1\n"
"v_mfma_f32_16x16x32_bf16 a[52:55], a[184:187], v[208:211], a[52:55]\n"
"s_waitcnt vmcnt(14)\n"
"buffer_load_dwordx4 a[44:47], v216, s[60:63], s87 offen offset:1024 sc1\n"
"v_mul_f32 v139, s0, v59\n"
"v_mfma_f32_16x16x32_bf16 a[88:91], a[184:187], v[0:3], a[88:91]\n"
"v_exp_f32 v136, v136\n"
"v_mul_f32 v120, v56, v120\n"
"v_mfma_f32_16x16x32_bf16 a[88:91], a[180:183], a[168:171], a[88:91]\n"
"v_exp_f32 v137, v137\n"
"v_mul_f32 v121, v57, v121\n"
"v_mfma_f32_16x16x32_bf16 a[92:95], a[188:191], a[168:171], a[92:95]\n"
"v_exp_f32 v138, v138\n"
"v_mfma_f32_16x16x32_bf16 a[92:95], a[192:195], v[0:3], a[92:95]\n"
"buffer_load_dwordx4 a[64:67], v216, s[60:63], s84 offen offset:2048 sc1\n"
"v_mul_f32 v122, v58, v122\n"
"v_exp_f32 v139, v139\n"
"v_mfma_f32_16x16x32_bf16 a[56:59], a[192:195], v[208:211], a[56:59]\n"
"ds_read_b128 a[156:159], v216 offset:14336\n"
"v_mul_f32 v123, v59, v123\n"
"v_add_f32 v136, 1.0, v136\n"
"v_mfma_f32_16x16x32_bf16 a[56:59], a[188:191], a[208:211], a[56:59]\n"
"ds_read_b128 a[160:163], v216 offset:19456\n"
"v_add_f32 v137, 1.0, v137\n"
"v_mfma_f32_16x16x32_bf16 a[20:23], a[188:191], a[204:207], a[20:23]\n"
"v_add_f32 v138, 1.0, v138\n"
"v_add_f32 v139, 1.0, v139\n"
"v_mfma_f32_16x16x32_bf16 a[20:23], a[192:195], a[176:179], a[20:23]\n"
"v_rcp_f32 v136, v136\n"
"v_rcp_f32 v137, v137\n"
"v_mfma_f32_16x16x32_bf16 v[156:159], a[192:195], a[172:175], v[156:159]\n"
"v_rcp_f32 v138, v138\n"
"v_mfma_f32_16x16x32_bf16 v[156:159], a[188:191], a[220:223], v[156:159]\n"
"v_rcp_f32 v139, v139\n"
"v_mul_f32 v136, v136, v120\n"
"v_mfma_f32_16x16x32_bf16 v[160:163], a[196:199], a[220:223], v[160:163]\n"
"buffer_load_dwordx4 a[68:71], v216, s[60:63], s85 offen offset:2048 sc1\n"
"v_mul_f32 v137, v137, v121\n"
"v_mul_f32 v138, v138, v122\n"
"v_mfma_f32_16x16x32_bf16 v[160:163], a[200:203], a[172:175], v[160:163]\n"
"v_mul_f32 v139, v139, v123\n"
"v_mul_f32 v140, s0, v60\n"
"v_mfma_f32_16x16x32_bf16 a[24:27], a[200:203], a[176:179], a[24:27]\n"
"v_mul_f32 v141, s0, v61\n"
"v_mul_f32 v142, s0, v62\n"
"v_mfma_f32_16x16x32_bf16 a[24:27], a[196:199], a[204:207], a[24:27]\n"
"v_mul_f32 v143, s0, v63\n"
"v_mul_f32 v124, v60, v124\n"
"v_mfma_f32_16x16x32_bf16 a[60:63], a[196:199], a[208:211], a[60:63]\n"
"v_exp_f32 v140, v140\n"
"v_mfma_f32_16x16x32_bf16 a[60:63], a[200:203], v[208:211], a[60:63]\n"
"v_exp_f32 v141, v141\n"
"v_mfma_f32_16x16x32_bf16 a[112:115], a[200:203], v[0:3], a[112:115]\n"
"buffer_load_dwordx4 a[72:75], v216, s[60:63], s86 offen offset:2048 sc1\n"
"v_mul_f32 v125, v61, v125\n"
"v_exp_f32 v142, v142\n"
"v_mfma_f32_16x16x32_bf16 a[112:115], a[196:199], a[168:171], a[112:115]\n"
"s_waitcnt lgkmcnt(0)\n"
"v_mul_f32 v126, v62, v126\n"
"v_mul_f32 v127, v63, v127\n"
"v_mfma_f32_16x16x32_bf16 v[164:167], a[148:151], a[220:223], v[164:167]\n"
"s_barrier\n"
"ds_read_b128 a[180:183], v216 offset:20480\n"
"v_exp_f32 v143, v143\n"
"v_mfma_f32_16x16x32_bf16 v[164:167], a[152:155], a[172:175], v[164:167]\n"
"ds_read_b128 a[184:187], v216 offset:25600\n"
"v_add_f32 v140, 1.0, v140\n"
"v_add_f32 v141, 1.0, v141\n"
"v_mfma_f32_16x16x32_bf16 a[28:31], a[152:155], a[176:179], a[28:31]\n"
"buffer_load_dwordx4 a[76:79], v216, s[60:63], s87 offen offset:2048 sc1\n"
"v_add_f32 v142, 1.0, v142\n"
"v_mfma_f32_16x16x32_bf16 a[28:31], a[148:151], a[204:207], a[28:31]\n"
"ds_read_b128 a[188:191], v216 offset:21504\n"
"v_add_f32 v143, 1.0, v143\n"
"v_rcp_f32 v140, v140\n"
"v_mfma_f32_16x16x32_bf16 a[80:83], a[148:151], a[208:211], a[80:83]\n"
"v_rcp_f32 v141, v141\n"
"v_rcp_f32 v142, v142\n"
"v_mfma_f32_16x16x32_bf16 a[80:83], a[152:155], v[208:211], a[80:83]\n"
"ds_read_b128 a[192:195], v216 offset:26624\n"
"v_rcp_f32 v143, v143\n"
"v_mfma_f32_16x16x32_bf16 a[116:119], a[152:155], v[0:3], a[116:119]\n"
"buffer_load_dwordx4 a[96:99], v216, s[60:63], s84 offen offset:3072 sc1\n"
"v_mul_f32 v140, v140, v124\n"
"v_mul_f32 v141, v141, v125\n"
"v_mfma_f32_16x16x32_bf16 a[116:119], a[148:151], a[168:171], a[116:119]\n"
"s_add_u32 s84, s16, s92\n"
"ds_read_b128 a[196:199], v216 offset:22528\n"
"v_mul_f32 v142, v142, v126\n"
"v_mul_f32 v143, v143, v127\n"
"v_mfma_f32_16x16x32_bf16 a[120:123], a[156:159], a[168:171], a[120:123]\n"
"ds_read_b128 a[200:203], v216 offset:27648\n"
"v_cvt_pk_bf16_f32 v136, v136, v137\n"
"v_mfma_f32_16x16x32_bf16 a[120:123], a[160:163], v[0:3], a[120:123]\n"
"v_cvt_pk_bf16_f32 v137, v138, v139\n"
"v_cvt_pk_bf16_f32 v138, v140, v141\n"
"v_mfma_f32_16x16x32_bf16 a[84:87], a[160:163], v[208:211], a[84:87]\n"
"buffer_load_dwordx4 a[100:103], v216, s[60:63], s85 offen offset:3072 sc1\n"
"v_cvt_pk_bf16_f32 v139, v142, v143\n"
"v_mfma_f32_16x16x32_bf16 a[84:87], a[156:159], a[208:211], a[84:87]\n"
"s_add_u32 s85, s17, s92\n"
"buffer_store_dwordx4 v[136:139], v216, s[68:71], s23 offen offset:1024 sc1\n"
"v_mfma_f32_16x16x32_bf16 a[48:51], a[156:159], a[204:207], a[48:51]\n"
"v_mfma_f32_16x16x32_bf16 a[48:51], a[160:163], a[176:179], a[48:51]\n"
"v_mfma_f32_16x16x32_bf16 v[168:171], a[160:163], a[172:175], v[168:171]\n"
"v_mfma_f32_16x16x32_bf16 v[168:171], a[156:159], a[220:223], v[168:171]\n"
"s_waitcnt vmcnt(15) lgkmcnt(3)\n"
"buffer_load_dwordx4 a[104:107], v216, s[60:63], s86 offen offset:3072 sc1\n"
"v_mfma_f32_16x16x32_bf16 v[152:155], a[180:183], a[0:3], v[152:155]\n"
"s_waitcnt vmcnt(11)\n"
"ds_read_b128 a[148:151], v216 offset:23552\n"
"s_add_u32 s86, s18, s92\n"
"v_mfma_f32_16x16x32_bf16 v[152:155], a[184:187], a[32:35], v[152:155]\n"
"s_waitcnt vmcnt(10) lgkmcnt(3)\n"
"ds_read_b128 a[152:155], v216 offset:28672\n"
"v_mfma_f32_16x16x32_bf16 a[16:19], a[184:187], a[36:39], a[16:19]\n"
"ds_read_b128 a[156:159], v216 offset:24576\n"
"v_mfma_f32_16x16x32_bf16 a[16:19], a[180:183], a[4:7], a[16:19]\n"
"buffer_load_dwordx4 a[108:111], v216, s[60:63], s87 offen offset:3072 sc1\n"
"v_mfma_f32_16x16x32_bf16 a[52:55], a[180:183], a[8:11], a[52:55]\n"
"s_waitcnt vmcnt(10) lgkmcnt(3)\n"
"ds_read_b128 a[160:163], v216 offset:29696\n"
"s_add_u32 s87, s19, s92\n"
"v_mfma_f32_16x16x32_bf16 a[52:55], a[184:187], a[40:43], a[52:55]\n"
"s_waitcnt vmcnt(9)\n"
"s_mul_i32 s92, s12, 54\n"
"s_add_u32 s92, s92, s72\n"
"v_mfma_f32_16x16x32_bf16 a[88:91], a[184:187], a[44:47], a[88:91]\n"
"s_mul_i32 s92, s92, 5120\n"
"s_add_u32 s32, s92, 33816576\n"
"s_lshr_b32 s92, s75, 3\n"
"v_mfma_f32_16x16x32_bf16 a[88:91], a[180:183], a[12:15], a[88:91]\n"
"s_mul_i32 s92, s92, 3\n"
"buffer_load_dwordx4 a[144:147], v216, s[60:63], s84 offen sc1\n"
"v_mfma_f32_16x16x32_bf16 a[92:95], a[188:191], a[12:15], a[92:95]\n"
"s_add_u32 s92, s92, s93\n"
"s_mul_i32 s93, s92, 79\n"
"s_lshr_b32 s93, s93, 10\n"
"v_mfma_f32_16x16x32_bf16 a[92:95], a[192:195], a[44:47], a[92:95]\n"
"s_mul_i32 s93, s93, 13\n"
"s_sub_u32 s33, s92, s93\n"
"s_mul_i32 s83, s33, 20480\n"
"v_mfma_f32_16x16x32_bf16 a[56:59], a[192:195], a[40:43], a[56:59]\n"
"s_add_u32 s83, s83, s32\n"
"s_add_u32 s82, s83, 1024\n"
"s_cmp_gt_u32 s11, 4\n"
"s_cselect_b32 s92, 4, 5\n"
"v_mfma_f32_16x16x32_bf16 a[56:59], a[188:191], a[8:11], a[56:59]\n"
"s_lshl_b32 s92, s92, 12\n"
"buffer_load_dwordx4 a[224:227], v216, s[60:63], s85 offen sc1\n"
"v_mfma_f32_16x16x32_bf16 a[20:23], a[188:191], a[4:7], a[20:23]\n"
"s_lshl_b32 s34, s33, 12\n"
"s_add_u32 s33, s33, 1\n"
"s_cmp_eq_u32 s33, 13\n"
"s_cselect_b32 s33, 0, s33\n"
"v_mfma_f32_16x16x32_bf16 a[20:23], a[192:195], a[36:39], a[20:23]\n"
"v_mfma_f32_16x16x32_bf16 v[156:159], a[192:195], a[32:35], v[156:159]\n"
"v_mfma_f32_16x16x32_bf16 v[156:159], a[188:191], a[0:3], v[156:159]\n"
"buffer_load_dwordx4 a[228:231], v216, s[60:63], s86 offen sc1\n"
"v_mfma_f32_16x16x32_bf16 v[160:163], a[196:199], a[0:3], v[160:163]\n"
"v_mfma_f32_16x16x32_bf16 v[160:163], a[200:203], a[32:35], v[160:163]\n"
"v_mfma_f32_16x16x32_bf16 a[24:27], a[200:203], a[36:39], a[24:27]\n"
"v_mfma_f32_16x16x32_bf16 a[24:27], a[196:199], a[4:7], a[24:27]\n"
"buffer_load_dwordx4 a[232:235], v216, s[60:63], s87 offen sc1\n"
"v_mfma_f32_16x16x32_bf16 a[60:63], a[196:199], a[8:11], a[60:63]\n"
"v_mfma_f32_16x16x32_bf16 a[60:63], a[200:203], a[40:43], a[60:63]\n"
"v_mfma_f32_16x16x32_bf16 a[112:115], a[200:203], a[44:47], a[112:115]\n"
"v_mfma_f32_16x16x32_bf16 a[112:115], a[196:199], a[12:15], a[112:115]\n"
"s_waitcnt lgkmcnt(0)\n"
"buffer_load_dwordx4 a[124:127], v216, s[60:63], s84 offen offset:1024 sc1\n"
"v_mfma_f32_16x16x32_bf16 v[164:167], a[148:151], a[0:3], v[164:167]\n"
"ds_read_b128 a[180:183], v216 offset:30720\n"
"v_mfma_f32_16x16x32_bf16 v[164:167], a[152:155], a[32:35], v[164:167]\n"
"ds_read_b128 a[184:187], v216 offset:35840\n"
"v_mfma_f32_16x16x32_bf16 a[28:31], a[152:155], a[36:39], a[28:31]\n"
"ds_read_b128 a[188:191], v216 offset:31744\n"
"v_mfma_f32_16x16x32_bf16 a[28:31], a[148:151], a[4:7], a[28:31]\n"
"buffer_load_dwordx4 a[236:239], v216, s[60:63], s85 offen offset:1024 sc1\n"
"v_mfma_f32_16x16x32_bf16 a[80:83], a[148:151], a[8:11], a[80:83]\n"
"ds_read_b128 a[192:195], v216 offset:36864\n"
"v_mfma_f32_16x16x32_bf16 a[80:83], a[152:155], a[40:43], a[80:83]\n"
"ds_read_b128 a[196:199], v216 offset:32768\n"
"v_mfma_f32_16x16x32_bf16 a[116:119], a[152:155], a[44:47], a[116:119]\n"
"ds_read_b128 a[200:203], v216 offset:37888\n"
"v_mfma_f32_16x16x32_bf16 a[116:119], a[148:151], a[12:15], a[116:119]\n"
"buffer_load_dwordx4 a[240:243], v216, s[60:63], s86 offen offset:1024 sc1\n"
"v_mfma_f32_16x16x32_bf16 a[120:123], a[156:159], a[12:15], a[120:123]\n"
"v_mfma_f32_16x16x32_bf16 a[120:123], a[160:163], a[44:47], a[120:123]\n"
"v_mfma_f32_16x16x32_bf16 a[84:87], a[160:163], a[40:43], a[84:87]\n"
"v_mfma_f32_16x16x32_bf16 a[84:87], a[156:159], a[8:11], a[84:87]\n"
"buffer_load_dwordx4 a[244:247], v216, s[60:63], s87 offen offset:1024 sc1\n"
"v_mfma_f32_16x16x32_bf16 a[48:51], a[156:159], a[4:7], a[48:51]\n"
"v_mfma_f32_16x16x32_bf16 a[48:51], a[160:163], a[36:39], a[48:51]\n"
"v_mfma_f32_16x16x32_bf16 v[168:171], a[160:163], a[32:35], v[168:171]\n"
"v_mfma_f32_16x16x32_bf16 v[168:171], a[156:159], a[0:3], v[168:171]\n"
"s_waitcnt vmcnt(16) lgkmcnt(3)\n"
"buffer_load_dwordx4 v216, s[60:63], s83 offen sc1 lds\n"
"v_mfma_f32_16x16x32_bf16 v[152:155], a[180:183], a[64:67], v[152:155]\n"
"s_waitcnt vmcnt(13) lgkmcnt(2)\n"
"ds_read_b128 a[148:151], v216 offset:33792\n"
"v_mfma_f32_16x16x32_bf16 v[152:155], a[184:187], a[96:99], v[152:155]\n"
"s_waitcnt vmcnt(12) lgkmcnt(2)\n"
"ds_read_b128 a[152:155], v216 offset:38912\n"
"v_mfma_f32_16x16x32_bf16 a[16:19], a[184:187], a[100:103], a[16:19]\n"
"ds_read_b128 a[156:159], v216 offset:34816\n"
"v_mfma_f32_16x16x32_bf16 a[16:19], a[180:183], a[68:71], a[16:19]\n"
"buffer_load_dwordx4 v216, s[60:63], s83 offen offset:1024 sc1 lds\n"
"v_mfma_f32_16x16x32_bf16 a[52:55], a[180:183], a[72:75], a[52:55]\n"
"s_waitcnt vmcnt(11) lgkmcnt(3)\n"
"ds_read_b128 a[160:163], v216 offset:39936\n"
"v_mfma_f32_16x16x32_bf16 a[52:55], a[184:187], a[104:107], a[52:55]\n"
"s_waitcnt vmcnt(10)\n"
"v_mfma_f32_16x16x32_bf16 a[88:91], a[184:187], a[108:111], a[88:91]\n"
"v_mfma_f32_16x16x32_bf16 a[88:91], a[180:183], a[76:79], a[88:91]\n"
"buffer_load_dwordx4 v216, s[60:63], s83 offen offset:2048 sc1 lds\n"
"v_mfma_f32_16x16x32_bf16 a[92:95], a[188:191], a[76:79], a[92:95]\n"
"v_mfma_f32_16x16x32_bf16 a[92:95], a[192:195], a[108:111], a[92:95]\n"
"v_mfma_f32_16x16x32_bf16 a[56:59], a[192:195], a[104:107], a[56:59]\n"
"v_mfma_f32_16x16x32_bf16 a[56:59], a[188:191], a[72:75], a[56:59]\n"
"buffer_load_dwordx4 v216, s[60:63], s83 offen offset:3072 sc1 lds\n"
"v_mfma_f32_16x16x32_bf16 a[20:23], a[188:191], a[68:71], a[20:23]\n"
"s_addk_i32 m0, 1024\n"
"s_mul_i32 s83, s33, 20480\n"
"s_add_u32 s83, s83, s32\n"
"v_mfma_f32_16x16x32_bf16 a[20:23], a[192:195], a[100:103], a[20:23]\n"
"s_add_u32 s33, s33, 1\n"
"s_cmp_eq_u32 s33, 13\n"
"s_cselect_b32 s33, 0, s33\n"
"v_mfma_f32_16x16x32_bf16 v[156:159], a[192:195], a[96:99], v[156:159]\n"
"v_mfma_f32_16x16x32_bf16 v[156:159], a[188:191], a[64:67], v[156:159]\n"
"buffer_load_dwordx4 v216, s[60:63], s82 offen offset:3072 sc1 lds\n"
"v_mfma_f32_16x16x32_bf16 v[160:163], a[196:199], a[64:67], v[160:163]\n"
"s_addk_i32 m0, 44032\n"
"s_add_u32 s82, s83, 1024\n"
"v_mfma_f32_16x16x32_bf16 v[160:163], a[200:203], a[96:99], v[160:163]\n"
"v_mfma_f32_16x16x32_bf16 a[24:27], a[200:203], a[100:103], a[24:27]\n"
"v_mfma_f32_16x16x32_bf16 a[24:27], a[196:199], a[68:71], a[24:27]\n"
"buffer_load_dwordx4 a[248:251], v216, s[60:63], s84 offen offset:2048 sc1\n"
"v_mfma_f32_16x16x32_bf16 a[60:63], a[196:199], a[72:75], a[60:63]\n"
"v_mfma_f32_16x16x32_bf16 a[60:63], a[200:203], a[104:107], a[60:63]\n"
"v_mfma_f32_16x16x32_bf16 a[112:115], a[200:203], a[108:111], a[112:115]\n"
"v_mfma_f32_16x16x32_bf16 a[112:115], a[196:199], a[76:79], a[112:115]\n"
"s_waitcnt lgkmcnt(0)\n"
"s_barrier\n"
"s_lshr_b32 s93, s27, 4\n"
"v_mfma_f32_16x16x32_bf16 v[164:167], a[148:151], a[64:67], v[164:167]\n"
"s_and_b32 s93, s93, 4\n"
"buffer_load_dwordx4 a[252:255], v216, s[60:63], s85 offen offset:2048 sc1\n"
"v_mfma_f32_16x16x32_bf16 v[164:167], a[152:155], a[96:99], v[164:167]\n"
"s_and_b32 s99, s27, 3\n"
"ds_read_b128 a[180:183], v217 offset:40960\n"
"s_add_u32 s99, s99, s93\n"
"v_mfma_f32_16x16x32_bf16 a[28:31], a[152:155], a[100:103], a[28:31]\n"
"s_lshl_b32 s99, s99, 5\n"
"ds_read_b128 a[184:187], v217 offset:46080\n"
"s_add_u32 s99, s99, s79\n"
"s_lshl_b32 s99, s99, 7\n"
"s_add_u32 s99, s99, 98304\n"
"buffer_store_dwordx2 v[232:233], v231, s[60:63], s99 offen sc1\n"
"v_mfma_f32_16x16x32_bf16 a[28:31], a[148:151], a[68:71], a[28:31]\n"
"ds_read_b128 a[188:191], v217 offset:41984\n"
"v_mfma_f32_16x16x32_bf16 a[80:83], a[148:151], a[72:75], a[80:83]\n"
"ds_read_b128 a[192:195], v217 offset:47104\n"
"v_mfma_f32_16x16x32_bf16 a[80:83], a[152:155], a[104:107], a[80:83]\n"
"ds_read_b128 a[196:199], v217 offset:43008\n"
"v_mfma_f32_16x16x32_bf16 a[116:119], a[152:155], a[108:111], a[116:119]\n"
"buffer_load_dwordx4 v[4:7], v216, s[60:63], s86 offen offset:2048 sc1\n"
"v_mfma_f32_16x16x32_bf16 a[116:119], a[148:151], a[76:79], a[116:119]\n"
"ds_read_b128 a[200:203], v217 offset:48128\n"
"v_mfma_f32_16x16x32_bf16 a[120:123], a[156:159], a[76:79], a[120:123]\n"
"v_mfma_f32_16x16x32_bf16 a[120:123], a[160:163], a[108:111], a[120:123]\n"
"v_mfma_f32_16x16x32_bf16 a[84:87], a[160:163], a[104:107], a[84:87]\n"
"buffer_load_dwordx4 v[64:67], v216, s[60:63], s87 offen offset:2048 sc1\n"
"v_mfma_f32_16x16x32_bf16 a[84:87], a[156:159], a[72:75], a[84:87]\n"
"v_mfma_f32_16x16x32_bf16 a[48:51], a[156:159], a[68:71], a[48:51]\n"
"v_mfma_f32_16x16x32_bf16 a[48:51], a[160:163], a[100:103], a[48:51]\n"
"v_mfma_f32_16x16x32_bf16 v[168:171], a[160:163], a[96:99], v[168:171]\n"
"buffer_load_dwordx4 v[68:71], v216, s[60:63], s84 offen offset:3072 sc1\n"
"v_mfma_f32_16x16x32_bf16 v[168:171], a[156:159], a[64:67], v[168:171]\n"
"s_waitcnt vmcnt(18) lgkmcnt(3)\n"
"ds_read_b128 a[148:151], v217 offset:44032\n"
"s_add_u32 s84, s16, s92\n"
"v_mfma_f32_16x16x32_bf16 v[152:155], a[180:183], a[144:147], v[152:155]\n"
"s_waitcnt vmcnt(14) lgkmcnt(3)\n"
"ds_read_b128 a[152:155], v217 offset:49152\n"
"v_mfma_f32_16x16x32_bf16 v[152:155], a[184:187], a[124:127], v[152:155]\n"
"s_waitcnt vmcnt(13) lgkmcnt(3)\n"
"buffer_load_dwordx4 v[8:11], v216, s[60:63], s85 offen offset:3072 sc1\n"
"v_mfma_f32_16x16x32_bf16 a[16:19], a[184:187], a[236:239], a[16:19]\n"
"s_add_u32 s85, s17, s92\n"
"ds_read_b128 a[156:159], v217 offset:45056\n"
"v_mfma_f32_16x16x32_bf16 a[16:19], a[180:183], a[224:227], a[16:19]\n"
"ds_read_b128 a[160:163], v217 offset:50176\n"
"v_mfma_f32_16x16x32_bf16 a[52:55], a[180:183], a[228:231], a[52:55]\n"
"s_waitcnt vmcnt(13) lgkmcnt(4)\n"
"buffer_load_dwordx4 v[12:15], v216, s[60:63], s86 offen offset:3072 sc1\n"
"v_mfma_f32_16x16x32_bf16 a[52:55], a[184:187], a[240:243], a[52:55]\n"
"s_waitcnt vmcnt(13)\n"
"buffer_load_dwordx4 v[72:75], v216, s[60:63], s87 offen offset:3072 sc1\n"
"v_mfma_f32_16x16x32_bf16 a[88:91], a[184:187], a[244:247], a[88:91]\n"
"s_add_u32 s86, s18, s92\n"
"s_add_u32 s87, s19, s92\n"
"s_cmp_gt_u32 s11, 5\n"
"s_cselect_b32 s92, 5, 6\n"
"v_mfma_f32_16x16x32_bf16 a[88:91], a[180:183], a[232:235], a[88:91]\n"
"s_lshl_b32 s92, s92, 12\n"
"v_mfma_f32_16x16x32_bf16 a[92:95], a[188:191], a[232:235], a[92:95]\n"
"v_mfma_f32_16x16x32_bf16 a[92:95], a[192:195], a[244:247], a[92:95]\n"
"buffer_load_dwordx4 a[128:131], v216, s[60:63], s84 offen sc1\n"
"v_mfma_f32_16x16x32_bf16 a[56:59], a[192:195], a[240:243], a[56:59]\n"
"v_mfma_f32_16x16x32_bf16 a[56:59], a[188:191], a[228:231], a[56:59]\n"
"v_mfma_f32_16x16x32_bf16 a[20:23], a[188:191], a[224:227], a[20:23]\n"
"v_mfma_f32_16x16x32_bf16 a[20:23], a[192:195], a[236:239], a[20:23]\n"
"buffer_load_dwordx4 a[132:135], v216, s[60:63], s85 offen sc1\n"
"v_mfma_f32_16x16x32_bf16 v[156:159], a[192:195], a[124:127], v[156:159]\n"
"v_mfma_f32_16x16x32_bf16 v[156:159], a[188:191], a[144:147], v[156:159]\n"
"v_mfma_f32_16x16x32_bf16 v[160:163], a[196:199], a[144:147], v[160:163]\n"
"v_mfma_f32_16x16x32_bf16 v[160:163], a[200:203], a[124:127], v[160:163]\n"
"buffer_load_dwordx4 a[136:139], v216, s[60:63], s86 offen sc1\n"
"v_mfma_f32_16x16x32_bf16 a[24:27], a[200:203], a[236:239], a[24:27]\n"
"v_mfma_f32_16x16x32_bf16 a[24:27], a[196:199], a[224:227], a[24:27]\n"
"v_mfma_f32_16x16x32_bf16 a[60:63], a[196:199], a[228:231], a[60:63]\n"
"v_mfma_f32_16x16x32_bf16 a[60:63], a[200:203], a[240:243], a[60:63]\n"
"buffer_load_dwordx4 a[140:143], v216, s[60:63], s87 offen sc1\n"
"v_mfma_f32_16x16x32_bf16 a[112:115], a[200:203], a[244:247], a[112:115]\n"
"v_mfma_f32_16x16x32_bf16 a[112:115], a[196:199], a[232:235], a[112:115]\n"
"s_waitcnt lgkmcnt(0)\n"
"ds_read_b128 a[180:183], v217 offset:51200\n"
"v_mfma_f32_16x16x32_bf16 v[164:167], a[148:151], a[144:147], v[164:167]\n"
"ds_read_b128 a[184:187], v217 offset:56320\n"
"v_mfma_f32_16x16x32_bf16 v[164:167], a[152:155], a[124:127], v[164:167]\n"
"buffer_load_dwordx4 a[212:215], v216, s[60:63], s84 offen offset:1024 sc1\n"
"v_mfma_f32_16x16x32_bf16 a[28:31], a[152:155], a[236:239], a[28:31]\n"
"ds_read_b128 a[188:191], v217 offset:52224\n"
"v_mfma_f32_16x16x32_bf16 a[28:31], a[148:151], a[224:227], a[28:31]\n"
"ds_read_b128 a[192:195], v217 offset:57344\n"
"v_mfma_f32_16x16x32_bf16 a[80:83], a[148:151], a[228:231], a[80:83]\n"
"ds_read_b128 a[196:199], v217 offset:53248\n"
"v_mfma_f32_16x16x32_bf16 a[80:83], a[152:155], a[240:243], a[80:83]\n"
"buffer_load_dwordx4 a[216:219], v216, s[60:63], s85 offen offset:1024 sc1\n"
"v_mfma_f32_16x16x32_bf16 a[116:119], a[152:155], a[244:247], a[116:119]\n"
"ds_read_b128 a[200:203], v217 offset:58368\n"
"v_mfma_f32_16x16x32_bf16 a[116:119], a[148:151], a[232:235], a[116:119]\n"
"v_mfma_f32_16x16x32_bf16 a[120:123], a[156:159], a[232:235], a[120:123]\n"
"v_mfma_f32_16x16x32_bf16 a[120:123], a[160:163], a[244:247], a[120:123]\n"
"buffer_load_dwordx4 v[76:79], v216, s[60:63], s86 offen offset:1024 sc1\n"
"v_mfma_f32_16x16x32_bf16 a[84:87], a[160:163], a[240:243], a[84:87]\n"
"v_mfma_f32_16x16x32_bf16 a[84:87], a[156:159], a[228:231], a[84:87]\n"
"v_mfma_f32_16x16x32_bf16 a[48:51], a[156:159], a[224:227], a[48:51]\n"
"v_mfma_f32_16x16x32_bf16 a[48:51], a[160:163], a[236:239], a[48:51]\n"
"buffer_load_dwordx4 v[16:19], v216, s[60:63], s87 offen offset:1024 sc1\n"
"v_mfma_f32_16x16x32_bf16 v[168:171], a[160:163], a[124:127], v[168:171]\n"
"v_mfma_f32_16x16x32_bf16 v[168:171], a[156:159], a[144:147], v[168:171]\n"
"s_waitcnt vmcnt(16) lgkmcnt(2)\n"
"ds_read_b128 a[148:151], v217 offset:54272\n"
"v_mfma_f32_16x16x32_bf16 v[152:155], a[180:183], a[248:251], v[152:155]\n"
"s_waitcnt vmcnt(11) lgkmcnt(2)\n"
"ds_read_b128 a[152:155], v217 offset:59392\n"
"v_mfma_f32_16x16x32_bf16 v[152:155], a[184:187], v[68:71], v[152:155]\n"
"s_waitcnt vmcnt(10)\n"
"buffer_load_dwordx4 v216, s[60:63], s83 offen sc1 lds\n"
"v_mfma_f32_16x16x32_bf16 a[16:19], a[184:187], v[8:11], a[16:19]\n"
"ds_read_b128 a[156:159], v217 offset:55296\n"
"v_mfma_f32_16x16x32_bf16 a[16:19], a[180:183], a[252:255], a[16:19]\n"
"ds_read_b128 a[160:163], v217 offset:60416\n"
"v_mfma_f32_16x16x32_bf16 a[52:55], a[180:183], v[4:7], a[52:55]\n"
"s_waitcnt vmcnt(10) lgkmcnt(4)\n"
"v_mfma_f32_16x16x32_bf16 a[52:55], a[184:187], v[12:15], a[52:55]\n"
"s_waitcnt vmcnt(9)\n"
"buffer_load_dwordx4 v216, s[60:63], s83 offen offset:1024 sc1 lds\n"
"v_mfma_f32_16x16x32_bf16 a[88:91], a[184:187], v[72:75], a[88:91]\n"
"v_mfma_f32_16x16x32_bf16 a[88:91], a[180:183], v[64:67], a[88:91]\n"
"v_mfma_f32_16x16x32_bf16 a[92:95], a[188:191], v[64:67], a[92:95]\n"
"v_mfma_f32_16x16x32_bf16 a[92:95], a[192:195], v[72:75], a[92:95]\n"
"buffer_load_dwordx4 v216, s[60:63], s83 offen offset:2048 sc1 lds\n"
"v_mfma_f32_16x16x32_bf16 a[56:59], a[192:195], v[12:15], a[56:59]\n"
"v_mfma_f32_16x16x32_bf16 a[56:59], a[188:191], v[4:7], a[56:59]\n"
"v_mfma_f32_16x16x32_bf16 a[20:23], a[188:191], a[252:255], a[20:23]\n"
"v_mfma_f32_16x16x32_bf16 a[20:23], a[192:195], v[8:11], a[20:23]\n"
"buffer_load_dwordx4 v216, s[60:63], s83 offen offset:3072 sc1 lds\n"
"v_mfma_f32_16x16x32_bf16 v[156:159], a[192:195], v[68:71], v[156:159]\n"
"s_addk_i32 m0, 1024\n"
"s_mul_i32 s83, s33, 20480\n"
"s_add_u32 s83, s83, s32\n"
"v_mfma_f32_16x16x32_bf16 v[156:159], a[188:191], a[248:251], v[156:159]\n"
"s_add_u32 s33, s33, 1\n"
"s_cmp_eq_u32 s33, 13\n"
"s_cselect_b32 s33, 0, s33\n"
"v_mfma_f32_16x16x32_bf16 v[160:163], a[196:199], a[248:251], v[160:163]\n"
"v_mfma_f32_16x16x32_bf16 v[160:163], a[200:203], v[68:71], v[160:163]\n"
"buffer_load_dwordx4 v216, s[60:63], s82 offen offset:3072 sc1 lds\n"
"v_mfma_f32_16x16x32_bf16 a[24:27], a[200:203], v[8:11], a[24:27]\n"
"s_add_u32 m0, s78, 0\n"
"s_add_u32 s82, s83, 1024\n"
"v_mfma_f32_16x16x32_bf16 a[24:27], a[196:199], a[252:255], a[24:27]\n"
"v_mfma_f32_16x16x32_bf16 a[60:63], a[196:199], v[4:7], a[60:63]\n"
"v_mfma_f32_16x16x32_bf16 a[60:63], a[200:203], v[12:15], a[60:63]\n"
"buffer_load_dwordx4 a[220:223], v216, s[60:63], s84 offen offset:2048 sc1\n"
"v_mfma_f32_16x16x32_bf16 a[112:115], a[200:203], v[72:75], a[112:115]\n"
"v_mfma_f32_16x16x32_bf16 a[112:115], a[196:199], v[64:67], a[112:115]\n"
"s_waitcnt lgkmcnt(0)\n"
"s_barrier\n"
"ds_read_b128 a[180:183], v218 offset:0\n"
"v_mfma_f32_16x16x32_bf16 v[164:167], a[148:151], a[248:251], v[164:167]\n"
"ds_read_b128 a[184:187], v218 offset:5120\n"
"v_mfma_f32_16x16x32_bf16 v[164:167], a[152:155], v[68:71], v[164:167]\n"
"buffer_load_dwordx4 a[204:207], v216, s[60:63], s85 offen offset:2048 sc1\n"
"v_mfma_f32_16x16x32_bf16 a[28:31], a[152:155], v[8:11], a[28:31]\n"
"ds_read_b128 a[188:191], v218 offset:1024\n"
"v_mfma_f32_16x16x32_bf16 a[28:31], a[148:151], a[252:255], a[28:31]\n"
"ds_read_b128 a[192:195], v218 offset:6144\n"
"v_mfma_f32_16x16x32_bf16 a[80:83], a[148:151], v[4:7], a[80:83]\n"
"ds_read_b128 a[196:199], v218 offset:2048\n"
"v_mfma_f32_16x16x32_bf16 a[80:83], a[152:155], v[12:15], a[80:83]\n"
"buffer_load_dwordx4 a[208:211], v216, s[60:63], s86 offen offset:2048 sc1\n"
"v_mfma_f32_16x16x32_bf16 a[116:119], a[152:155], v[72:75], a[116:119]\n"
"ds_read_b128 a[200:203], v218 offset:7168\n"
"v_mfma_f32_16x16x32_bf16 a[116:119], a[148:151], v[64:67], a[116:119]\n"
"v_mfma_f32_16x16x32_bf16 a[120:123], a[156:159], v[64:67], a[120:123]\n"
"v_mfma_f32_16x16x32_bf16 a[120:123], a[160:163], v[72:75], a[120:123]\n"
"buffer_load_dwordx4 a[168:171], v216, s[60:63], s87 offen offset:2048 sc1\n"
"v_mfma_f32_16x16x32_bf16 a[84:87], a[160:163], v[12:15], a[84:87]\n"
"v_mfma_f32_16x16x32_bf16 a[84:87], a[156:159], v[4:7], a[84:87]\n"
"v_mfma_f32_16x16x32_bf16 a[48:51], a[156:159], a[252:255], a[48:51]\n"
"v_mfma_f32_16x16x32_bf16 a[48:51], a[160:163], v[8:11], a[48:51]\n"
"buffer_load_dwordx4 a[172:175], v216, s[60:63], s84 offen offset:3072 sc1\n"
"v_mfma_f32_16x16x32_bf16 v[168:171], a[160:163], v[68:71], v[168:171]\n"
"s_add_u32 s84, s16, s92\n"
"v_mfma_f32_16x16x32_bf16 v[168:171], a[156:159], a[248:251], v[168:171]\n"
"s_waitcnt vmcnt(17) lgkmcnt(2)\n"
"ds_read_b128 a[148:151], v218 offset:3072\n"
"v_mfma_f32_16x16x32_bf16 v[152:155], a[180:183], a[128:131], v[152:155]\n"
"s_waitcnt vmcnt(13) lgkmcnt(2)\n"
"ds_read_b128 a[152:155], v218 offset:8192\n"
"v_mfma_f32_16x16x32_bf16 v[152:155], a[184:187], a[212:215], v[152:155]\n"
"s_waitcnt vmcnt(12)\n"
"buffer_load_dwordx4 a[176:179], v216, s[60:63], s85 offen offset:3072 sc1\n"
"v_mfma_f32_16x16x32_bf16 a[16:19], a[184:187], a[216:219], a[16:19]\n"
"s_add_u32 s85, s17, s92\n"
"ds_read_b128 a[156:159], v218 offset:4096\n"
"v_mfma_f32_16x16x32_bf16 a[16:19], a[180:183], a[132:135], a[16:19]\n"
"ds_read_b128 a[160:163], v218 offset:9216\n"
"v_mfma_f32_16x16x32_bf16 a[52:55], a[180:183], a[136:139], a[52:55]\n"
"s_waitcnt vmcnt(12) lgkmcnt(4)\n"
"buffer_load_dwordx4 v[20:23], v216, s[60:63], s86 offen offset:3072 sc1\n"
"v_mfma_f32_16x16x32_bf16 a[52:55], a[184:187], v[76:79], a[52:55]\n"
"s_waitcnt vmcnt(12)\n"
"buffer_load_dwordx4 v[80:83], v216, s[60:63], s87 offen offset:3072 sc1\n"
"v_mfma_f32_16x16x32_bf16 a[88:91], a[184:187], v[16:19], a[88:91]\n"
"s_add_u32 s86, s18, s92\n"
"s_add_u32 s87, s19, s92\n"
"s_cmp_gt_u32 s11, 6\n"
"s_cselect_b32 s92, 6, 7\n"
"v_mfma_f32_16x16x32_bf16 a[88:91], a[180:183], a[140:143], a[88:91]\n"
"s_lshl_b32 s92, s92, 12\n"
"v_mfma_f32_16x16x32_bf16 a[92:95], a[188:191], a[140:143], a[92:95]\n"
"v_mfma_f32_16x16x32_bf16 a[92:95], a[192:195], v[16:19], a[92:95]\n"
"buffer_load_dwordx4 a[0:3], v216, s[60:63], s84 offen sc1\n"
"v_mfma_f32_16x16x32_bf16 a[56:59], a[192:195], v[76:79], a[56:59]\n"
"v_mfma_f32_16x16x32_bf16 a[56:59], a[188:191], a[136:139], a[56:59]\n"
"v_mfma_f32_16x16x32_bf16 a[20:23], a[188:191], a[132:135], a[20:23]\n"
"v_mfma_f32_16x16x32_bf16 a[20:23], a[192:195], a[216:219], a[20:23]\n"
"buffer_load_dwordx4 a[4:7], v216, s[60:63], s85 offen sc1\n"
"v_mfma_f32_16x16x32_bf16 v[156:159], a[192:195], a[212:215], v[156:159]\n"
"v_mfma_f32_16x16x32_bf16 v[156:159], a[188:191], a[128:131], v[156:159]\n"
"v_mfma_f32_16x16x32_bf16 v[160:163], a[196:199], a[128:131], v[160:163]\n"
"v_mfma_f32_16x16x32_bf16 v[160:163], a[200:203], a[212:215], v[160:163]\n"
"buffer_load_dwordx4 a[8:11], v216, s[60:63], s86 offen sc1\n"
"v_mfma_f32_16x16x32_bf16 a[24:27], a[200:203], a[216:219], a[24:27]\n"
"v_mfma_f32_16x16x32_bf16 a[24:27], a[196:199], a[132:135], a[24:27]\n"
"v_mfma_f32_16x16x32_bf16 a[60:63], a[196:199], a[136:139], a[60:63]\n"
"v_mfma_f32_16x16x32_bf16 a[60:63], a[200:203], v[76:79], a[60:63]\n"
"buffer_load_dwordx4 a[12:15], v216, s[60:63], s87 offen sc1\n"
"v_mfma_f32_16x16x32_bf16 a[112:115], a[200:203], v[16:19], a[112:115]\n"
"v_mfma_f32_16x16x32_bf16 a[112:115], a[196:199], a[140:143], a[112:115]\n"
"s_waitcnt lgkmcnt(0)\n"
"ds_read_b128 a[180:183], v218 offset:10240\n"
"v_mfma_f32_16x16x32_bf16 v[164:167], a[148:151], a[128:131], v[164:167]\n"
"ds_read_b128 a[184:187], v218 offset:15360\n"
"v_mfma_f32_16x16x32_bf16 v[164:167], a[152:155], a[212:215], v[164:167]\n"
"buffer_load_dwordx4 a[32:35], v216, s[60:63], s84 offen offset:1024 sc1\n"
"v_mfma_f32_16x16x32_bf16 a[28:31], a[152:155], a[216:219], a[28:31]\n"
"ds_read_b128 a[188:191], v218 offset:11264\n"
"v_mfma_f32_16x16x32_bf16 a[28:31], a[148:151], a[132:135], a[28:31]\n"
"ds_read_b128 a[192:195], v218 offset:16384\n"
"v_mfma_f32_16x16x32_bf16 a[80:83], a[148:151], a[136:139], a[80:83]\n"
"ds_read_b128 a[196:199], v218 offset:12288\n"
"v_mfma_f32_16x16x32_bf16 a[80:83], a[152:155], v[76:79], a[80:83]\n"
"buffer_load_dwordx4 a[36:39], v216, s[60:63], s85 offen offset:1024 sc1\n"
"v_mfma_f32_16x16x32_bf16 a[116:119], a[152:155], v[16:19], a[116:119]\n"
"ds_read_b128 a[200:203], v218 offset:17408\n"
"v_mfma_f32_16x16x32_bf16 a[116:119], a[148:151], a[140:143], a[116:119]\n"
"v_mfma_f32_16x16x32_bf16 a[120:123], a[156:159], a[140:143], a[120:123]\n"
"v_mfma_f32_16x16x32_bf16 a[120:123], a[160:163], v[16:19], a[120:123]\n"
"buffer_load_dwordx4 a[40:43], v216, s[60:63], s86 offen offset:1024 sc1\n"
"v_mfma_f32_16x16x32_bf16 a[84:87], a[160:163], v[76:79], a[84:87]\n"
"v_mfma_f32_16x16x32_bf16 a[84:87], a[156:159], a[136:139], a[84:87]\n"
"v_mfma_f32_16x16x32_bf16 a[48:51], a[156:159], a[132:135], a[48:51]\n"
"v_mfma_f32_16x16x32_bf16 a[48:51], a[160:163], a[216:219], a[48:51]\n"
"buffer_load_dwordx4 a[44:47], v216, s[60:63], s87 offen offset:1024 sc1\n"
"v_mfma_f32_16x16x32_bf16 v[168:171], a[160:163], a[212:215], v[168:171]\n"
"v_mfma_f32_16x16x32_bf16 v[168:171], a[156:159], a[128:131], v[168:171]\n"
"s_waitcnt vmcnt(15) lgkmcnt(2)\n"
"s_getpc_b64 s[4:5]\n"
"L_pc6_%=:\n"
"s_mov_b32 s6, L_end_%=-L_pc6_%=\n"
"s_and_b32 s5, s5, 0xffff\n"
"s_mov_b32 s7, 0x20000\n"
"v_mfma_f32_16x16x32_bf16 v[152:155], a[180:183], a[220:223], v[152:155]\n"
"s_waitcnt vmcnt(11) lgkmcnt(1)\n"
"ds_read_b128 a[148:151], v218 offset:13312\n"
"v_mfma_f32_16x16x32_bf16 v[152:155], a[184:187], a[172:175], v[152:155]\n"
"s_waitcnt vmcnt(10)\n"
"buffer_load_dword v247, v252, s[4:7], 0 offen\n"
"v_mfma_f32_16x16x32_bf16 a[16:19], a[184:187], a[176:179], a[16:19]\n"
"ds_read_b128 a[152:155], v218 offset:18432\n"
"v_mfma_f32_16x16x32_bf16 a[16:19], a[180:183], a[204:207], a[16:19]\n"
"ds_read_b128 a[156:159], v218 offset:14336\n"
"v_mfma_f32_16x16x32_bf16 a[52:55], a[180:183], a[208:211], a[52:55]\n"
"s_waitcnt vmcnt(10) lgkmcnt(3)\n"
"ds_read_b128 a[160:163], v218 offset:19456\n"
"v_mfma_f32_16x16x32_bf16 a[52:55], a[184:187], v[20:23], a[52:55]\n"
"s_waitcnt vmcnt(9)\n"
"buffer_load_dwordx4 v216, s[60:63], s83 offen sc1 lds\n"
"v_mfma_f32_16x16x32_bf16 a[88:91], a[184:187], v[80:83], a[88:91]\n"
"v_mfma_f32_16x16x32_bf16 a[88:91], a[180:183], a[168:171], a[88:91]\n"
"v_mfma_f32_16x16x32_bf16 a[92:95], a[188:191], a[168:171], a[92:95]\n"
"v_mfma_f32_16x16x32_bf16 a[92:95], a[192:195], v[80:83], a[92:95]\n"
"buffer_load_dwordx4 v216, s[60:63], s83 offen offset:1024 sc1 lds\n"
"v_mfma_f32_16x16x32_bf16 a[56:59], a[192:195], v[20:23], a[56:59]\n"
"v_mfma_f32_16x16x32_bf16 a[56:59], a[188:191], a[208:211], a[56:59]\n"
"v_mfma_f32_16x16x32_bf16 a[20:23], a[188:191], a[204:207], a[20:23]\n"
"v_mfma_f32_16x16x32_bf16 a[20:23], a[192:195], a[176:179], a[20:23]\n"
"buffer_load_dwordx4 v216, s[60:63], s83 offen offset:2048 sc1 lds\n"
"v_mfma_f32_16x16x32_bf16 v[156:159], a[192:195], a[172:175], v[156:159]\n"
"v_mfma_f32_16x16x32_bf16 v[156:159], a[188:191], a[220:223], v[156:159]\n"
"v_mfma_f32_16x16x32_bf16 v[160:163], a[196:199], a[220:223], v[160:163]\n"
"v_mfma_f32_16x16x32_bf16 v[160:163], a[200:203], a[172:175], v[160:163]\n"
"buffer_load_dwordx4 v216, s[60:63], s83 offen offset:3072 sc1 lds\n"
"v_mfma_f32_16x16x32_bf16 a[24:27], a[200:203], a[176:179], a[24:27]\n"
"s_addk_i32 m0, 1024\n"
"s_mul_i32 s83, s33, 20480\n"
"s_add_u32 s83, s83, s32\n"
"v_mfma_f32_16x16x32_bf16 a[24:27], a[196:199], a[204:207], a[24:27]\n"
"s_add_u32 s33, s33, 1\n"
"s_cmp_eq_u32 s33, 13\n"
"s_cselect_b32 s33, 0, s33\n"
"v_mfma_f32_16x16x32_bf16 a[60:63], a[196:199], a[208:211], a[60:63]\n"
"v_mfma_f32_16x16x32_bf16 a[60:63], a[200:203], v[20:23], a[60:63]\n"
"buffer_load_dwordx4 v216, s[60:63], s82 offen offset:3072 sc1 lds\n"
"v_mfma_f32_16x16x32_bf16 a[112:115], a[200:203], v[80:83], a[112:115]\n"
"s_addk_i32 m0, 19456\n"
"s_add_u32 s82, s83, 1024\n"
"v_mfma_f32_16x16x32_bf16 a[112:115], a[196:199], a[168:171], a[112:115]\n"
"s_waitcnt lgkmcnt(0)\n"
"s_and_b32 s3, s73, 7\n"
"s_lshl_b32 s3, s3, 5\n"
"v_mfma_f32_16x16x32_bf16 v[164:167], a[148:151], a[220:223], v[164:167]\n"
"s_barrier\n"
"ds_read_b128 a[180:183], v218 offset:20480\n"
"s_lshl_b32 s3, s3, 7\n"
"v_mfma_f32_16x16x32_bf16 v[164:167], a[152:155], a[172:175], v[164:167]\n"
"s_add_u32 s3, s3, 98304\n"
"buffer_load_dwordx4 a[64:67], v216, s[60:63], s84 offen offset:2048 sc1\n"
"v_mfma_f32_16x16x32_bf16 a[28:31], a[152:155], a[176:179], a[28:31]\n"
"s_and_b32 s93, s72, 1\n"
"ds_read_b128 a[184:187], v218 offset:25600\n"
"v_min_u32 v239, 26, v253\n"
"v_mfma_f32_16x16x32_bf16 a[28:31], a[148:151], a[204:207], a[28:31]\n"
"s_mul_i32 s94, s93, 5120\n"
"ds_read_b128 a[188:191], v218 offset:21504\n"
"v_lshlrev_b32 v239, 7, v239\n"
"v_mfma_f32_16x16x32_bf16 a[80:83], a[148:151], a[208:211], a[80:83]\n"
"s_lshl_b32 s93, s93, 10\n"
"ds_read_b128 a[192:195], v218 offset:26624\n"
"v_add_u32 v229, s94, v217\n"
"v_mfma_f32_16x16x32_bf16 a[80:83], a[152:155], v[20:23], a[80:83]\n"
"s_add_u32 s93, s93, 21364736\n"
"buffer_load_dwordx2 v[240:241], v239, s[60:63], s3 offen sc1\n"
"v_mfma_f32_16x16x32_bf16 a[116:119], a[152:155], v[80:83], a[116:119]\n"
"ds_read_b128 a[196:199], v218 offset:22528\n"
"v_add_u32 v228, s94, v216\n"
"v_mfma_f32_16x16x32_bf16 a[116:119], a[148:151], a[168:171], a[116:119]\n"
"ds_read_b128 a[200:203], v218 offset:27648\n"
"v_add_u32 v219, s94, v218\n"
"v_mfma_f32_16x16x32_bf16 a[120:123], a[156:159], a[168:171], a[120:123]\n"
"v_mfma_f32_16x16x32_bf16 a[120:123], a[160:163], v[80:83], a[120:123]\n"
"buffer_load_dwordx4 a[68:71], v216, s[60:63], s85 offen offset:2048 sc1\n"
"v_mfma_f32_16x16x32_bf16 a[84:87], a[160:163], v[20:23], a[84:87]\n"
"v_mfma_f32_16x16x32_bf16 a[84:87], a[156:159], a[208:211], a[84:87]\n"
"v_mfma_f32_16x16x32_bf16 a[48:51], a[156:159], a[204:207], a[48:51]\n"
"v_mfma_f32_16x16x32_bf16 a[48:51], a[160:163], a[176:179], a[48:51]\n"
"buffer_load_dwordx4 a[72:75], v216, s[60:63], s86 offen offset:2048 sc1\n"
"v_mfma_f32_16x16x32_bf16 v[168:171], a[160:163], a[172:175], v[168:171]\n"
"v_mfma_f32_16x16x32_bf16 v[168:171], a[156:159], a[220:223], v[168:171]\n"
"s_waitcnt vmcnt(17) lgkmcnt(3)\n"
"ds_read_b128 a[148:151], v218 offset:23552\n"
"v_mfma_f32_16x16x32_bf16 v[152:155], a[180:183], a[0:3], v[152:155]\n"
"s_waitcnt vmcnt(13) lgkmcnt(3)\n"
"buffer_load_dwordx4 a[76:79], v216, s[60:63], s87 offen offset:2048 sc1\n"
"buffer_load_dwordx4 a[96:99], v216, s[60:63], s84 offen offset:3072 sc1\n"
"v_mfma_f32_16x16x32_bf16 v[152:155], a[184:187], a[32:35], v[152:155]\n"
"s_waitcnt vmcnt(14)\n"
"buffer_load_dwordx4 a[100:103], v216, s[60:63], s85 offen offset:3072 sc1\n"
"v_mfma_f32_16x16x32_bf16 a[16:19], a[184:187], a[36:39], a[16:19]\n"
"s_add_u32 s84, s16, s92\n"
"ds_read_b128 a[152:155], v218 offset:28672\n"
"s_add_u32 s85, s17, s92\n"
"v_mfma_f32_16x16x32_bf16 a[16:19], a[180:183], a[4:7], a[16:19]\n"
"ds_read_b128 a[156:159], v218 offset:24576\n"
"v_mfma_f32_16x16x32_bf16 a[52:55], a[180:183], a[8:11], a[52:55]\n"
"s_waitcnt vmcnt(14) lgkmcnt(3)\n"
"buffer_load_dwordx4 a[104:107], v216, s[60:63], s86 offen offset:3072 sc1\n"
"v_mfma_f32_16x16x32_bf16 a[52:55], a[184:187], a[40:43], a[52:55]\n"
"s_waitcnt vmcnt(14)\n"
"buffer_load_dwordx4 a[108:111], v216, s[60:63], s87 offen offset:3072 sc1\n"
"v_mfma_f32_16x16x32_bf16 a[88:91], a[184:187], a[44:47], a[88:91]\n"
"s_add_u32 s86, s18, s92\n"
"ds_read_b128 a[160:163], v218 offset:29696\n"
"s_add_u32 s87, s19, s92\n"
"v_mfma_f32_16x16x32_bf16 a[88:91], a[180:183], a[12:15], a[88:91]\n"
"s_xor_b32 s92, s26, 0\n"
"s_mul_i32 s92, s92, 56320\n"
"s_add_u32 s35, s92, s93\n"
"v_mfma_f32_16x16x32_bf16 a[92:95], a[188:191], a[12:15], a[92:95]\n"
"s_xor_b32 s92, s26, 4\n"
"s_mul_i32 s92, s92, 56320\n"
"s_add_u32 s44, s92, s93\n"
"v_mfma_f32_16x16x32_bf16 a[92:95], a[192:195], a[44:47], a[92:95]\n"
"buffer_load_dwordx4 a[144:147], v216, s[60:63], s84 offen sc1\n"
"v_mfma_f32_16x16x32_bf16 a[56:59], a[192:195], a[40:43], a[56:59]\n"
"v_mfma_f32_16x16x32_bf16 a[56:59], a[188:191], a[8:11], a[56:59]\n"
"v_mfma_f32_16x16x32_bf16 a[20:23], a[188:191], a[4:7], a[20:23]\n"
"v_mfma_f32_16x16x32_bf16 a[20:23], a[192:195], a[36:39], a[20:23]\n"
"buffer_load_dwordx4 a[224:227], v216, s[60:63], s85 offen sc1\n"
"v_mfma_f32_16x16x32_bf16 v[156:159], a[192:195], a[32:35], v[156:159]\n"
"v_mfma_f32_16x16x32_bf16 v[156:159], a[188:191], a[0:3], v[156:159]\n"
"v_mfma_f32_16x16x32_bf16 v[160:163], a[196:199], a[0:3], v[160:163]\n"
"v_mfma_f32_16x16x32_bf16 v[160:163], a[200:203], a[32:35], v[160:163]\n"
"buffer_load_dwordx4 a[228:231], v216, s[60:63], s86 offen sc1\n"
"v_mfma_f32_16x16x32_bf16 a[24:27], a[200:203], a[36:39], a[24:27]\n"
"v_mfma_f32_16x16x32_bf16 a[24:27], a[196:199], a[4:7], a[24:27]\n"
"v_mfma_f32_16x16x32_bf16 a[60:63], a[196:199], a[8:11], a[60:63]\n"
"v_mfma_f32_16x16x32_bf16 a[60:63], a[200:203], a[40:43], a[60:63]\n"
"buffer_load_dwordx4 a[232:235], v216, s[60:63], s87 offen sc1\n"
"v_mfma_f32_16x16x32_bf16 a[112:115], a[200:203], a[44:47], a[112:115]\n"
"v_mfma_f32_16x16x32_bf16 a[112:115], a[196:199], a[12:15], a[112:115]\n"
"s_waitcnt lgkmcnt(0)\n"
"ds_read_b128 a[180:183], v218 offset:30720\n"
"v_mfma_f32_16x16x32_bf16 v[164:167], a[148:151], a[0:3], v[164:167]\n"
"ds_read_b128 a[184:187], v218 offset:35840\n"
"v_mfma_f32_16x16x32_bf16 v[164:167], a[152:155], a[32:35], v[164:167]\n"
"buffer_load_dwordx4 a[124:127], v216, s[60:63], s84 offen offset:1024 sc1\n"
"v_mfma_f32_16x16x32_bf16 a[28:31], a[152:155], a[36:39], a[28:31]\n"
"ds_read_b128 a[188:191], v218 offset:31744\n"
"v_mfma_f32_16x16x32_bf16 a[28:31], a[148:151], a[4:7], a[28:31]\n"
"ds_read_b128 a[192:195], v218 offset:36864\n"
"v_mfma_f32_16x16x32_bf16 a[80:83], a[148:151], a[8:11], a[80:83]\n"
"ds_read_b128 a[196:199], v218 offset:32768\n"
"v_mfma_f32_16x16x32_bf16 a[80:83], a[152:155], a[40:43], a[80:83]\n"
"buffer_load_dwordx4 a[236:239], v216, s[60:63], s85 offen offset:1024 sc1\n"
"v_mfma_f32_16x16x32_bf16 a[116:119], a[152:155], a[44:47], a[116:119]\n"
"ds_read_b128 a[200:203], v218 offset:37888\n"
"v_mfma_f32_16x16x32_bf16 a[116:119], a[148:151], a[12:15], a[116:119]\n"
"v_mfma_f32_16x16x32_bf16 a[120:123], a[156:159], a[12:15], a[120:123]\n"
"v_mfma_f32_16x16x32_bf16 a[120:123], a[160:163], a[44:47], a[120:123]\n"
"buffer_load_dwordx4 a[240:243], v216, s[60:63], s86 offen offset:1024 sc1\n"
"v_mfma_f32_16x16x32_bf16 a[84:87], a[160:163], a[40:43], a[84:87]\n"
"v_mfma_f32_16x16x32_bf16 a[84:87], a[156:159], a[8:11], a[84:87]\n"
"v_mfma_f32_16x16x32_bf16 a[48:51], a[156:159], a[4:7], a[48:51]\n"
"v_mfma_f32_16x16x32_bf16 a[48:51], a[160:163], a[36:39], a[48:51]\n"
"buffer_load_dwordx4 a[244:247], v216, s[60:63], s87 offen offset:1024 sc1\n"
"v_mfma_f32_16x16x32_bf16 v[168:171], a[160:163], a[32:35], v[168:171]\n"
"v_mfma_f32_16x16x32_bf16 v[168:171], a[156:159], a[0:3], v[168:171]\n"
"s_waitcnt vmcnt(16) lgkmcnt(2)\n"
"ds_read_b128 a[148:151], v218 offset:33792\n"
"v_mfma_f32_16x16x32_bf16 v[152:155], a[180:183], a[64:67], v[152:155]\n"
"s_waitcnt vmcnt(11) lgkmcnt(2)\n"
"ds_read_b128 a[152:155], v218 offset:38912\n"
"v_mfma_f32_16x16x32_bf16 v[152:155], a[184:187], a[96:99], v[152:155]\n"
"s_waitcnt vmcnt(10)\n"
"buffer_load_dwordx4 v216, s[60:63], s83 offen sc1 lds\n"
"v_mfma_f32_16x16x32_bf16 a[16:19], a[184:187], a[100:103], a[16:19]\n"
"ds_read_b128 a[156:159], v218 offset:34816\n"
"v_mfma_f32_16x16x32_bf16 a[16:19], a[180:183], a[68:71], a[16:19]\n"
"ds_read_b128 a[160:163], v218 offset:39936\n"
"v_mfma_f32_16x16x32_bf16 a[52:55], a[180:183], a[72:75], a[52:55]\n"
"s_waitcnt vmcnt(10) lgkmcnt(4)\n"
"v_mfma_f32_16x16x32_bf16 a[52:55], a[184:187], a[104:107], a[52:55]\n"
"s_waitcnt vmcnt(9)\n"
"buffer_load_dwordx4 v216, s[60:63], s83 offen offset:1024 sc1 lds\n"
"v_mfma_f32_16x16x32_bf16 a[88:91], a[184:187], a[108:111], a[88:91]\n"
"v_mfma_f32_16x16x32_bf16 a[88:91], a[180:183], a[76:79], a[88:91]\n"
"v_mfma_f32_16x16x32_bf16 a[92:95], a[188:191], a[76:79], a[92:95]\n"
"v_mfma_f32_16x16x32_bf16 a[92:95], a[192:195], a[108:111], a[92:95]\n"
"buffer_load_dwordx4 v216, s[60:63], s83 offen offset:2048 sc1 lds\n"
"v_mfma_f32_16x16x32_bf16 a[56:59], a[192:195], a[104:107], a[56:59]\n"
"v_mfma_f32_16x16x32_bf16 a[56:59], a[188:191], a[72:75], a[56:59]\n"
"v_mfma_f32_16x16x32_bf16 a[20:23], a[188:191], a[68:71], a[20:23]\n"
"v_mfma_f32_16x16x32_bf16 a[20:23], a[192:195], a[100:103], a[20:23]\n"
"buffer_load_dwordx4 v216, s[60:63], s83 offen offset:3072 sc1 lds\n"
"v_mfma_f32_16x16x32_bf16 v[156:159], a[192:195], a[96:99], v[156:159]\n"
"s_addk_i32 m0, 1024\n"
"s_mul_i32 s83, s33, 20480\n"
"s_add_u32 s83, s83, s32\n"
"v_mfma_f32_16x16x32_bf16 v[156:159], a[188:191], a[64:67], v[156:159]\n"
"s_add_u32 s33, s33, 1\n"
"s_cmp_eq_u32 s33, 13\n"
"s_cselect_b32 s33, 0, s33\n"
"v_mfma_f32_16x16x32_bf16 v[160:163], a[196:199], a[64:67], v[160:163]\n"
"v_mfma_f32_16x16x32_bf16 v[160:163], a[200:203], a[96:99], v[160:163]\n"
"buffer_load_dwordx4 v216, s[60:63], s82 offen offset:3072 sc1 lds\n"
"v_mfma_f32_16x16x32_bf16 a[24:27], a[200:203], a[100:103], a[24:27]\n"
"s_add_u32 m0, s78, 102400\n"
"s_add_u32 s82, s83, 1024\n"
"v_mfma_f32_16x16x32_bf16 a[24:27], a[196:199], a[68:71], a[24:27]\n"
"v_mfma_f32_16x16x32_bf16 a[60:63], a[196:199], a[72:75], a[60:63]\n"
"v_mfma_f32_16x16x32_bf16 a[60:63], a[200:203], a[104:107], a[60:63]\n"
"buffer_load_dwordx4 a[248:251], v216, s[60:63], s84 offen offset:2048 sc1\n"
"v_mfma_f32_16x16x32_bf16 a[112:115], a[200:203], a[108:111], a[112:115]\n"
"v_mfma_f32_16x16x32_bf16 a[112:115], a[196:199], a[76:79], a[112:115]\n"
"s_waitcnt lgkmcnt(0)\n"
"s_barrier\n"
"ds_read_b128 a[180:183], v217 offset:20480\n"
"v_mfma_f32_16x16x32_bf16 v[164:167], a[148:151], a[64:67], v[164:167]\n"
"ds_read_b128 a[184:187], v217 offset:25600\n"
"v_mfma_f32_16x16x32_bf16 v[164:167], a[152:155], a[96:99], v[164:167]\n"
"buffer_load_dwordx4 a[252:255], v216, s[60:63], s85 offen offset:2048 sc1\n"
"v_mfma_f32_16x16x32_bf16 a[28:31], a[152:155], a[100:103], a[28:31]\n"
"ds_read_b128 a[188:191], v217 offset:21504\n"
"v_mfma_f32_16x16x32_bf16 a[28:31], a[148:151], a[68:71], a[28:31]\n"
"ds_read_b128 a[192:195], v217 offset:26624\n"
"v_mfma_f32_16x16x32_bf16 a[80:83], a[148:151], a[72:75], a[80:83]\n"
"ds_read_b128 a[196:199], v217 offset:22528\n"
"v_mfma_f32_16x16x32_bf16 a[80:83], a[152:155], a[104:107], a[80:83]\n"
"buffer_load_dwordx4 v[84:87], v216, s[60:63], s86 offen offset:2048 sc1\n"
"v_mfma_f32_16x16x32_bf16 a[116:119], a[152:155], a[108:111], a[116:119]\n"
"ds_read_b128 a[200:203], v217 offset:27648\n"
"v_mfma_f32_16x16x32_bf16 a[116:119], a[148:151], a[76:79], a[116:119]\n"
"v_mfma_f32_16x16x32_bf16 a[120:123], a[156:159], a[76:79], a[120:123]\n"
"v_mfma_f32_16x16x32_bf16 a[120:123], a[160:163], a[108:111], a[120:123]\n"
"buffer_load_dwordx4 v[24:27], v216, s[60:63], s87 offen offset:2048 sc1\n"
"v_mfma_f32_16x16x32_bf16 a[84:87], a[160:163], a[104:107], a[84:87]\n"
"v_mfma_f32_16x16x32_bf16 a[84:87], a[156:159], a[72:75], a[84:87]\n"
"v_mfma_f32_16x16x32_bf16 a[48:51], a[156:159], a[68:71], a[48:51]\n"
"v_mfma_f32_16x16x32_bf16 a[48:51], a[160:163], a[100:103], a[48:51]\n"
"buffer_load_dwordx4 v[28:31], v216, s[60:63], s84 offen offset:3072 sc1\n"
"v_mfma_f32_16x16x32_bf16 v[168:171], a[160:163], a[96:99], v[168:171]\n"
"v_mfma_f32_16x16x32_bf16 v[168:171], a[156:159], a[64:67], v[168:171]\n"
"s_waitcnt vmcnt(17) lgkmcnt(2)\n"
"v_cmp_eq_u64_e64 s[4:5], v[240:241], s[80:81]\n"
"s_nop 1\n"
"s_cmp_eq_u64 s[4:5], -1\n"
"s_cbranch_scc0 L_spinH_%=\n"
"L_retH_%=:\n"
"v_mfma_f32_16x16x32_bf16 v[152:155], a[180:183], a[144:147], v[152:155]\n"
"s_waitcnt vmcnt(13) lgkmcnt(1)\n"
"buffer_load_dwordx4 v[88:91], v216, s[60:63], s85 offen offset:3072 sc1\n"
"v_mfma_f32_16x16x32_bf16 v[152:155], a[184:187], a[124:127], v[152:155]\n"
"s_waitcnt vmcnt(13)\n"
"ds_read_b128 a[148:151], v217 offset:23552\n"
"s_add_u32 s8, s35, s34\n"
"v_mfma_f32_16x16x32_bf16 a[16:19], a[184:187], a[236:239], a[16:19]\n"
"s_add_u32 s9, s44, s34\n"
"ds_read_b128 a[152:155], v217 offset:28672\n"
"s_addk_i32 s34, 4096\n"
"v_mfma_f32_16x16x32_bf16 a[16:19], a[180:183], a[224:227], a[16:19]\n"
"s_cmp_eq_u32 s34, 53248\n"
"s_cselect_b32 s34, 0, s34\n"
"ds_read_b128 a[156:159], v217 offset:24576\n"
"v_mfma_f32_16x16x32_bf16 a[52:55], a[180:183], a[228:231], a[52:55]\n"
"s_waitcnt vmcnt(12) lgkmcnt(3)\n"
"buffer_load_dwordx4 v[92:95], v216, s[60:63], s86 offen offset:3072 sc1\n"
"v_mfma_f32_16x16x32_bf16 a[52:55], a[184:187], a[240:243], a[52:55]\n"
"s_waitcnt vmcnt(12)\n"
"ds_read_b128 a[160:163], v217 offset:29696\n"
"v_mfma_f32_16x16x32_bf16 a[88:91], a[184:187], a[244:247], a[88:91]\n"
"v_mfma_f32_16x16x32_bf16 a[88:91], a[180:183], a[232:235], a[88:91]\n"
"v_mfma_f32_16x16x32_bf16 a[92:95], a[188:191], a[232:235], a[92:95]\n"
"buffer_load_dwordx4 v[32:35], v216, s[60:63], s87 offen offset:3072 sc1\n"
"v_mfma_f32_16x16x32_bf16 a[92:95], a[192:195], a[244:247], a[92:95]\n"
"v_mfma_f32_16x16x32_bf16 a[56:59], a[192:195], a[240:243], a[56:59]\n"
"v_mfma_f32_16x16x32_bf16 a[56:59], a[188:191], a[228:231], a[56:59]\n"
"v_mfma_f32_16x16x32_bf16 a[20:23], a[188:191], a[224:227], a[20:23]\n"
"buffer_load_dwordx4 v216, s[60:63], s83 offen sc1 lds\n"
"v_mfma_f32_16x16x32_bf16 a[20:23], a[192:195], a[236:239], a[20:23]\n"
"v_mfma_f32_16x16x32_bf16 v[156:159], a[192:195], a[124:127], v[156:159]\n"
"v_mfma_f32_16x16x32_bf16 v[156:159], a[188:191], a[144:147], v[156:159]\n"
"v_mfma_f32_16x16x32_bf16 v[160:163], a[196:199], a[144:147], v[160:163]\n"
"buffer_load_dwordx4 v216, s[60:63], s83 offen offset:1024 sc1 lds\n"
"v_mfma_f32_16x16x32_bf16 v[160:163], a[200:203], a[124:127], v[160:163]\n"
"v_mfma_f32_16x16x32_bf16 a[24:27], a[200:203], a[236:239], a[24:27]\n"
"v_mfma_f32_16x16x32_bf16 a[24:27], a[196:199], a[224:227], a[24:27]\n"
"v_mfma_f32_16x16x32_bf16 a[60:63], a[196:199], a[228:231], a[60:63]\n"
"buffer_load_dwordx4 v216, s[60:63], s83 offen offset:2048 sc1 lds\n"
"v_mfma_f32_16x16x32_bf16 a[60:63], a[200:203], a[240:243], a[60:63]\n"
"v_mfma_f32_16x16x32_bf16 a[112:115], a[200:203], a[244:247], a[112:115]\n"
"v_mfma_f32_16x16x32_bf16 a[112:115], a[196:199], a[232:235], a[112:115]\n"
"s_waitcnt lgkmcnt(0)\n"
"ds_read_b128 a[180:183], v217 offset:30720\n"
"v_mfma_f32_16x16x32_bf16 v[164:167], a[148:151], a[144:147], v[164:167]\n"
"buffer_load_dwordx4 v216, s[60:63], s83 offen offset:3072 sc1 lds\n"
"v_mfma_f32_16x16x32_bf16 v[164:167], a[152:155], a[124:127], v[164:167]\n"
"s_addk_i32 m0, 1024\n"
"ds_read_b128 a[184:187], v217 offset:35840\n"
"s_mul_i32 s83, s33, 20480\n"
"v_mfma_f32_16x16x32_bf16 a[28:31], a[152:155], a[236:239], a[28:31]\n"
"s_add_u32 s83, s83, s32\n"
"ds_read_b128 a[188:191], v217 offset:31744\n"
"s_add_u32 s33, s33, 1\n"
"v_mfma_f32_16x16x32_bf16 a[28:31], a[148:151], a[224:227], a[28:31]\n"
"s_cmp_eq_u32 s33, 13\n"
"s_cselect_b32 s33, 0, s33\n"
"ds_read_b128 a[192:195], v217 offset:36864\n"
"v_mfma_f32_16x16x32_bf16 a[80:83], a[148:151], a[228:231], a[80:83]\n"
"buffer_load_dwordx4 v216, s[60:63], s82 offen offset:3072 sc1 lds\n"
"v_mfma_f32_16x16x32_bf16 a[80:83], a[152:155], a[240:243], a[80:83]\n"
"s_addk_i32 m0, 19456\n"
"ds_read_b128 a[196:199], v217 offset:32768\n"
"s_add_u32 s82, s83, 1024\n"
"v_mfma_f32_16x16x32_bf16 a[116:119], a[152:155], a[244:247], a[116:119]\n"
"ds_read_b128 a[200:203], v217 offset:37888\n"
"v_mfma_f32_16x16x32_bf16 a[116:119], a[148:151], a[232:235], a[116:119]\n"
"v_mfma_f32_16x16x32_bf16 a[120:123], a[156:159], a[232:235], a[120:123]\n"
"buffer_load_dwordx4 v216, s[60:63], s83 offen sc1 lds\n"
"v_mfma_f32_16x16x32_bf16 a[120:123], a[160:163], a[244:247], a[120:123]\n"
"v_mfma_f32_16x16x32_bf16 a[84:87], a[160:163], a[240:243], a[84:87]\n"
"v_mfma_f32_16x16x32_bf16 a[84:87], a[156:159], a[228:231], a[84:87]\n"
"v_mfma_f32_16x16x32_bf16 a[48:51], a[156:159], a[224:227], a[48:51]\n"
"buffer_load_dwordx4 v216, s[60:63], s83 offen offset:1024 sc1 lds\n"
"v_mfma_f32_16x16x32_bf16 a[48:51], a[160:163], a[236:239], a[48:51]\n"
"v_mfma_f32_16x16x32_bf16 v[168:171], a[160:163], a[124:127], v[168:171]\n"
"buffer_load_dwordx4 v216, s[60:63], s83 offen offset:2048 sc1 lds\n"
"buffer_load_dwordx4 v216, s[60:63], s83 offen offset:3072 sc1 lds\n"
"s_addk_i32 m0, 1024\n"
"v_mfma_f32_16x16x32_bf16 v[168:171], a[156:159], a[144:147], v[168:171]\n"
"s_waitcnt vmcnt(15) lgkmcnt(2)\n"
"buffer_load_dwordx4 v216, s[60:63], s82 offen offset:3072 sc1 lds\n"
"buffer_load_dwordx4 a[128:131], v216, s[60:63], s8 offen sc1\n"
"v_mfma_f32_16x16x32_bf16 a[16:19], a[180:183], a[252:255], a[16:19]\n"
"s_waitcnt vmcnt(13)\n"
"ds_read_b128 a[148:151], v217 offset:33792\n"
"v_mfma_f32_16x16x32_bf16 a[16:19], a[184:187], v[88:91], a[16:19]\n"
"ds_read_b128 a[152:155], v217 offset:38912\n"
"v_mfma_f32_16x16x32_bf16 a[20:23], a[188:191], a[252:255], a[20:23]\n"
"ds_read_b128 a[156:159], v217 offset:34816\n"
"v_mfma_f32_16x16x32_bf16 a[20:23], a[192:195], v[88:91], a[20:23]\n"
"s_waitcnt lgkmcnt(3)\n"
"buffer_load_dwordx4 a[132:135], v216, s[60:63], s9 offen sc1\n"
"v_mfma_f32_16x16x32_bf16 a[24:27], a[196:199], a[252:255], a[24:27]\n"
"ds_read_b128 a[160:163], v217 offset:39936\n"
"v_mfma_f32_16x16x32_bf16 a[24:27], a[200:203], v[88:91], a[24:27]\n"
"v_mfma_f32_16x16x32_bf16 a[52:55], a[180:183], v[84:87], a[52:55]\n"
"s_waitcnt vmcnt(13)\n"
"v_mfma_f32_16x16x32_bf16 a[52:55], a[184:187], v[92:95], a[52:55]\n"
"buffer_load_dwordx4 a[136:139], v216, s[60:63], s8 offen offset:2048 sc1\n"
"v_mfma_f32_16x16x32_bf16 a[56:59], a[188:191], v[84:87], a[56:59]\n"
"s_add_u32 s8, s35, s34\n"
"v_mfma_f32_16x16x32_bf16 a[56:59], a[192:195], v[92:95], a[56:59]\n"
"v_mfma_f32_16x16x32_bf16 a[60:63], a[196:199], v[84:87], a[60:63]\n"
"v_mfma_f32_16x16x32_bf16 a[60:63], a[200:203], v[92:95], a[60:63]\n"
"buffer_load_dwordx4 a[140:143], v216, s[60:63], s9 offen offset:2048 sc1\n"
"v_mfma_f32_16x16x32_bf16 a[88:91], a[180:183], v[24:27], a[88:91]\n"
"s_waitcnt vmcnt(14) lgkmcnt(3)\n"
"s_add_u32 s9, s44, s34\n"
"s_addk_i32 s34, 4096\n"
"v_mfma_f32_16x16x32_bf16 a[88:91], a[184:187], v[32:35], a[88:91]\n"
"s_cmp_eq_u32 s34, 53248\n"
"s_cselect_b32 s34, 0, s34\n"
"v_mfma_f32_16x16x32_bf16 a[92:95], a[188:191], v[24:27], a[92:95]\n"
"v_mfma_f32_16x16x32_bf16 a[92:95], a[192:195], v[32:35], a[92:95]\n"
"buffer_load_dwordx4 a[212:215], v216, s[60:63], s8 offen sc1\n"
"v_mfma_f32_16x16x32_bf16 a[112:115], a[196:199], v[24:27], a[112:115]\n"
"v_mfma_f32_16x16x32_bf16 a[112:115], a[200:203], v[32:35], a[112:115]\n"
"v_mfma_f32_16x16x32_bf16 v[152:155], a[180:183], a[248:251], v[152:155]\n"
"v_mfma_f32_16x16x32_bf16 v[152:155], a[184:187], v[28:31], v[152:155]\n"
"buffer_load_dwordx4 a[216:219], v216, s[60:63], s9 offen sc1\n"
"v_mfma_f32_16x16x32_bf16 v[156:159], a[188:191], a[248:251], v[156:159]\n"
"v_mfma_f32_16x16x32_bf16 v[156:159], a[192:195], v[28:31], v[156:159]\n"
"v_mfma_f32_16x16x32_bf16 v[160:163], a[196:199], a[248:251], v[160:163]\n"
"v_mfma_f32_16x16x32_bf16 v[160:163], a[200:203], v[28:31], v[160:163]\n"
"s_waitcnt lgkmcnt(0)\n"
"buffer_load_dwordx4 a[220:223], v216, s[60:63], s8 offen offset:2048 sc1\n"
"v_mfma_f32_16x16x32_bf16 a[28:31], a[148:151], a[252:255], a[28:31]\n"
"s_barrier\n"
"ds_read_b128 a[180:183], v229 offset:0\n"
"s_add_u32 s8, s35, s34\n"
"v_mfma_f32_16x16x32_bf16 a[28:31], a[152:155], v[88:91], a[28:31]\n"
"s_addk_i32 m0, 19456\n"
"ds_read_b128 a[184:187], v229 offset:1024\n"
"s_mul_i32 s83, s33, 20480\n"
"v_mfma_f32_16x16x32_bf16 a[48:51], a[156:159], a[252:255], a[48:51]\n"
"s_add_u32 s83, s83, s32\n"
"ds_read_b128 a[188:191], v229 offset:2048\n"
"s_add_u32 s82, s83, 1024\n"
"v_mfma_f32_16x16x32_bf16 a[48:51], a[160:163], v[88:91], a[48:51]\n"
"s_add_u32 s33, s33, 1\n"
"buffer_load_dwordx4 a[204:207], v216, s[60:63], s9 offen offset:2048 sc1\n"
"v_mfma_f32_16x16x32_bf16 a[80:83], a[148:151], v[84:87], a[80:83]\n"
"s_add_u32 s9, s44, s34\n"
"ds_read_b128 a[192:195], v229 offset:3072\n"
"s_addk_i32 s34, 4096\n"
"v_mfma_f32_16x16x32_bf16 a[80:83], a[152:155], v[92:95], a[80:83]\n"
"s_cmp_eq_u32 s34, 53248\n"
"s_cselect_b32 s34, 0, s34\n"
"ds_read_b128 a[196:199], v229 offset:4096\n"
"s_cmp_eq_u32 s33, 13\n"
"s_cselect_b32 s33, 0, s33\n"
"v_mfma_f32_16x16x32_bf16 a[84:87], a[156:159], v[84:87], a[84:87]\n"
"v_mfma_f32_16x16x32_bf16 a[84:87], a[160:163], v[92:95], a[84:87]\n"
"buffer_load_dwordx4 a[208:211], v216, s[60:63], s8 offen sc1\n"
"v_mfma_f32_16x16x32_bf16 a[116:119], a[148:151], v[24:27], a[116:119]\n"
"v_mfma_f32_16x16x32_bf16 a[116:119], a[152:155], v[32:35], a[116:119]\n"
"v_mfma_f32_16x16x32_bf16 a[120:123], a[156:159], v[24:27], a[120:123]\n"
"v_mfma_f32_16x16x32_bf16 a[120:123], a[160:163], v[32:35], a[120:123]\n"
"buffer_load_dwordx4 v216, s[60:63], s83 offen sc1 lds\n"
"v_mfma_f32_16x16x32_bf16 v[164:167], a[148:151], a[248:251], v[164:167]\n"
"v_mfma_f32_16x16x32_bf16 v[164:167], a[152:155], v[28:31], v[164:167]\n"
"v_mfma_f32_16x16x32_bf16 v[168:171], a[156:159], a[248:251], v[168:171]\n"
"v_mfma_f32_16x16x32_bf16 v[168:171], a[160:163], v[28:31], v[168:171]\n"
"s_waitcnt vmcnt(9) lgkmcnt(2)\n"
"ds_read_b128 a[148:151], v229 offset:10240\n"
"s_xor_b32 s92, s73, 1\n"
"s_lshl_b32 s92, s92, 2\n"
"s_add_u32 s92, s92, s72\n"
"v_mfma_f32_16x16x32_bf16 v[36:39], a[180:183], a[128:131], 0\n"
"s_mul_i32 s92, s92, 7680\n"
"ds_read_b128 a[152:155], v229 offset:11264\n"
"s_add_u32 s89, s92, 36044800\n"
"v_accvgpr_read_b32 v220, a16\n"
"v_mfma_f32_16x16x32_bf16 v[96:99], a[184:187], a[128:131], 0\n"
"ds_read_b128 a[156:159], v229 offset:12288\n"
"ds_read_b128 a[160:163], v229 offset:13312\n"
"ds_read_b128 a[164:167], v229 offset:14336\n"
"v_accvgpr_read_b32 v221, a17\n"
"v_accvgpr_read_b32 v222, a18\n"
"v_accvgpr_read_b32 v223, a19\n"
"v_mfma_f32_16x16x32_bf16 v[100:103], a[188:191], a[128:131], 0\n"
"s_waitcnt lgkmcnt(5)\n"
"buffer_load_dwordx4 v216, s[60:63], s83 offen offset:1024 sc1 lds\n"
"v_cvt_pk_bf16_f32 v212, v220, v221\n"
"v_cvt_pk_bf16_f32 v213, v222, v223\n"
"v_accvgpr_read_b32 v224, a20\n"
"v_mfma_f32_16x16x32_bf16 v[40:43], a[192:195], a[128:131], 0\n"
"buffer_load_dwordx4 v216, s[60:63], s83 offen offset:2048 sc1 lds\n"
"buffer_load_dwordx4 v216, s[60:63], s83 offen offset:3072 sc1 lds\n"
"s_addk_i32 m0, 1024\n"
"v_accvgpr_read_b32 v225, a21\n"
"v_accvgpr_read_b32 v226, a22\n"
"v_accvgpr_read_b32 v227, a23\n"
"v_mfma_f32_16x16x32_bf16 v[44:47], a[196:199], a[128:131], 0\n"
"s_waitcnt vmcnt(11)\n"
"buffer_load_dwordx4 v216, s[60:63], s82 offen offset:3072 sc1 lds\n"
"buffer_load_dwordx4 a[168:171], v216, s[60:63], s9 offen sc1\n"
"v_cvt_pk_bf16_f32 v214, v224, v225\n"
"v_cvt_pk_bf16_f32 v215, v226, v227\n"
"buffer_store_dwordx4 v[212:215], v216, s[60:63], s89 offen sc1\n"
"v_mfma_f32_16x16x32_bf16 a[0:3], a[180:183], a[132:135], 0\n"
"s_add_u32 m0, s78, 81920\n"
"v_accvgpr_read_b32 v220, a24\n"
"v_accvgpr_read_b32 v221, a25\n"
"v_accvgpr_read_b32 v222, a26\n"
"v_accvgpr_read_b32 v223, a27\n"
"v_mfma_f32_16x16x32_bf16 a[4:7], a[184:187], a[132:135], 0\n"
"s_mul_i32 s83, s33, 20480\n"
"v_cvt_pk_bf16_f32 v248, v220, v221\n"
"v_cvt_pk_bf16_f32 v249, v222, v223\n"
"v_accvgpr_read_b32 v224, a28\n"
"v_mfma_f32_16x16x32_bf16 a[8:11], a[188:191], a[132:135], 0\n"
"s_add_u32 s83, s83, s32\n"
"buffer_load_dwordx4 a[172:175], v216, s[60:63], s8 offen offset:2048 sc1\n"
"v_accvgpr_read_b32 v225, a29\n"
"v_accvgpr_read_b32 v226, a30\n"
"v_accvgpr_read_b32 v227, a31\n"
"v_mfma_f32_16x16x32_bf16 a[12:15], a[192:195], a[132:135], 0\n"
"s_add_u32 s8, s35, s34\n"
"v_cvt_pk_bf16_f32 v250, v224, v225\n"
"v_cvt_pk_bf16_f32 v251, v226, v227\n"
"buffer_store_dwordx4 v[248:251], v216, s[60:63], s89 offen offset:1024 sc1\n"
"v_mfma_f32_16x16x32_bf16 a[32:35], a[196:199], a[132:135], 0\n"
"s_waitcnt vmcnt(15) lgkmcnt(4)\n"
"v_accvgpr_read_b32 v220, a48\n"
"v_accvgpr_read_b32 v221, a49\n"
"v_accvgpr_read_b32 v222, a50\n"
"s_xor_b32 s92, s73, 2\n"
"s_lshl_b32 s92, s92, 2\n"
"s_add_u32 s92, s92, s72\n"
"v_mfma_f32_16x16x32_bf16 v[36:39], a[148:151], a[136:139], v[36:39]\n"
"s_waitcnt lgkmcnt(0)\n"
"s_barrier\n"
"ds_read_b128 a[36:39], v228 offset:40960\n"
"v_accvgpr_read_b32 v223, a51\n"
"v_cvt_pk_bf16_f32 v212, v220, v221\n"
"v_cvt_pk_bf16_f32 v213, v222, v223\n"
"s_mul_i32 s92, s92, 7680\n"
"s_add_u32 s90, s92, 36047360\n"
"v_accvgpr_read_b32 v220, a52\n"
"v_mfma_f32_16x16x32_bf16 v[96:99], a[152:155], a[136:139], v[96:99]\n"
"s_add_u32 s82, s83, 1024\n"
"ds_read_b128 a[40:43], v228 offset:41984\n"
"buffer_store_dwordx2 v[212:213], v238, s[60:63], s89 offen offset:2048 sc1\n"
"v_accvgpr_read_b32 v221, a53\n"
"v_accvgpr_read_b32 v222, a54\n"
"v_accvgpr_read_b32 v223, a55\n"
"v_mfma_f32_16x16x32_bf16 v[100:103], a[156:159], a[136:139], v[100:103]\n"
"s_add_u32 s33, s33, 1\n"
"ds_read_b128 a[44:47], v228 offset:43008\n"
"v_cvt_pk_bf16_f32 v248, v220, v221\n"
"v_cvt_pk_bf16_f32 v249, v222, v223\n"
"v_accvgpr_read_b32 v224, a56\n"
"v_mfma_f32_16x16x32_bf16 v[40:43], a[160:163], a[136:139], v[40:43]\n"
"s_cmp_eq_u32 s33, 13\n"
"s_cselect_b32 s33, 0, s33\n"
"ds_read_b128 a[64:67], v228 offset:44032\n"
"v_accvgpr_read_b32 v225, a57\n"
"v_accvgpr_read_b32 v226, a58\n"
"v_accvgpr_read_b32 v227, a59\n"
"v_mfma_f32_16x16x32_bf16 v[44:47], a[164:167], a[136:139], v[44:47]\n"
"s_waitcnt vmcnt(15)\n"
"buffer_load_dwordx4 a[176:179], v216, s[60:63], s9 offen offset:2048 sc1\n"
"ds_read_b128 a[68:71], v228 offset:45056\n"
"v_cvt_pk_bf16_f32 v250, v224, v225\n"
"v_cvt_pk_bf16_f32 v251, v226, v227\n"
"buffer_store_dwordx4 v[248:251], v216, s[60:63], s90 offen sc1\n"
"v_mfma_f32_16x16x32_bf16 a[0:3], a[148:151], a[140:143], a[0:3]\n"
"s_add_u32 s9, s44, s34\n"
"ds_read_b128 a[180:183], v228 offset:51200\n"
"v_accvgpr_read_b32 v220, a60\n"
"v_accvgpr_read_b32 v221, a61\n"
"v_accvgpr_read_b32 v222, a62\n"
"v_accvgpr_read_b32 v223, a63\n"
"v_mfma_f32_16x16x32_bf16 a[4:7], a[152:155], a[140:143], a[4:7]\n"
"s_addk_i32 s34, 4096\n"
"ds_read_b128 a[184:187], v228 offset:52224\n"
"v_cvt_pk_bf16_f32 v212, v220, v221\n"
"v_cvt_pk_bf16_f32 v213, v222, v223\n"
"v_accvgpr_read_b32 v224, a80\n"
"v_mfma_f32_16x16x32_bf16 a[8:11], a[156:159], a[140:143], a[8:11]\n"
"s_cmp_eq_u32 s34, 53248\n"
"s_cselect_b32 s34, 0, s34\n"
"buffer_load_dwordx4 a[72:75], v216, s[60:63], s8 offen sc1\n"
"v_accvgpr_read_b32 v225, a81\n"
"v_accvgpr_read_b32 v226, a82\n"
"v_accvgpr_read_b32 v227, a83\n"
"v_mfma_f32_16x16x32_bf16 a[12:15], a[160:163], a[140:143], a[12:15]\n"
"ds_read_b128 a[188:191], v228 offset:53248\n"
"v_cvt_pk_bf16_f32 v214, v224, v225\n"
"v_cvt_pk_bf16_f32 v215, v226, v227\n"
"buffer_store_dwordx4 v[212:215], v216, s[60:63], s90 offen offset:1024 sc1\n"
"v_mfma_f32_16x16x32_bf16 a[32:35], a[164:167], a[140:143], a[32:35]\n"
"s_waitcnt vmcnt(18) lgkmcnt(7)\n"
"ds_read_b128 a[192:195], v228 offset:54272\n"
"v_accvgpr_read_b32 v220, a84\n"
"v_accvgpr_read_b32 v221, a85\n"
"v_accvgpr_read_b32 v222, a86\n"
"s_xor_b32 s92, s73, 3\n"
"s_lshl_b32 s92, s92, 2\n"
"s_add_u32 s92, s92, s72\n"
"v_mfma_f32_16x16x32_bf16 v[36:39], a[36:39], a[212:215], v[36:39]\n"
"s_waitcnt lgkmcnt(7)\n"
"ds_read_b128 a[196:199], v228 offset:55296\n"
"v_accvgpr_read_b32 v223, a87\n"
"v_cvt_pk_bf16_f32 v248, v220, v221\n"
"v_cvt_pk_bf16_f32 v249, v222, v223\n"
"s_mul_i32 s92, s92, 7680\n"
"s_add_u32 s91, s92, 36049920\n"
"v_accvgpr_read_b32 v220, a88\n"
"v_mfma_f32_16x16x32_bf16 v[96:99], a[40:43], a[212:215], v[96:99]\n"
"s_waitcnt lgkmcnt(7)\n"
"buffer_store_dwordx2 v[248:249], v238, s[60:63], s90 offen offset:2048 sc1\n"
"v_accvgpr_read_b32 v221, a89\n"
"v_accvgpr_read_b32 v222, a90\n"
"v_accvgpr_read_b32 v223, a91\n"
"v_cvt_pk_bf16_f32 v212, v220, v221\n"
"v_cvt_pk_bf16_f32 v213, v222, v223\n"
"v_accvgpr_read_b32 v224, a92\n"
"v_accvgpr_read_b32 v225, a93\n"
"v_accvgpr_read_b32 v226, a94\n"
"v_accvgpr_read_b32 v227, a95\n"
"v_cvt_pk_bf16_f32 v214, v224, v225\n"
"v_cvt_pk_bf16_f32 v215, v226, v227\n"
"buffer_store_dwordx4 v[212:215], v216, s[60:63], s91 offen sc1\n"
"v_accvgpr_read_b32 v220, a112\n"
"v_accvgpr_read_b32 v221, a113\n"
"v_accvgpr_read_b32 v222, a114\n"
"v_accvgpr_read_b32 v223, a115\n"
"v_cvt_pk_bf16_f32 v248, v220, v221\n"
"v_cvt_pk_bf16_f32 v249, v222, v223\n"
"v_accvgpr_read_b32 v224, a116\n"
"v_accvgpr_read_b32 v225, a117\n"
"v_accvgpr_read_b32 v226, a118\n"
"v_accvgpr_read_b32 v227, a119\n"
"v_cvt_pk_bf16_f32 v250, v224, v225\n"
"v_cvt_pk_bf16_f32 v251, v226, v227\n"
"buffer_store_dwordx4 v[248:251], v216, s[60:63], s91 offen offset:1024 sc1\n"
"v_accvgpr_read_b32 v220, a120\n"
"v_accvgpr_read_b32 v221, a121\n"
"v_accvgpr_read_b32 v222, a122\n"
"v_accvgpr_read_b32 v223, a123\n"
"v_cvt_pk_bf16_f32 v212, v220, v221\n"
"v_cvt_pk_bf16_f32 v213, v222, v223\n"
"buffer_store_dwordx2 v[212:213], v238, s[60:63], s91 offen offset:2048 sc1\n"
"v_mfma_f32_16x16x32_bf16 v[100:103], a[44:47], a[212:215], v[100:103]\n"
"s_waitcnt lgkmcnt(6)\n"
"v_mfma_f32_16x16x32_bf16 v[40:43], a[64:67], a[212:215], v[40:43]\n"
"s_waitcnt lgkmcnt(5)\n"
"v_mfma_f32_16x16x32_bf16 v[44:47], a[68:71], a[212:215], v[44:47]\n"
"s_waitcnt vmcnt(21)\n"
"buffer_load_dwordx4 a[76:79], v216, s[60:63], s9 offen sc1\n"
"v_mfma_f32_16x16x32_bf16 a[0:3], a[36:39], a[216:219], a[0:3]\n"
"v_mfma_f32_16x16x32_bf16 a[4:7], a[40:43], a[216:219], a[4:7]\n"
"v_mfma_f32_16x16x32_bf16 a[8:11], a[44:47], a[216:219], a[8:11]\n"
"buffer_load_dwordx4 a[96:99], v216, s[60:63], s8 offen offset:2048 sc1\n"
"v_mfma_f32_16x16x32_bf16 a[12:15], a[64:67], a[216:219], a[12:15]\n"
"s_add_u32 s8, s35, s34\n"
"v_mfma_f32_16x16x32_bf16 a[32:35], a[68:71], a[216:219], a[32:35]\n"
"s_waitcnt vmcnt(22) lgkmcnt(0)\n"
"s_barrier\n"
"ds_read_b128 a[148:151], v228 offset:0\n"
"v_mfma_f32_16x16x32_bf16 v[36:39], a[180:183], a[220:223], v[36:39]\n"
"s_mov_b32 s99, s73\n"
"ds_read_b128 a[152:155], v228 offset:1024\n"
"s_lshl_b32 s93, s72, 8\n"
"v_mfma_f32_16x16x32_bf16 v[96:99], a[184:187], a[220:223], v[96:99]\n"
"s_add_u32 s99, s99, s93\n"
"ds_read_b128 a[156:159], v228 offset:2048\n"
"s_lshl_b32 s99, s99, 7\n"
"v_mfma_f32_16x16x32_bf16 v[100:103], a[188:191], a[220:223], v[100:103]\n"
"s_add_u32 s99, s99, 1048576\n"
"ds_read_b128 a[160:163], v228 offset:3072\n"
"v_cmp_ne_u32 vcc, 0, v253\n"
"v_mfma_f32_16x16x32_bf16 v[40:43], a[192:195], a[220:223], v[40:43]\n"
"buffer_load_dwordx4 a[100:103], v216, s[60:63], s9 offen offset:2048 sc1\n"
"v_mfma_f32_16x16x32_bf16 v[44:47], a[196:199], a[220:223], v[44:47]\n"
"s_waitcnt vmcnt(22)\n"
"ds_read_b128 a[164:167], v228 offset:4096\n"
"s_add_u32 s9, s44, s34\n"
"v_mfma_f32_16x16x32_bf16 a[0:3], a[180:183], a[204:207], a[0:3]\n"
"s_addk_i32 s34, 4096\n"
"ds_read_b128 a[36:39], v228 offset:10240\n"
"s_cmp_eq_u32 s34, 53248\n"
"s_cselect_b32 s34, 0, s34\n"
"v_mfma_f32_16x16x32_bf16 a[4:7], a[184:187], a[204:207], a[4:7]\n"
"buffer_load_dwordx4 v216, s[60:63], s83 offen sc1 lds\n"
"v_mfma_f32_16x16x32_bf16 a[8:11], a[188:191], a[204:207], a[8:11]\n"
"ds_read_b128 a[40:43], v228 offset:11264\n"
"v_mov_b32 v246, 0x40000000\n"
"v_mfma_f32_16x16x32_bf16 a[12:15], a[192:195], a[204:207], a[12:15]\n"
"buffer_load_dwordx4 v216, s[60:63], s83 offen offset:1024 sc1 lds\n"
"buffer_load_dwordx4 v216, s[60:63], s83 offen offset:2048 sc1 lds\n"
"buffer_load_dwordx4 v216, s[60:63], s83 offen offset:3072 sc1 lds\n"
"s_addk_i32 m0, 1024\n"
"v_mfma_f32_16x16x32_bf16 a[32:35], a[196:199], a[204:207], a[32:35]\n"
"s_waitcnt vmcnt(25) lgkmcnt(6)\n"
"buffer_load_dwordx4 v216, s[60:63], s82 offen offset:3072 sc1 lds\n"
"buffer_load_dwordx4 a[104:107], v216, s[60:63], s8 offen sc1\n"
"v_mfma_f32_16x16x32_bf16 v[36:39], a[148:151], a[208:211], v[36:39]\n"
"s_waitcnt lgkmcnt(5)\n"
"ds_read_b128 a[44:47], v228 offset:12288\n"
"v_cndmask_b32 v246, 0, v246, vcc\n"
"v_mfma_f32_16x16x32_bf16 v[96:99], a[152:155], a[208:211], v[96:99]\n"
"s_waitcnt lgkmcnt(5)\n"
"ds_read_b128 a[64:67], v228 offset:13312\n"
"ds_read_b128 a[68:71], v228 offset:14336\n"
"v_mfma_f32_16x16x32_bf16 v[100:103], a[156:159], a[208:211], v[100:103]\n"
"s_waitcnt lgkmcnt(6)\n"
"buffer_load_dwordx4 a[108:111], v216, s[60:63], s9 offen sc1\n"
"v_mfma_f32_16x16x32_bf16 v[40:43], a[160:163], a[208:211], v[40:43]\n"
"s_waitcnt lgkmcnt(5)\n"
"s_addk_i32 m0, 44032\n"
"s_mul_i32 s83, s33, 20480\n"
"v_mfma_f32_16x16x32_bf16 v[44:47], a[164:167], a[208:211], v[44:47]\n"
"s_waitcnt vmcnt(22)\n"
"s_add_u32 s83, s83, s32\n"
"s_add_u32 s82, s83, 1024\n"
"v_mfma_f32_16x16x32_bf16 a[0:3], a[148:151], a[168:171], a[0:3]\n"
"s_add_u32 s33, s33, 1\n"
"buffer_load_dwordx4 a[144:147], v216, s[60:63], s8 offen offset:2048 sc1\n"
"v_mfma_f32_16x16x32_bf16 a[4:7], a[152:155], a[168:171], a[4:7]\n"
"s_add_u32 s8, s35, s34\n"
"s_cmp_eq_u32 s33, 13\n"
"s_cselect_b32 s33, 0, s33\n"
"v_mfma_f32_16x16x32_bf16 a[8:11], a[156:159], a[168:171], a[8:11]\n"
"v_mfma_f32_16x16x32_bf16 a[12:15], a[160:163], a[168:171], a[12:15]\n"
"buffer_load_dwordx4 a[224:227], v216, s[60:63], s9 offen offset:2048 sc1\n"
"v_mfma_f32_16x16x32_bf16 a[32:35], a[164:167], a[168:171], a[32:35]\n"
"s_waitcnt vmcnt(22) lgkmcnt(3)\n"
"s_add_u32 s9, s44, s34\n"
"s_addk_i32 s34, 4096\n"
"v_mfma_f32_16x16x32_bf16 v[36:39], a[36:39], a[172:175], v[36:39]\n"
"s_waitcnt lgkmcnt(0)\n"
"s_barrier\n"
"ds_read_b128 a[180:183], v228 offset:20480\n"
"v_mfma_f32_16x16x32_bf16 v[96:99], a[40:43], a[172:175], v[96:99]\n"
"s_cmp_eq_u32 s34, 53248\n"
"s_cselect_b32 s34, 0, s34\n"
"ds_read_b128 a[184:187], v228 offset:21504\n"
"v_mov_b32 v226, 0\n"
"v_mfma_f32_16x16x32_bf16 v[100:103], a[44:47], a[172:175], v[100:103]\n"
"ds_read_b128 a[188:191], v228 offset:22528\n"
"v_mov_b32 v227, 0\n"
"v_mfma_f32_16x16x32_bf16 v[40:43], a[64:67], a[172:175], v[40:43]\n"
"ds_read_b128 a[192:195], v228 offset:23552\n"
"v_and_b32 v220, 1, v253\n"
"v_mfma_f32_16x16x32_bf16 v[44:47], a[68:71], a[172:175], v[44:47]\n"
"s_waitcnt vmcnt(19)\n"
"ds_read_b128 a[196:199], v228 offset:24576\n"
"v_lshlrev_b32 v220, 4, v220\n"
"v_mfma_f32_16x16x32_bf16 a[0:3], a[36:39], a[176:179], a[0:3]\n"
"buffer_load_dwordx4 a[228:231], v216, s[60:63], s8 offen sc1\n"
"v_mfma_f32_16x16x32_bf16 a[4:7], a[40:43], a[176:179], a[4:7]\n"
"ds_read_b128 a[148:151], v228 offset:30720\n"
"v_mov_b32 v221, 0x3f80\n"
"v_mfma_f32_16x16x32_bf16 a[8:11], a[44:47], a[176:179], a[8:11]\n"
"ds_read_b128 a[152:155], v228 offset:31744\n"
"v_lshlrev_b32 v220, v220, v221\n"
"v_mfma_f32_16x16x32_bf16 a[12:15], a[64:67], a[176:179], a[12:15]\n"
"buffer_load_dwordx4 a[232:235], v216, s[60:63], s9 offen sc1\n"
"v_mfma_f32_16x16x32_bf16 a[32:35], a[68:71], a[176:179], a[32:35]\n"
"s_waitcnt vmcnt(19) lgkmcnt(6)\n"
"ds_read_b128 a[156:159], v228 offset:32768\n"
"v_bfe_u32 v221, v253, 1, 3\n"
"v_mfma_f32_16x16x32_bf16 v[36:39], a[180:183], a[72:75], v[36:39]\n"
"s_waitcnt lgkmcnt(6)\n"
"ds_read_b128 a[160:163], v228 offset:33792\n"
"v_lshrrev_b32 v224, 4, v253\n"
"v_mfma_f32_16x16x32_bf16 v[96:99], a[184:187], a[72:75], v[96:99]\n"
"s_waitcnt vmcnt(14) lgkmcnt(6)\n"
"buffer_store_dwordx2 v[232:233], v246, s[60:63], s99 offen sc1\n"
"ds_read_b128 a[164:167], v228 offset:34816\n"
"v_mfma_f32_16x16x32_bf16 v[100:103], a[188:191], a[72:75], v[100:103]\n"
"s_waitcnt lgkmcnt(6)\n"
"v_lshlrev_b32 v224, 1, v224\n"
"v_sub_u32 v221, v221, v224\n"
"v_mfma_f32_16x16x32_bf16 v[40:43], a[192:195], a[72:75], v[40:43]\n"
"s_waitcnt lgkmcnt(5)\n"
"v_cmp_eq_u32 vcc, 0, v221\n"
"s_nop 1\n"
"v_cndmask_b32 v224, 0, v220, vcc\n"
"v_mfma_f32_16x16x32_bf16 v[44:47], a[196:199], a[72:75], v[44:47]\n"
"s_waitcnt vmcnt(14)\n"
"buffer_load_dwordx4 a[124:127], v216, s[60:63], s8 offen offset:2048 sc1\n"
"v_mfma_f32_16x16x32_bf16 a[0:3], a[180:183], a[76:79], a[0:3]\n"
"s_add_u32 s8, s35, s34\n"
"v_cmp_eq_u32 vcc, 1, v221\n"
"s_nop 1\n"
"v_cndmask_b32 v225, 0, v220, vcc\n"
"v_mfma_f32_16x16x32_bf16 a[4:7], a[184:187], a[76:79], a[4:7]\n"
"v_mov_b32 v222, 0\n"
"v_mov_b32 v223, 0\n"
"v_mfma_f32_16x16x32_bf16 a[8:11], a[188:191], a[76:79], a[8:11]\n"
"buffer_load_dwordx4 a[236:239], v216, s[60:63], s9 offen offset:2048 sc1\n"
"v_mfma_f32_16x16x32_bf16 a[12:15], a[192:195], a[76:79], a[12:15]\n"
"s_add_u32 s9, s44, s34\n"
"s_addk_i32 s34, 4096\n"
"s_cmp_eq_u32 s34, 53248\n"
"s_cselect_b32 s34, 0, s34\n"
"v_mfma_f32_16x16x32_bf16 a[32:35], a[196:199], a[76:79], a[32:35]\n"
"s_waitcnt vmcnt(15) lgkmcnt(3)\n"
"v_mfma_f32_16x16x32_bf16 v[36:39], a[148:151], a[96:99], v[36:39]\n"
"s_waitcnt lgkmcnt(0)\n"
"s_barrier\n"
"ds_read_b128 a[36:39], v229 offset:40960\n"
"v_mfma_f32_16x16x32_bf16 v[96:99], a[152:155], a[96:99], v[96:99]\n"
"ds_read_b128 a[40:43], v229 offset:41984\n"
"v_mfma_f32_16x16x32_bf16 v[100:103], a[156:159], a[96:99], v[100:103]\n"
"ds_read_b128 a[44:47], v229 offset:43008\n"
"v_mfma_f32_16x16x32_bf16 v[40:43], a[160:163], a[96:99], v[40:43]\n"
"ds_read_b128 a[64:67], v229 offset:44032\n"
"v_mfma_f32_16x16x32_bf16 v[44:47], a[164:167], a[96:99], v[44:47]\n"
"s_waitcnt vmcnt(14)\n"
"ds_read_b128 a[68:71], v229 offset:45056\n"
"v_mfma_f32_16x16x32_bf16 a[0:3], a[148:151], a[100:103], a[0:3]\n"
"buffer_load_dwordx4 v216, s[60:63], s83 offen sc1 lds\n"
"v_mfma_f32_16x16x32_bf16 a[4:7], a[152:155], a[100:103], a[4:7]\n"
"ds_read_b128 a[180:183], v229 offset:51200\n"
"v_mfma_f32_16x16x32_bf16 a[8:11], a[156:159], a[100:103], a[8:11]\n"
"ds_read_b128 a[184:187], v229 offset:52224\n"
"v_mfma_f32_16x16x32_bf16 a[12:15], a[160:163], a[100:103], a[12:15]\n"
"buffer_load_dwordx4 v216, s[60:63], s83 offen offset:1024 sc1 lds\n"
"buffer_load_dwordx4 v216, s[60:63], s83 offen offset:2048 sc1 lds\n"
"buffer_load_dwordx4 v216, s[60:63], s83 offen offset:3072 sc1 lds\n"
"s_addk_i32 m0, 1024\n"
"v_mfma_f32_16x16x32_bf16 a[32:35], a[164:167], a[100:103], a[32:35]\n"
"s_waitcnt vmcnt(12) lgkmcnt(6)\n"
"buffer_load_dwordx4 v216, s[60:63], s82 offen offset:3072 sc1 lds\n"
"buffer_load_dwordx4 a[240:243], v216, s[60:63], s8 offen sc1\n"
"v_mfma_f32_16x16x32_bf16 v[36:39], a[36:39], a[104:107], v[36:39]\n"
"s_waitcnt lgkmcnt(5)\n"
"ds_read_b128 a[188:191], v229 offset:53248\n"
"s_addk_i32 m0, 44032\n"
"v_mfma_f32_16x16x32_bf16 v[96:99], a[40:43], a[104:107], v[96:99]\n"
"s_waitcnt lgkmcnt(5)\n"
"ds_read_b128 a[192:195], v229 offset:54272\n"
"ds_read_b128 a[196:199], v229 offset:55296\n"
"v_mfma_f32_16x16x32_bf16 v[100:103], a[44:47], a[104:107], v[100:103]\n"
"s_waitcnt lgkmcnt(6)\n"
"buffer_load_dwordx4 a[244:247], v216, s[60:63], s9 offen sc1\n"
"v_mfma_f32_16x16x32_bf16 v[40:43], a[64:67], a[104:107], v[40:43]\n"
"s_waitcnt lgkmcnt(5)\n"
"s_mul_i32 s83, s33, 20480\n"
"s_add_u32 s83, s83, s32\n"
"v_mfma_f32_16x16x32_bf16 v[44:47], a[68:71], a[104:107], v[44:47]\n"
"s_waitcnt vmcnt(14)\n"
"s_add_u32 s82, s83, 1024\n"
"s_add_u32 s33, s33, 1\n"
"v_mfma_f32_16x16x32_bf16 a[0:3], a[36:39], a[108:111], a[0:3]\n"
"s_cmp_eq_u32 s33, 13\n"
"s_cselect_b32 s33, 0, s33\n"
"buffer_load_dwordx4 a[248:251], v216, s[60:63], s8 offen offset:2048 sc1\n"
"v_mfma_f32_16x16x32_bf16 a[4:7], a[40:43], a[108:111], a[4:7]\n"
"s_add_u32 s8, s35, s34\n"
"v_mfma_f32_16x16x32_bf16 a[8:11], a[44:47], a[108:111], a[8:11]\n"
"v_mfma_f32_16x16x32_bf16 a[12:15], a[64:67], a[108:111], a[12:15]\n"
"buffer_load_dwordx4 a[252:255], v216, s[60:63], s9 offen offset:2048 sc1\n"
"v_mfma_f32_16x16x32_bf16 a[32:35], a[68:71], a[108:111], a[32:35]\n"
"s_waitcnt vmcnt(15) lgkmcnt(3)\n"
"s_add_u32 s9, s44, s34\n"
"s_addk_i32 s34, 4096\n"
"v_mfma_f32_16x16x32_bf16 v[36:39], a[180:183], a[144:147], v[36:39]\n"
"s_waitcnt lgkmcnt(0)\n"
"s_barrier\n"
"ds_read_b128 a[148:151], v219 offset:0\n"
"v_mfma_f32_16x16x32_bf16 v[96:99], a[184:187], a[144:147], v[96:99]\n"
"s_cmp_eq_u32 s34, 53248\n"
"s_cselect_b32 s34, 0, s34\n"
"ds_read_b128 a[152:155], v219 offset:1024\n"
"v_mfma_f32_16x16x32_bf16 v[100:103], a[188:191], a[144:147], v[100:103]\n"
"ds_read_b128 a[156:159], v219 offset:2048\n"
"v_mfma_f32_16x16x32_bf16 v[40:43], a[192:195], a[144:147], v[40:43]\n"
"ds_read_b128 a[160:163], v219 offset:3072\n"
"v_mfma_f32_16x16x32_bf16 v[44:47], a[196:199], a[144:147], v[44:47]\n"
"s_waitcnt vmcnt(14)\n"
"ds_read_b128 a[164:167], v219 offset:4096\n"
"v_mfma_f32_16x16x32_bf16 a[0:3], a[180:183], a[224:227], a[0:3]\n"
"buffer_load_dwordx4 a[200:203], v216, s[60:63], s8 offen sc1\n"
"v_mfma_f32_16x16x32_bf16 a[4:7], a[184:187], a[224:227], a[4:7]\n"
"ds_read_b128 a[36:39], v219 offset:10240\n"
"v_mfma_f32_16x16x32_bf16 a[8:11], a[188:191], a[224:227], a[8:11]\n"
"ds_read_b128 a[40:43], v219 offset:11264\n"
"v_mfma_f32_16x16x32_bf16 a[12:15], a[192:195], a[224:227], a[12:15]\n"
"buffer_load_dwordx4 v216, s[60:63], s83 offen sc1 lds\n"
"v_mfma_f32_16x16x32_bf16 a[32:35], a[196:199], a[224:227], a[32:35]\n"
"s_waitcnt vmcnt(15) lgkmcnt(6)\n"
"ds_read_b128 a[44:47], v219 offset:12288\n"
"v_mfma_f32_16x16x32_bf16 v[36:39], a[148:151], a[228:231], v[36:39]\n"
"s_waitcnt lgkmcnt(6)\n"
"ds_read_b128 a[64:67], v219 offset:13312\n"
"v_mfma_f32_16x16x32_bf16 v[96:99], a[152:155], a[228:231], v[96:99]\n"
"s_waitcnt lgkmcnt(6)\n"
"ds_read_b128 a[68:71], v219 offset:14336\n"
"v_mfma_f32_16x16x32_bf16 v[100:103], a[156:159], a[228:231], v[100:103]\n"
"s_waitcnt lgkmcnt(6)\n"
"buffer_load_dwordx4 v216, s[60:63], s83 offen offset:1024 sc1 lds\n"
"v_mfma_f32_16x16x32_bf16 v[40:43], a[160:163], a[228:231], v[40:43]\n"
"s_waitcnt lgkmcnt(5)\n"
"buffer_load_dwordx4 v216, s[60:63], s83 offen offset:2048 sc1 lds\n"
"buffer_load_dwordx4 v216, s[60:63], s83 offen offset:3072 sc1 lds\n"
"s_addk_i32 m0, 1024\n"
"v_mfma_f32_16x16x32_bf16 v[44:47], a[164:167], a[228:231], v[44:47]\n"
"s_waitcnt vmcnt(17)\n"
"buffer_load_dwordx4 v216, s[60:63], s82 offen offset:3072 sc1 lds\n"
"buffer_load_dwordx4 a[128:131], v216, s[60:63], s9 offen sc1\n"
"v_mfma_f32_16x16x32_bf16 a[0:3], a[148:151], a[232:235], a[0:3]\n"
"s_add_u32 m0, s78, 0\n"
"s_mul_i32 s83, s33, 20480\n"
"s_add_u32 s83, s83, s32\n"
"v_mfma_f32_16x16x32_bf16 a[4:7], a[152:155], a[232:235], a[4:7]\n"
"s_add_u32 s82, s83, 1024\n"
"s_add_u32 s33, s33, 1\n"
"s_cmp_eq_u32 s33, 13\n"
"s_cselect_b32 s33, 0, s33\n"
"v_mfma_f32_16x16x32_bf16 a[8:11], a[156:159], a[232:235], a[8:11]\n"
"buffer_load_dwordx4 a[132:135], v216, s[60:63], s8 offen offset:2048 sc1\n"
"v_mfma_f32_16x16x32_bf16 a[12:15], a[160:163], a[232:235], a[12:15]\n"
"s_add_u32 s8, s35, s34\n"
"v_mfma_f32_16x16x32_bf16 a[32:35], a[164:167], a[232:235], a[32:35]\n"
"s_waitcnt vmcnt(18) lgkmcnt(3)\n"
"v_mfma_f32_16x16x32_bf16 v[36:39], a[36:39], a[124:127], v[36:39]\n"
"s_waitcnt lgkmcnt(0)\n"
"s_barrier\n"
"ds_read_b128 a[180:183], v219 offset:20480\n"
"v_mfma_f32_16x16x32_bf16 v[96:99], a[40:43], a[124:127], v[96:99]\n"
"ds_read_b128 a[184:187], v219 offset:21504\n"
"v_mfma_f32_16x16x32_bf16 v[100:103], a[44:47], a[124:127], v[100:103]\n"
"ds_read_b128 a[188:191], v219 offset:22528\n"
"v_mfma_f32_16x16x32_bf16 v[40:43], a[64:67], a[124:127], v[40:43]\n"
"ds_read_b128 a[192:195], v219 offset:23552\n"
"v_mfma_f32_16x16x32_bf16 v[44:47], a[68:71], a[124:127], v[44:47]\n"
"s_waitcnt vmcnt(17)\n"
"buffer_load_dwordx4 a[16:19], v216, s[60:63], s9 offen offset:2048 sc1\n"
"ds_read_b128 a[196:199], v219 offset:24576\n"
"v_mfma_f32_16x16x32_bf16 a[0:3], a[36:39], a[236:239], a[0:3]\n"
"s_add_u32 s9, s44, s34\n"
"ds_read_b128 a[148:151], v219 offset:30720\n"
"s_addk_i32 s34, 4096\n"
"v_mfma_f32_16x16x32_bf16 a[4:7], a[40:43], a[236:239], a[4:7]\n"
"s_cmp_eq_u32 s34, 53248\n"
"s_cselect_b32 s34, 0, s34\n"
"ds_read_b128 a[152:155], v219 offset:31744\n"
"v_mfma_f32_16x16x32_bf16 a[8:11], a[44:47], a[236:239], a[8:11]\n"
"buffer_load_dwordx4 a[20:23], v216, s[60:63], s8 offen sc1\n"
"v_mfma_f32_16x16x32_bf16 a[12:15], a[64:67], a[236:239], a[12:15]\n"
"ds_read_b128 a[156:159], v219 offset:32768\n"
"v_mfma_f32_16x16x32_bf16 a[32:35], a[68:71], a[236:239], a[32:35]\n"
"s_waitcnt vmcnt(13) lgkmcnt(7)\n"
"ds_read_b128 a[160:163], v219 offset:33792\n"
"s_barrier\n"
"v_mfma_f32_16x16x32_bf16 v[36:39], a[180:183], a[240:243], v[36:39]\n"
"s_waitcnt lgkmcnt(7)\n"
"buffer_load_dwordx4 v216, s[60:63], s83 offen sc1 lds\n"
"v_mfma_f32_16x16x32_bf16 v[96:99], a[184:187], a[240:243], v[96:99]\n"
"s_waitcnt lgkmcnt(6)\n"
"ds_read_b128 a[164:167], v219 offset:34816\n"
"v_mfma_f32_16x16x32_bf16 v[100:103], a[188:191], a[240:243], v[100:103]\n"
"s_waitcnt lgkmcnt(6)\n"
"ds_read_b128 a[36:39], v229 offset:20480\n"
"v_mfma_f32_16x16x32_bf16 v[40:43], a[192:195], a[240:243], v[40:43]\n"
"s_waitcnt lgkmcnt(6)\n"
"buffer_load_dwordx4 v216, s[60:63], s83 offen offset:1024 sc1 lds\n"
"buffer_load_dwordx4 v216, s[60:63], s83 offen offset:2048 sc1 lds\n"
"buffer_load_dwordx4 v216, s[60:63], s83 offen offset:3072 sc1 lds\n"
"s_addk_i32 m0, 1024\n"
"v_mfma_f32_16x16x32_bf16 v[44:47], a[196:199], a[240:243], v[44:47]\n"
"s_waitcnt vmcnt(16)\n"
"buffer_load_dwordx4 v216, s[60:63], s82 offen offset:3072 sc1 lds\n"
"buffer_load_dwordx4 a[24:27], v216, s[60:63], s9 offen sc1\n"
"v_mfma_f32_16x16x32_bf16 a[0:3], a[180:183], a[244:247], a[0:3]\n"
"s_addk_i32 m0, 19456\n"
"ds_read_b128 a[40:43], v229 offset:21504\n"
"s_mul_i32 s83, s33, 20480\n"
"v_mfma_f32_16x16x32_bf16 a[4:7], a[184:187], a[244:247], a[4:7]\n"
"s_add_u32 s83, s83, s32\n"
"ds_read_b128 a[44:47], v229 offset:22528\n"
"s_add_u32 s82, s83, 1024\n"
"v_mfma_f32_16x16x32_bf16 a[8:11], a[188:191], a[244:247], a[8:11]\n"
"s_add_u32 s33, s33, 1\n"
"buffer_load_dwordx4 a[28:31], v216, s[60:63], s8 offen offset:2048 sc1\n"
"v_mfma_f32_16x16x32_bf16 a[12:15], a[192:195], a[244:247], a[12:15]\n"
"s_add_u32 s8, s35, s34\n"
"ds_read_b128 a[64:67], v229 offset:23552\n"
"s_cmp_eq_u32 s33, 13\n"
"s_cselect_b32 s33, 0, s33\n"
"v_mfma_f32_16x16x32_bf16 a[32:35], a[196:199], a[244:247], a[32:35]\n"
"s_waitcnt vmcnt(18) lgkmcnt(7)\n"
"ds_read_b128 a[68:71], v229 offset:24576\n"
"v_mfma_f32_16x16x32_bf16 v[36:39], a[148:151], a[248:251], v[36:39]\n"
"buffer_load_dwordx4 a[48:51], v216, s[60:63], s9 offen offset:2048 sc1\n"
"v_mfma_f32_16x16x32_bf16 v[96:99], a[152:155], a[248:251], v[96:99]\n"
"s_waitcnt lgkmcnt(6)\n"
"ds_read_b128 a[180:183], v229 offset:30720\n"
"s_add_u32 s9, s44, s34\n"
"v_mfma_f32_16x16x32_bf16 v[100:103], a[156:159], a[248:251], v[100:103]\n"
"s_addk_i32 s34, 4096\n"
"ds_read_b128 a[184:187], v229 offset:31744\n"
"s_cmp_eq_u32 s34, 53248\n"
"s_cselect_b32 s34, 0, s34\n"
"v_mfma_f32_16x16x32_bf16 v[40:43], a[160:163], a[248:251], v[40:43]\n"
"s_waitcnt lgkmcnt(7)\n"
"buffer_load_dwordx4 a[136:139], v216, s[60:63], s8 offen sc1\n"
"v_mfma_f32_16x16x32_bf16 v[44:47], a[164:167], a[248:251], v[44:47]\n"
"s_waitcnt vmcnt(19) lgkmcnt(6)\n"
"ds_read_b128 a[188:191], v229 offset:32768\n"
"v_mfma_f32_16x16x32_bf16 a[0:3], a[148:151], a[252:255], a[0:3]\n"
"ds_read_b128 a[192:195], v229 offset:33792\n"
"v_mfma_f32_16x16x32_bf16 a[4:7], a[152:155], a[252:255], a[4:7]\n"
"buffer_load_dwordx4 v216, s[60:63], s83 offen sc1 lds\n"
"v_mfma_f32_16x16x32_bf16 a[8:11], a[156:159], a[252:255], a[8:11]\n"
"ds_read_b128 a[196:199], v229 offset:34816\n"
"v_mfma_f32_16x16x32_bf16 a[12:15], a[160:163], a[252:255], a[12:15]\n"
"v_mfma_f32_16x16x32_bf16 a[32:35], a[164:167], a[252:255], a[32:35]\n"
"s_waitcnt vmcnt(19) lgkmcnt(7)\n"
"buffer_load_dwordx4 v216, s[60:63], s83 offen offset:1024 sc1 lds\n"
"v_mfma_f32_16x16x32_bf16 v[36:39], a[36:39], a[200:203], v[36:39]\n"
"s_barrier\n"
"ds_read_b128 a[148:151], v229 offset:0\n"
"v_mfma_f32_16x16x32_bf16 v[96:99], a[40:43], a[200:203], v[96:99]\n"
"ds_read_b128 a[152:155], v229 offset:1024\n"
"v_mfma_f32_16x16x32_bf16 v[100:103], a[44:47], a[200:203], v[100:103]\n"
"s_waitcnt lgkmcnt(7)\n"
"buffer_load_dwordx4 v216, s[60:63], s83 offen offset:2048 sc1 lds\n"
"v_mfma_f32_16x16x32_bf16 v[40:43], a[64:67], a[200:203], v[40:43]\n"
"buffer_load_dwordx4 v216, s[60:63], s83 offen offset:3072 sc1 lds\n"
"s_addk_i32 m0, 1024\n"
"v_mfma_f32_16x16x32_bf16 v[44:47], a[68:71], a[200:203], v[44:47]\n"
"s_waitcnt vmcnt(16) lgkmcnt(5)\n"
"buffer_load_dwordx4 v216, s[60:63], s82 offen offset:3072 sc1 lds\n"
"buffer_load_dwordx4 a[140:143], v216, s[60:63], s9 offen sc1\n"
"v_mfma_f32_16x16x32_bf16 a[0:3], a[36:39], a[128:131], a[0:3]\n"
"s_add_u32 m0, s78, 102400\n"
"ds_read_b128 a[156:159], v229 offset:2048\n"
"s_mul_i32 s83, s33, 20480\n"
"v_mfma_f32_16x16x32_bf16 a[4:7], a[40:43], a[128:131], a[4:7]\n"
"s_add_u32 s83, s83, s32\n"
"ds_read_b128 a[160:163], v229 offset:3072\n"
"s_add_u32 s82, s83, 1024\n"
"v_mfma_f32_16x16x32_bf16 a[8:11], a[44:47], a[128:131], a[8:11]\n"
"s_add_u32 s33, s33, 1\n"
"buffer_load_dwordx4 a[52:55], v216, s[60:63], s8 offen offset:2048 sc1\n"
"v_mfma_f32_16x16x32_bf16 a[12:15], a[64:67], a[128:131], a[12:15]\n"
"s_add_u32 s8, s35, s34\n"
"ds_read_b128 a[164:167], v229 offset:4096\n"
"s_cmp_eq_u32 s33, 13\n"
"s_cselect_b32 s33, 0, s33\n"
"v_mfma_f32_16x16x32_bf16 a[32:35], a[68:71], a[128:131], a[32:35]\n"
"s_waitcnt vmcnt(18) lgkmcnt(5)\n"
"ds_read_b128 a[36:39], v229 offset:10240\n"
"v_mfma_f32_16x16x32_bf16 v[36:39], a[180:183], a[132:135], v[36:39]\n"
"buffer_load_dwordx4 a[56:59], v216, s[60:63], s9 offen offset:2048 sc1\n"
"v_mfma_f32_16x16x32_bf16 v[96:99], a[184:187], a[132:135], v[96:99]\n"
"s_add_u32 s9, s44, s34\n"
"ds_read_b128 a[40:43], v229 offset:11264\n"
"s_addk_i32 s34, 4096\n"
"v_mfma_f32_16x16x32_bf16 v[100:103], a[188:191], a[132:135], v[100:103]\n"
"s_cmp_eq_u32 s34, 53248\n"
"s_cselect_b32 s34, 0, s34\n"
"ds_read_b128 a[44:47], v229 offset:12288\n"
"v_mfma_f32_16x16x32_bf16 v[40:43], a[192:195], a[132:135], v[40:43]\n"
"buffer_load_dwordx4 a[60:63], v216, s[60:63], s8 offen sc1\n"
"v_mfma_f32_16x16x32_bf16 v[44:47], a[196:199], a[132:135], v[44:47]\n"
"s_waitcnt vmcnt(19) lgkmcnt(6)\n"
"ds_read_b128 a[64:67], v229 offset:13312\n"
"v_mfma_f32_16x16x32_bf16 a[0:3], a[180:183], a[16:19], a[0:3]\n"
"ds_read_b128 a[68:71], v229 offset:14336\n"
"v_mfma_f32_16x16x32_bf16 a[4:7], a[184:187], a[16:19], a[4:7]\n"
"buffer_load_dwordx4 v216, s[60:63], s83 offen sc1 lds\n"
"v_mfma_f32_16x16x32_bf16 a[8:11], a[188:191], a[16:19], a[8:11]\n"
"v_mfma_f32_16x16x32_bf16 a[12:15], a[192:195], a[16:19], a[12:15]\n"
"v_mfma_f32_16x16x32_bf16 a[32:35], a[196:199], a[16:19], a[32:35]\n"
"s_waitcnt vmcnt(19) lgkmcnt(6)\n"
"buffer_load_dwordx4 v216, s[60:63], s83 offen offset:1024 sc1 lds\n"
"v_mfma_f32_16x16x32_bf16 v[36:39], a[148:151], a[20:23], v[36:39]\n"
"s_barrier\n"
"ds_read_b128 a[180:183], v228 offset:40960\n"
"s_and_b32 s3, s73, 252\n"
"v_mfma_f32_16x16x32_bf16 v[96:99], a[152:155], a[20:23], v[96:99]\n"
"s_lshl_b32 s93, s72, 8\n"
"ds_read_b128 a[184:187], v228 offset:41984\n"
"s_add_u32 s3, s3, s93\n"
"v_mfma_f32_16x16x32_bf16 v[100:103], a[156:159], a[20:23], v[100:103]\n"
"s_lshl_b32 s3, s3, 7\n"
"buffer_load_dwordx4 v216, s[60:63], s83 offen offset:2048 sc1 lds\n"
"v_mfma_f32_16x16x32_bf16 v[40:43], a[160:163], a[20:23], v[40:43]\n"
"s_waitcnt lgkmcnt(5)\n"
"buffer_load_dwordx4 v216, s[60:63], s83 offen offset:3072 sc1 lds\n"
"s_addk_i32 m0, 1024\n"
"v_mfma_f32_16x16x32_bf16 v[44:47], a[164:167], a[20:23], v[44:47]\n"
"s_waitcnt vmcnt(16) lgkmcnt(4)\n"
"buffer_load_dwordx4 v216, s[60:63], s82 offen offset:3072 sc1 lds\n"
"buffer_load_dwordx4 a[80:83], v216, s[60:63], s9 offen sc1\n"
"v_mfma_f32_16x16x32_bf16 a[0:3], a[148:151], a[24:27], a[0:3]\n"
"s_add_u32 s3, s3, 1048576\n"
"ds_read_b128 a[188:191], v228 offset:43008\n"
"v_and_b32 v239, 3, v253\n"
"v_lshlrev_b32 v239, 7, v239\n"
"buffer_load_dwordx2 v[240:241], v239, s[60:63], s3 offen sc1\n"
"v_mfma_f32_16x16x32_bf16 a[4:7], a[152:155], a[24:27], a[4:7]\n"
"s_addk_i32 m0, 19456\n"
"ds_read_b128 a[192:195], v228 offset:44032\n"
"s_add_u32 s83, s32, 266240\n"
"v_mfma_f32_16x16x32_bf16 a[8:11], a[156:159], a[24:27], a[8:11]\n"
"s_add_u32 s82, s83, 1024\n"
"ds_read_b128 a[196:199], v228 offset:45056\n"
"v_lshlrev_b32 v104, 16, v242\n"
"v_mfma_f32_16x16x32_bf16 a[12:15], a[160:163], a[24:27], a[12:15]\n"
"s_add_u32 s92, s78, 0\n"
"buffer_load_dwordx4 a[84:87], v216, s[60:63], s8 offen offset:2048 sc1\n"
"v_mfma_f32_16x16x32_bf16 a[32:35], a[164:167], a[24:27], a[32:35]\n"
"s_waitcnt vmcnt(19) lgkmcnt(5)\n"
"ds_read_b128 a[148:151], v228 offset:51200\n"
"s_add_u32 s8, s35, s34\n"
"v_mfma_f32_16x16x32_bf16 v[36:39], a[36:39], a[28:31], v[36:39]\n"
"ds_read_b128 a[152:155], v228 offset:52224\n"
"v_add_u32 v234, s92, v216\n"
"v_mfma_f32_16x16x32_bf16 v[96:99], a[40:43], a[28:31], v[96:99]\n"
"s_xor_b32 s92, s72, 1\n"
"buffer_load_dwordx4 a[212:215], v216, s[60:63], s9 offen offset:2048 sc1\n"
"v_mfma_f32_16x16x32_bf16 v[100:103], a[44:47], a[28:31], v[100:103]\n"
"s_add_u32 s9, s44, s34\n"
"ds_read_b128 a[156:159], v228 offset:53248\n"
"s_addk_i32 s34, 4096\n"
"v_mfma_f32_16x16x32_bf16 v[40:43], a[64:67], a[28:31], v[40:43]\n"
"s_cmp_eq_u32 s34, 53248\n"
"s_cselect_b32 s34, 0, s34\n"
"ds_read_b128 a[160:163], v228 offset:54272\n"
"v_and_b32 v105, s1, v242\n"
"v_mfma_f32_16x16x32_bf16 v[44:47], a[68:71], a[28:31], v[44:47]\n"
"s_waitcnt vmcnt(19) lgkmcnt(7)\n"
"buffer_load_dwordx4 a[216:219], v216, s[60:63], s8 offen sc1\n"
"v_mfma_f32_16x16x32_bf16 a[0:3], a[36:39], a[48:51], a[0:3]\n"
"s_mul_i32 s92, s92, 5120\n"
"ds_read_b128 a[164:167], v228 offset:55296\n"
"v_lshlrev_b32 v106, 16, v243\n"
"v_mfma_f32_16x16x32_bf16 a[4:7], a[40:43], a[48:51], a[4:7]\n"
"s_add_u32 s92, s92, 0\n"
"v_add_u32 v235, s92, v216\n"
"v_and_b32 v107, s1, v243\n"
"v_mfma_f32_16x16x32_bf16 a[8:11], a[44:47], a[48:51], a[8:11]\n"
"buffer_load_dwordx4 v216, s[60:63], s83 offen sc1 lds\n"
"v_mfma_f32_16x16x32_bf16 a[12:15], a[64:67], a[48:51], a[12:15]\n"
"v_lshlrev_b32 v108, 16, v244\n"
"v_and_b32 v109, s1, v244\n"
"v_mfma_f32_16x16x32_bf16 a[32:35], a[68:71], a[48:51], a[32:35]\n"
"s_waitcnt vmcnt(20) lgkmcnt(5)\n"
"s_barrier\n"
"ds_read_b128 a[36:39], v228 offset:0\n"
"v_mfma_f32_16x16x32_bf16 v[36:39], a[180:183], a[136:139], v[36:39]\n"
"buffer_load_dwordx4 v216, s[60:63], s83 offen offset:1024 sc1 lds\n"
"v_mfma_f32_16x16x32_bf16 v[96:99], a[184:187], a[136:139], v[96:99]\n"
"ds_read_b128 a[40:43], v228 offset:1024\n"
"v_lshlrev_b32 v110, 16, v245\n"
"v_mfma_f32_16x16x32_bf16 v[100:103], a[188:191], a[136:139], v[100:103]\n"
"ds_read_b128 a[44:47], v228 offset:2048\n"
"v_and_b32 v111, s1, v245\n"
"v_mfma_f32_16x16x32_bf16 v[40:43], a[192:195], a[136:139], v[40:43]\n"
"buffer_load_dwordx4 v216, s[60:63], s83 offen offset:2048 sc1 lds\n"
"buffer_load_dwordx4 v216, s[60:63], s83 offen offset:3072 sc1 lds\n"
"s_addk_i32 m0, 1024\n"
"v_mfma_f32_16x16x32_bf16 v[44:47], a[196:199], a[136:139], v[44:47]\n"
"s_waitcnt vmcnt(17) lgkmcnt(5)\n"
"buffer_load_dwordx4 v216, s[60:63], s82 offen offset:3072 sc1 lds\n"
"buffer_load_dwordx4 a[88:91], v216, s[60:63], s9 offen sc1\n"
"v_mfma_f32_16x16x32_bf16 a[0:3], a[180:183], a[140:143], a[0:3]\n"
"ds_read_b128 a[64:67], v228 offset:3072\n"
"v_lshlrev_b32 v176, 16, v172\n"
"v_mfma_f32_16x16x32_bf16 a[4:7], a[184:187], a[140:143], a[4:7]\n"
"ds_read_b128 a[68:71], v228 offset:4096\n"
"v_and_b32 v177, s1, v172\n"
"v_mfma_f32_16x16x32_bf16 a[8:11], a[188:191], a[140:143], a[8:11]\n"
"buffer_load_dwordx4 a[92:95], v216, s[60:63], s8 offen offset:2048 sc1\n"
"v_mfma_f32_16x16x32_bf16 a[12:15], a[192:195], a[140:143], a[12:15]\n"
"s_add_u32 s8, s35, s34\n"
"v_lshlrev_b32 v178, 16, v173\n"
"v_and_b32 v179, s1, v173\n"
"v_mfma_f32_16x16x32_bf16 a[32:35], a[196:199], a[140:143], a[32:35]\n"
"s_waitcnt vmcnt(19) lgkmcnt(5)\n"
"ds_read_b128 a[180:183], v228 offset:10240\n"
"v_lshlrev_b32 v180, 16, v174\n"
"v_mfma_f32_16x16x32_bf16 v[36:39], a[148:151], a[52:55], v[36:39]\n"
"buffer_load_dwordx4 a[112:115], v216, s[60:63], s9 offen offset:2048 sc1\n"
"v_mfma_f32_16x16x32_bf16 v[96:99], a[152:155], a[52:55], v[96:99]\n"
"s_add_u32 s9, s44, s34\n"
"ds_read_b128 a[184:187], v228 offset:11264\n"
"s_addk_i32 s34, 4096\n"
"v_mfma_f32_16x16x32_bf16 v[100:103], a[156:159], a[52:55], v[100:103]\n"
"s_cmp_eq_u32 s34, 53248\n"
"s_cselect_b32 s34, 0, s34\n"
"ds_read_b128 a[188:191], v228 offset:12288\n"
"s_mov_b32 s34, 53248\n"
"v_mfma_f32_16x16x32_bf16 v[40:43], a[160:163], a[52:55], v[40:43]\n"
"buffer_load_dwordx4 a[116:119], v216, s[60:63], s8 offen sc1\n"
"v_mfma_f32_16x16x32_bf16 v[44:47], a[164:167], a[52:55], v[44:47]\n"
"s_waitcnt vmcnt(20) lgkmcnt(5)\n"
"ds_read_b128 a[192:195], v228 offset:13312\n"
"v_and_b32 v181, s1, v174\n"
"v_mfma_f32_16x16x32_bf16 a[0:3], a[148:151], a[56:59], a[0:3]\n"
"ds_read_b128 a[196:199], v228 offset:14336\n"
"v_lshlrev_b32 v182, 16, v175\n"
"v_mfma_f32_16x16x32_bf16 a[4:7], a[152:155], a[56:59], a[4:7]\n"
"buffer_load_dwordx4 a[120:123], v216, s[60:63], s9 offen sc1\n"
"v_mfma_f32_16x16x32_bf16 a[8:11], a[156:159], a[56:59], a[8:11]\n"
"v_and_b32 v183, s1, v175\n"
"v_lshlrev_b32 v184, 16, v236\n"
"v_mfma_f32_16x16x32_bf16 a[12:15], a[160:163], a[56:59], a[12:15]\n"
"v_and_b32 v185, s1, v236\n"
"v_lshlrev_b32 v186, 16, v237\n"
"v_mfma_f32_16x16x32_bf16 a[32:35], a[164:167], a[56:59], a[32:35]\n"
"s_waitcnt vmcnt(20) lgkmcnt(5)\n"
"buffer_load_dwordx4 a[220:223], v216, s[60:63], s8 offen offset:2048 sc1\n"
"v_mfma_f32_16x16x32_bf16 v[36:39], a[36:39], a[60:63], v[36:39]\n"
"s_add_u32 s8, s35, s34\n"
"v_and_b32 v187, s1, v237\n"
"v_mfma_f32_16x16x32_bf16 v[96:99], a[40:43], a[60:63], v[96:99]\n"
"v_mfma_f32_16x16x32_bf16 v[100:103], a[44:47], a[60:63], v[100:103]\n"
"buffer_load_dwordx4 a[204:207], v216, s[60:63], s9 offen offset:2048 sc1\n"
"v_mfma_f32_16x16x32_bf16 v[40:43], a[64:67], a[60:63], v[40:43]\n"
"s_add_u32 s9, s44, s34\n"
"s_addk_i32 s34, 4096\n"
"s_cmp_eq_u32 s34, 53248\n"
"s_cselect_b32 s34, 0, s34\n"
"v_mfma_f32_16x16x32_bf16 v[44:47], a[68:71], a[60:63], v[44:47]\n"
"s_waitcnt vmcnt(16) lgkmcnt(0)\n"
"s_barrier\n"
"ds_read_b128 a[148:151], v228 offset:20480\n"
"v_mfma_f32_16x16x32_bf16 a[0:3], a[36:39], a[80:83], a[0:3]\n"
"buffer_load_dwordx4 a[208:211], v216, s[60:63], s8 offen sc1\n"
"v_mfma_f32_16x16x32_bf16 a[4:7], a[40:43], a[80:83], a[4:7]\n"
"ds_read_b128 a[152:155], v228 offset:21504\n"
"v_mfma_f32_16x16x32_bf16 a[8:11], a[44:47], a[80:83], a[8:11]\n"
"ds_read_b128 a[156:159], v228 offset:22528\n"
"v_mfma_f32_16x16x32_bf16 a[12:15], a[64:67], a[80:83], a[12:15]\n"
"buffer_load_dwordx4 a[168:171], v216, s[60:63], s9 offen sc1\n"
"v_mfma_f32_16x16x32_bf16 a[32:35], a[68:71], a[80:83], a[32:35]\n"
"s_waitcnt vmcnt(16)\n"
"ds_read_b128 a[160:163], v228 offset:23552\n"
"v_mfma_f32_16x16x32_bf16 v[36:39], a[180:183], a[84:87], v[36:39]\n"
"ds_read_b128 a[164:167], v228 offset:24576\n"
"v_mfma_f32_16x16x32_bf16 v[96:99], a[184:187], a[84:87], v[96:99]\n"
"ds_read_b128 a[36:39], v228 offset:30720\n"
"v_mfma_f32_16x16x32_bf16 v[100:103], a[188:191], a[84:87], v[100:103]\n"
"ds_read_b128 a[40:43], v228 offset:31744\n"
"v_mfma_f32_16x16x32_bf16 v[40:43], a[192:195], a[84:87], v[40:43]\n"
"ds_read_b128 a[44:47], v228 offset:32768\n"
"v_mfma_f32_16x16x32_bf16 v[44:47], a[196:199], a[84:87], v[44:47]\n"
"s_waitcnt vmcnt(15)\n"
"ds_read_b128 a[64:67], v228 offset:33792\n"
"v_mfma_f32_16x16x32_bf16 a[0:3], a[180:183], a[212:215], a[0:3]\n"
"ds_read_b128 a[68:71], v228 offset:34816\n"
"v_mfma_f32_16x16x32_bf16 a[4:7], a[184:187], a[212:215], a[4:7]\n"
"v_mfma_f32_16x16x32_bf16 a[8:11], a[188:191], a[212:215], a[8:11]\n"
"v_mfma_f32_16x16x32_bf16 a[12:15], a[192:195], a[212:215], a[12:15]\n"
"v_mfma_f32_16x16x32_bf16 a[32:35], a[196:199], a[212:215], a[32:35]\n"
"s_waitcnt vmcnt(14) lgkmcnt(7)\n"
"v_cmp_eq_u64_e64 s[4:5], v[240:241], s[80:81]\n"
"s_nop 1\n"
"s_cmp_eq_u64 s[4:5], -1\n"
"s_cbranch_scc0 L_spinS_%=\n"
"L_retS_%=:\n"
"v_mfma_f32_16x16x32_bf16 v[36:39], a[148:151], a[216:219], v[36:39]\n"
"s_barrier\n"
"ds_read_b128 a[180:183], v229 offset:40960\n"
"s_lshl_b32 s92, s73, 2\n"
"v_mfma_f32_16x16x32_bf16 v[96:99], a[152:155], a[216:219], v[96:99]\n"
"s_add_u32 s92, s92, s72\n"
"ds_read_b128 a[184:187], v229 offset:41984\n"
"s_mul_i32 s92, s92, 7680\n"
"v_mfma_f32_16x16x32_bf16 v[100:103], a[156:159], a[216:219], v[100:103]\n"
"s_waitcnt lgkmcnt(7)\n"
"ds_read_b128 a[188:191], v229 offset:43008\n"
"s_add_u32 s84, s92, 36044800\n"
"v_mfma_f32_16x16x32_bf16 v[40:43], a[160:163], a[216:219], v[40:43]\n"
"s_add_u32 s85, s92, 36047360\n"
"buffer_load_dwordx4 a[172:175], v216, s[60:63], s84 offen sc1\n"
"v_mfma_f32_16x16x32_bf16 v[44:47], a[164:167], a[216:219], v[44:47]\n"
"s_waitcnt vmcnt(9) lgkmcnt(6)\n"
"ds_read_b128 a[192:195], v229 offset:44032\n"
"s_add_u32 s86, s92, 36049920\n"
"v_mfma_f32_16x16x32_bf16 a[0:3], a[148:151], a[88:91], a[0:3]\n"
"ds_read_b128 a[196:199], v229 offset:45056\n"
"v_accvgpr_write_b32 a74, 0\n"
"v_mfma_f32_16x16x32_bf16 a[4:7], a[152:155], a[88:91], a[4:7]\n"
"buffer_load_dwordx4 a[176:179], v216, s[60:63], s84 offen offset:1024 sc1\n"
"v_mfma_f32_16x16x32_bf16 a[8:11], a[156:159], a[88:91], a[8:11]\n"
"v_accvgpr_write_b32 a75, 0\n"
"v_accvgpr_write_b32 a102, 0\n"
"v_mfma_f32_16x16x32_bf16 a[12:15], a[160:163], a[88:91], a[12:15]\n"
"v_accvgpr_write_b32 a103, 0\n"
"v_accvgpr_write_b32 a146, 0\n"
"v_mfma_f32_16x16x32_bf16 a[32:35], a[164:167], a[88:91], a[32:35]\n"
"s_waitcnt vmcnt(9) lgkmcnt(5)\n"
"buffer_load_dwordx2 a[72:73], v238, s[60:63], s84 offen offset:2048 sc1\n"
"v_mfma_f32_16x16x32_bf16 v[36:39], a[36:39], a[92:95], v[36:39]\n"
"buffer_load_dwordx4 a[76:79], v216, s[60:63], s85 offen sc1\n"
"v_mfma_f32_16x16x32_bf16 v[96:99], a[40:43], a[92:95], v[96:99]\n"
"buffer_load_dwordx4 a[96:99], v216, s[60:63], s85 offen offset:1024 sc1\n"
"buffer_load_dwordx2 a[100:101], v238, s[60:63], s85 offen offset:2048 sc1\n"
"buffer_load_dwordx4 a[104:107], v216, s[60:63], s86 offen sc1\n"
"v_mfma_f32_16x16x32_bf16 v[100:103], a[44:47], a[92:95], v[100:103]\n"
"ds_read_b128 a[148:151], v229 offset:51200\n"
"v_accvgpr_write_b32 a147, 0\n"
"v_mfma_f32_16x16x32_bf16 v[40:43], a[64:67], a[92:95], v[40:43]\n"
"ds_read_b128 a[152:155], v229 offset:52224\n"
"v_mfma_f32_16x16x32_bf16 v[44:47], a[68:71], a[92:95], v[44:47]\n"
"s_waitcnt vmcnt(13) lgkmcnt(4)\n"
"buffer_load_dwordx4 a[108:111], v216, s[60:63], s86 offen offset:1024 sc1\n"
"v_mfma_f32_16x16x32_bf16 a[0:3], a[36:39], a[112:115], a[0:3]\n"
"ds_read_b128 a[156:159], v229 offset:53248\n"
"v_mfma_f32_16x16x32_bf16 a[4:7], a[40:43], a[112:115], a[4:7]\n"
"ds_read_b128 a[160:163], v229 offset:54272\n"
"v_mfma_f32_16x16x32_bf16 a[8:11], a[44:47], a[112:115], a[8:11]\n"
"ds_read_b128 a[164:167], v229 offset:55296\n"
"v_mfma_f32_16x16x32_bf16 a[12:15], a[64:67], a[112:115], a[12:15]\n"
"buffer_load_dwordx2 a[144:145], v238, s[60:63], s86 offen offset:2048 sc1\n"
"v_mfma_f32_16x16x32_bf16 a[32:35], a[68:71], a[112:115], a[32:35]\n"
"s_waitcnt vmcnt(13) lgkmcnt(5)\n"
"s_barrier\n"
"ds_read_b128 a[36:39], v219 offset:0\n"
"v_mfma_f32_16x16x32_bf16 a[0:3], a[180:183], a[120:123], a[0:3]\n"
"ds_read_b128 a[40:43], v219 offset:1024\n"
"v_mfma_f32_16x16x32_bf16 a[4:7], a[184:187], a[120:123], a[4:7]\n"
"ds_read_b128 a[44:47], v219 offset:2048\n"
"v_mfma_f32_16x16x32_bf16 a[8:11], a[188:191], a[120:123], a[8:11]\n"
"ds_read_b128 a[64:67], v219 offset:3072\n"
"v_mfma_f32_16x16x32_bf16 a[12:15], a[192:195], a[120:123], a[12:15]\n"
"ds_read_b128 a[68:71], v219 offset:4096\n"
"v_mfma_f32_16x16x32_bf16 a[32:35], a[196:199], a[120:123], a[32:35]\n"
"s_waitcnt vmcnt(11) lgkmcnt(9)\n"
"v_mfma_f32_16x16x32_bf16 a[0:3], a[148:151], a[204:207], a[0:3]\n"
"s_waitcnt lgkmcnt(8)\n"
"v_mfma_f32_16x16x32_bf16 a[4:7], a[152:155], a[204:207], a[4:7]\n"
"s_waitcnt lgkmcnt(7)\n"
"v_mfma_f32_16x16x32_bf16 a[8:11], a[156:159], a[204:207], a[8:11]\n"
"s_waitcnt lgkmcnt(6)\n"
"v_mfma_f32_16x16x32_bf16 a[12:15], a[160:163], a[204:207], a[12:15]\n"
"s_waitcnt lgkmcnt(5)\n"
"v_mfma_f32_16x16x32_bf16 a[32:35], a[164:167], a[204:207], a[32:35]\n"
"s_waitcnt vmcnt(9) lgkmcnt(4)\n"
"v_mfma_f32_16x16x32_bf16 a[0:3], a[36:39], a[168:171], a[0:3]\n"
"s_waitcnt lgkmcnt(3)\n"
"v_mfma_f32_16x16x32_bf16 a[4:7], a[40:43], a[168:171], a[4:7]\n"
"s_waitcnt lgkmcnt(2)\n"
"v_mfma_f32_16x16x32_bf16 a[8:11], a[44:47], a[168:171], a[8:11]\n"
"s_waitcnt lgkmcnt(1)\n"
"v_mfma_f32_16x16x32_bf16 a[12:15], a[64:67], a[168:171], a[12:15]\n"
"s_waitcnt lgkmcnt(0)\n"
"v_mfma_f32_16x16x32_bf16 a[32:35], a[68:71], a[168:171], a[32:35]\n"
"ds_write_b128 v234, a[0:3] offset:0\n"
"v_mfma_f32_16x16x32_bf16 v[36:39], a[180:183], a[116:119], v[36:39]\n"
"ds_write_b128 v234, a[4:7] offset:1024\n"
"v_mfma_f32_16x16x32_bf16 v[96:99], a[184:187], a[116:119], v[96:99]\n"
"ds_write_b128 v234, a[8:11] offset:2048\n"
"v_mfma_f32_16x16x32_bf16 v[100:103], a[188:191], a[116:119], v[100:103]\n"
"ds_write_b128 v234, a[12:15] offset:3072\n"
"v_mfma_f32_16x16x32_bf16 v[40:43], a[192:195], a[116:119], v[40:43]\n"
"ds_write_b128 v234, a[32:35] offset:4096\n"
"v_mfma_f32_16x16x32_bf16 v[44:47], a[196:199], a[116:119], v[44:47]\n"
"v_mfma_f32_16x16x32_bf16 v[36:39], a[148:151], a[220:223], v[36:39]\n"
"v_mfma_f32_16x16x32_bf16 v[96:99], a[152:155], a[220:223], v[96:99]\n"
"v_mfma_f32_16x16x32_bf16 v[100:103], a[156:159], a[220:223], v[100:103]\n"
"v_mfma_f32_16x16x32_bf16 v[40:43], a[160:163], a[220:223], v[40:43]\n"
"v_mfma_f32_16x16x32_bf16 v[44:47], a[164:167], a[220:223], v[44:47]\n"
"v_mfma_f32_16x16x32_bf16 v[36:39], a[36:39], a[208:211], v[36:39]\n"
"v_mfma_f32_16x16x32_bf16 v[96:99], a[40:43], a[208:211], v[96:99]\n"
"v_mfma_f32_16x16x32_bf16 v[100:103], a[44:47], a[208:211], v[100:103]\n"
"s_waitcnt lgkmcnt(0)\n"
"s_barrier\n"
"ds_read_b128 v[188:191], v235 offset:0\n"
"v_mfma_f32_16x16x32_bf16 v[40:43], a[64:67], a[208:211], v[40:43]\n"
"ds_read_b128 v[192:195], v235 offset:1024\n"
"v_mfma_f32_16x16x32_bf16 v[44:47], a[68:71], a[208:211], v[44:47]\n"
"s_waitcnt vmcnt(8)\n"
"ds_read_b128 v[196:199], v235 offset:2048\n"
"v_mfma_f32_16x16x32_bf16 v[152:155], v[224:227], a[172:175], v[152:155]\n"
"s_waitcnt vmcnt(5)\n"
"ds_read_b128 v[48:51], v235 offset:3072\n"
"v_mfma_f32_16x16x32_bf16 v[152:155], v[224:227], a[76:79], v[152:155]\n"
"s_waitcnt vmcnt(2)\n"
"ds_read_b128 v[52:55], v235 offset:4096\n"
"v_mfma_f32_16x16x32_bf16 v[152:155], v[224:227], a[104:107], v[152:155]\n"
"v_mfma_f32_16x16x32_bf16 v[156:159], v[222:225], a[172:175], v[156:159]\n"
"v_mfma_f32_16x16x32_bf16 v[156:159], v[222:225], a[76:79], v[156:159]\n"
"v_mfma_f32_16x16x32_bf16 v[156:159], v[222:225], a[104:107], v[156:159]\n"
"v_mfma_f32_16x16x32_bf16 v[160:163], v[224:227], a[176:179], v[160:163]\n"
"s_waitcnt lgkmcnt(4)\n"
"v_add_f32 v36, v36, v188\n"
"v_add_f32 v37, v37, v189\n"
"v_mfma_f32_16x16x32_bf16 v[160:163], v[224:227], a[96:99], v[160:163]\n"
"s_waitcnt vmcnt(1)\n"
"v_add_f32 v38, v38, v190\n"
"v_add_f32 v39, v39, v191\n"
"v_mfma_f32_16x16x32_bf16 v[160:163], v[224:227], a[108:111], v[160:163]\n"
"s_waitcnt lgkmcnt(3)\n"
"v_add_f32 v96, v96, v192\n"
"v_add_f32 v97, v97, v193\n"
"v_mfma_f32_16x16x32_bf16 v[164:167], v[222:225], a[176:179], v[164:167]\n"
"v_add_f32 v98, v98, v194\n"
"v_add_f32 v99, v99, v195\n"
"v_mfma_f32_16x16x32_bf16 v[164:167], v[222:225], a[96:99], v[164:167]\n"
"v_mul_f32 v188, s0, v36\n"
"v_mul_f32 v189, s0, v37\n"
"v_mfma_f32_16x16x32_bf16 v[164:167], v[222:225], a[108:111], v[164:167]\n"
"v_mul_f32 v190, s0, v38\n"
"v_mul_f32 v191, s0, v39\n"
"v_mfma_f32_16x16x32_bf16 v[168:171], v[224:227], a[72:75], v[168:171]\n"
"v_mul_f32 v192, s0, v96\n"
"v_mul_f32 v193, s0, v97\n"
"v_mfma_f32_16x16x32_bf16 v[168:171], v[224:227], a[100:103], v[168:171]\n"
"s_waitcnt vmcnt(0) lgkmcnt(0)\n"
"v_mul_f32 v194, s0, v98\n"
"v_mul_f32 v195, s0, v99\n"
"v_mfma_f32_16x16x32_bf16 v[168:171], v[224:227], a[144:147], v[168:171]\n"
"v_exp_f32 v188, v188\n"
"v_exp_f32 v189, v189\n"
"v_exp_f32 v190, v190\n"
"v_exp_f32 v191, v191\n"
"v_exp_f32 v192, v192\n"
"v_exp_f32 v193, v193\n"
"v_exp_f32 v194, v194\n"
"v_exp_f32 v195, v195\n"
"v_add_f32 v188, 1.0, v188\n"
"v_add_f32 v189, 1.0, v189\n"
"v_add_f32 v190, 1.0, v190\n"
"v_add_f32 v191, 1.0, v191\n"
"v_add_f32 v192, 1.0, v192\n"
"v_add_f32 v193, 1.0, v193\n"
"v_add_f32 v194, 1.0, v194\n"
"v_add_f32 v195, 1.0, v195\n"
"v_rcp_f32 v188, v188\n"
"v_rcp_f32 v189, v189\n"
"v_rcp_f32 v190, v190\n"
"v_rcp_f32 v191, v191\n"
"v_rcp_f32 v192, v192\n"
"v_rcp_f32 v193, v193\n"
"v_rcp_f32 v194, v194\n"
"v_rcp_f32 v195, v195\n"
"v_mul_f32 v36, v36, v188\n"
"v_mul_f32 v37, v37, v189\n"
"v_mul_f32 v38, v38, v190\n"
"v_mul_f32 v39, v39, v191\n"
"v_mul_f32 v96, v96, v192\n"
"v_mul_f32 v97, v97, v193\n"
"v_mul_f32 v98, v98, v194\n"
"v_mul_f32 v99, v99, v195\n"
"v_fmac_f32 v104, v36, v152\n"
"v_fmac_f32 v105, v37, v153\n"
"v_fmac_f32 v106, v38, v154\n"
"v_fmac_f32 v107, v39, v155\n"
"v_fmac_f32 v108, v96, v156\n"
"v_fmac_f32 v109, v97, v157\n"
"v_fmac_f32 v110, v98, v158\n"
"v_fmac_f32 v111, v99, v159\n"
"v_cvt_pk_bf16_f32 v242, v104, v105\n"
"v_cvt_pk_bf16_f32 v243, v106, v107\n"
"v_cvt_pk_bf16_f32 v244, v108, v109\n"
"v_cvt_pk_bf16_f32 v245, v110, v111\n"
"buffer_store_dwordx4 v[242:245], v230, s[64:67], s45 offen\n"
"v_add_f32 v100, v100, v196\n"
"v_add_f32 v101, v101, v197\n"
"v_add_f32 v102, v102, v198\n"
"v_add_f32 v103, v103, v199\n"
"v_add_f32 v40, v40, v48\n"
"v_add_f32 v41, v41, v49\n"
"v_add_f32 v42, v42, v50\n"
"v_add_f32 v43, v43, v51\n"
"v_mul_f32 v196, s0, v100\n"
"v_mul_f32 v197, s0, v101\n"
"v_mul_f32 v198, s0, v102\n"
"v_mul_f32 v199, s0, v103\n"
"v_mul_f32 v48, s0, v40\n"
"v_mul_f32 v49, s0, v41\n"
"v_mul_f32 v50, s0, v42\n"
"v_mul_f32 v51, s0, v43\n"
"v_exp_f32 v196, v196\n"
"v_exp_f32 v197, v197\n"
"v_exp_f32 v198, v198\n"
"v_exp_f32 v199, v199\n"
"v_exp_f32 v48, v48\n"
"v_exp_f32 v49, v49\n"
"v_exp_f32 v50, v50\n"
"v_exp_f32 v51, v51\n"
"v_add_f32 v196, 1.0, v196\n"
"v_add_f32 v197, 1.0, v197\n"
"v_add_f32 v198, 1.0, v198\n"
"v_add_f32 v199, 1.0, v199\n"
"v_add_f32 v48, 1.0, v48\n"
"v_add_f32 v49, 1.0, v49\n"
"v_add_f32 v50, 1.0, v50\n"
"v_add_f32 v51, 1.0, v51\n"
"v_rcp_f32 v196, v196\n"
"v_rcp_f32 v197, v197\n"
"v_rcp_f32 v198, v198\n"
"v_rcp_f32 v199, v199\n"
"v_rcp_f32 v48, v48\n"
"v_rcp_f32 v49, v49\n"
"v_rcp_f32 v50, v50\n"
"v_rcp_f32 v51, v51\n"
"v_mul_f32 v100, v100, v196\n"
"v_mul_f32 v101, v101, v197\n"
"v_mul_f32 v102, v102, v198\n"
"v_mul_f32 v103, v103, v199\n"
"v_mul_f32 v40, v40, v48\n"
"v_mul_f32 v41, v41, v49\n"
"v_mul_f32 v42, v42, v50\n"
"v_mul_f32 v43, v43, v51\n"
"v_fmac_f32 v176, v100, v160\n"
"v_fmac_f32 v177, v101, v161\n"
"v_fmac_f32 v178, v102, v162\n"
"v_fmac_f32 v179, v103, v163\n"
"v_fmac_f32 v180, v40, v164\n"
"v_fmac_f32 v181, v41, v165\n"
"v_fmac_f32 v182, v42, v166\n"
"v_fmac_f32 v183, v43, v167\n"
"v_cvt_pk_bf16_f32 v172, v176, v177\n"
"v_cvt_pk_bf16_f32 v173, v178, v179\n"
"v_cvt_pk_bf16_f32 v174, v180, v181\n"
"v_cvt_pk_bf16_f32 v175, v182, v183\n"
"buffer_store_dwordx4 v[172:175], v230, s[64:67], s45 offen offset:64\n"
"v_add_f32 v44, v44, v52\n"
"v_add_f32 v45, v45, v53\n"
"v_add_f32 v46, v46, v54\n"
"v_add_f32 v47, v47, v55\n"
"v_mul_f32 v52, s0, v44\n"
"v_mul_f32 v53, s0, v45\n"
"v_mul_f32 v54, s0, v46\n"
"v_mul_f32 v55, s0, v47\n"
"v_exp_f32 v52, v52\n"
"v_exp_f32 v53, v53\n"
"v_exp_f32 v54, v54\n"
"v_exp_f32 v55, v55\n"
"v_add_f32 v52, 1.0, v52\n"
"v_add_f32 v53, 1.0, v53\n"
"v_add_f32 v54, 1.0, v54\n"
"v_add_f32 v55, 1.0, v55\n"
"v_rcp_f32 v52, v52\n"
"v_rcp_f32 v53, v53\n"
"v_rcp_f32 v54, v54\n"
"v_rcp_f32 v55, v55\n"
"v_mul_f32 v44, v44, v52\n"
"v_mul_f32 v45, v45, v53\n"
"v_mul_f32 v46, v46, v54\n"
"v_mul_f32 v47, v47, v55\n"
"v_fmac_f32 v184, v44, v168\n"
"v_fmac_f32 v185, v45, v169\n"
"v_fmac_f32 v186, v46, v170\n"
"v_fmac_f32 v187, v47, v171\n"
"v_cvt_pk_bf16_f32 v236, v184, v185\n"
"v_cvt_pk_bf16_f32 v237, v186, v187\n"
"buffer_store_dwordx2 v[236:237], v230, s[64:67], s46 offen offset:128\n"
"s_nop 0\n"
"s_waitcnt vmcnt(0) lgkmcnt(0)\n"
"s_endpgm\n"
"L_spinG_%=:\n"
"s_mov_b32 s2, 400000\n"
"L_spinlG_%=:\n"
"s_sleep 4\n"
"s_xor_b32 s4, s26, 0\n"
"s_lshl_b32 s4, s4, 10\n"
"s_add_u32 s4, s4, 262144\n"
"buffer_load_dwordx2 v[240:241], v239, s[60:63], s4 offen sc1\n"
"s_xor_b32 s5, s26, 1\n"
"s_lshl_b32 s5, s5, 10\n"
"s_add_u32 s5, s5, 262144\n"
"buffer_load_dwordx2 v[242:243], v239, s[60:63], s5 offen sc1\n"
"s_xor_b32 s6, s26, 2\n"
"s_lshl_b32 s6, s6, 10\n"
"s_add_u32 s6, s6, 262144\n"
"buffer_load_dwordx2 v[244:245], v239, s[60:63], s6 offen sc1\n"
"s_xor_b32 s7, s26, 3\n"
"s_lshl_b32 s7, s7, 10\n"
"s_add_u32 s7, s7, 262144\n"
"buffer_load_dwordx2 v[246:247], v239, s[60:63], s7 offen sc1\n"
"s_waitcnt vmcnt(0)\n"
"v_cmp_eq_u32_e64 s[4:5], v240, s80\n"
"v_cmp_eq_u32_e64 s[6:7], v242, s80\n"
"s_nop 1\n"
"s_and_b64 s[4:5], s[4:5], s[6:7]\n"
"v_cmp_eq_u32_e64 s[6:7], v244, s80\n"
"s_nop 1\n"
"s_and_b64 s[4:5], s[4:5], s[6:7]\n"
"v_cmp_eq_u32_e64 s[6:7], v246, s80\n"
"s_nop 1\n"
"s_and_b64 s[4:5], s[4:5], s[6:7]\n"
"s_cmp_eq_u64 s[4:5], -1\n"
"s_cbranch_scc1 L_retG_%=\n"
"s_sub_u32 s2, s2, 1\n"
"s_cmp_lg_u32 s2, 0\n"
"s_cbranch_scc1 L_spinlG_%=\n"
"s_branch L_retG_%=\n"
"L_spinX_%=:\n"
"s_mov_b32 s2, 400000\n"
"L_spinlX_%=:\n"
"s_sleep 4\n"
"buffer_load_dwordx2 v[240:241], v239, s[60:63], s3 offen sc1\n"
"s_waitcnt vmcnt(0)\n"
"v_cmp_eq_u64_e64 s[4:5], v[240:241], s[80:81]\n"
"s_nop 1\n"
"s_cmp_eq_u64 s[4:5], -1\n"
"s_cbranch_scc1 L_retX_%=\n"
"s_sub_u32 s2, s2, 1\n"
"s_cmp_lg_u32 s2, 0\n"
"s_cbranch_scc1 L_spinlX_%=\n"
"s_branch L_retX_%=\n"
"L_slow_%=:\n"
"s_waitcnt vmcnt(0)\n"
"s_nop 15\n"
"s_nop 7\n"
"v_bfe_u32 v246, v253, 2, 2\n"
"v_and_b32 v247, 3, v253\n"
"v_lshl_add_u32 v246, v246, 3, v247\n"
"v_lshrrev_b32 v247, 4, v253\n"
"v_lshlrev_b32 v244, 8, v246\n"
"v_lshl_add_u32 v244, v247, 4, v244\n"
"v_lshlrev_b32 v245, 11, v247\n"
"v_lshl_add_u32 v245, v246, 1, v245\n"
"v_mov_b32 v240, 0xff800000\n"
"v_mov_b32 v241, 0xff800000\n"
"v_mov_b32 v242, 0xff800000\n"
"v_mov_b32 v243, 0xff800000\n"
"s_mov_b32 s3, 0\n"
"s_waitcnt vmcnt(0) lgkmcnt(0)\n"
"s_nop 15\n"
"L_sl1_%=:\n"
"s_nop 7\n"
"buffer_load_dwordx4 v[68:71], v244, s[44:47], s3 offen\n"
"buffer_load_dwordx4 v[76:79], v244, s[44:47], s3 offen offset:64\n"
"buffer_load_dwordx4 v[84:87], v244, s[44:47], s3 offen offset:128\n"
"buffer_load_dwordx4 v[92:95], v244, s[44:47], s3 offen offset:192\n"
"buffer_load_dwordx4 v[100:103], v244, s[44:47], s3 offen offset:1024\n"
"buffer_load_dwordx4 v[108:111], v244, s[44:47], s3 offen offset:1088\n"
"buffer_load_dwordx4 v[116:119], v244, s[44:47], s3 offen offset:1152\n"
"buffer_load_dwordx4 v[124:127], v244, s[44:47], s3 offen offset:1216\n"
"s_waitcnt vmcnt(7)\n"
"v_mfma_f32_16x16x32_bf16 v[4:7], v[68:71], v[0:3], 0\n"
"s_waitcnt vmcnt(6)\n"
"v_mfma_f32_16x16x32_bf16 v[4:7], v[76:79], v[32:35], v[4:7]\n"
"s_waitcnt vmcnt(5)\n"
"v_mfma_f32_16x16x32_bf16 v[4:7], v[84:87], v[64:67], v[4:7]\n"
"s_waitcnt vmcnt(4)\n"
"v_mfma_f32_16x16x32_bf16 v[4:7], v[92:95], v[96:99], v[4:7]\n"
"s_waitcnt vmcnt(3)\n"
"v_mfma_f32_16x16x32_bf16 v[36:39], v[100:103], v[0:3], 0\n"
"s_waitcnt vmcnt(2)\n"
"v_mfma_f32_16x16x32_bf16 v[36:39], v[108:111], v[32:35], v[36:39]\n"
"s_waitcnt vmcnt(1)\n"
"v_mfma_f32_16x16x32_bf16 v[36:39], v[116:119], v[64:67], v[36:39]\n"
"s_waitcnt vmcnt(0)\n"
"v_mfma_f32_16x16x32_bf16 v[36:39], v[124:127], v[96:99], v[36:39]\n"
"v_mfma_f32_16x16x32_bf16 v[12:15], v[68:71], v[8:11], 0\n"
"v_mfma_f32_16x16x32_bf16 v[12:15], v[76:79], v[40:43], v[12:15]\n"
"v_mfma_f32_16x16x32_bf16 v[12:15], v[84:87], v[72:75], v[12:15]\n"
"v_mfma_f32_16x16x32_bf16 v[12:15], v[92:95], v[104:107], v[12:15]\n"
"v_mfma_f32_16x16x32_bf16 v[44:47], v[100:103], v[8:11], 0\n"
"v_mfma_f32_16x16x32_bf16 v[44:47], v[108:111], v[40:43], v[44:47]\n"
"v_mfma_f32_16x16x32_bf16 v[44:47], v[116:119], v[72:75], v[44:47]\n"
"v_mfma_f32_16x16x32_bf16 v[44:47], v[124:127], v[104:107], v[44:47]\n"
"v_mfma_f32_16x16x32_bf16 v[20:23], v[68:71], v[16:19], 0\n"
"v_mfma_f32_16x16x32_bf16 v[20:23], v[76:79], v[48:51], v[20:23]\n"
"v_mfma_f32_16x16x32_bf16 v[20:23], v[84:87], v[80:83], v[20:23]\n"
"v_mfma_f32_16x16x32_bf16 v[20:23], v[92:95], v[112:115], v[20:23]\n"
"v_mfma_f32_16x16x32_bf16 v[52:55], v[100:103], v[16:19], 0\n"
"v_mfma_f32_16x16x32_bf16 v[52:55], v[108:111], v[48:51], v[52:55]\n"
"v_mfma_f32_16x16x32_bf16 v[52:55], v[116:119], v[80:83], v[52:55]\n"
"v_mfma_f32_16x16x32_bf16 v[52:55], v[124:127], v[112:115], v[52:55]\n"
"v_mfma_f32_16x16x32_bf16 v[28:31], v[68:71], v[24:27], 0\n"
"v_mfma_f32_16x16x32_bf16 v[28:31], v[76:79], v[56:59], v[28:31]\n"
"v_mfma_f32_16x16x32_bf16 v[28:31], v[84:87], v[88:91], v[28:31]\n"
"v_mfma_f32_16x16x32_bf16 v[28:31], v[92:95], v[120:123], v[28:31]\n"
"v_mfma_f32_16x16x32_bf16 v[60:63], v[100:103], v[24:27], 0\n"
"v_mfma_f32_16x16x32_bf16 v[60:63], v[108:111], v[56:59], v[60:63]\n"
"v_mfma_f32_16x16x32_bf16 v[60:63], v[116:119], v[88:91], v[60:63]\n"
"v_mfma_f32_16x16x32_bf16 v[60:63], v[124:127], v[120:123], v[60:63]\n"
"v_max3_f32 v240, v240, v4, v5\n"
"v_max3_f32 v240, v240, v6, v7\n"
"v_max3_f32 v240, v240, v36, v37\n"
"v_max3_f32 v240, v240, v38, v39\n"
"v_max3_f32 v241, v241, v12, v13\n"
"v_max3_f32 v241, v241, v14, v15\n"
"v_max3_f32 v241, v241, v44, v45\n"
"v_max3_f32 v241, v241, v46, v47\n"
"v_max3_f32 v242, v242, v20, v21\n"
"v_max3_f32 v242, v242, v22, v23\n"
"v_max3_f32 v242, v242, v52, v53\n"
"v_max3_f32 v242, v242, v54, v55\n"
"v_max3_f32 v243, v243, v28, v29\n"
"v_max3_f32 v243, v243, v30, v31\n"
"v_max3_f32 v243, v243, v60, v61\n"
"v_max3_f32 v243, v243, v62, v63\n"
"s_waitcnt vmcnt(0) lgkmcnt(0)\n"
"s_nop 15\n"
"s_add_u32 s3, s3, 8192\n"
"s_cmp_lt_u32 s3, 131072\n"
"s_cbranch_scc1 L_sl1_%=\n"
"s_nop 7\n"
"v_mov_b32 v246, v240\n"
"s_nop 1\n"
"v_permlane16_swap_b32 v240, v246\n"
"v_max_f32 v240, v240, v246\n"
"v_mov_b32 v246, v240\n"
"s_nop 1\n"
"v_permlane32_swap_b32 v240, v246\n"
"v_max_f32 v240, v240, v246\n"
"v_sub_f32 v160, 0, v240\n"
"v_sub_f32 v161, 0, v240\n"
"v_sub_f32 v162, 0, v240\n"
"v_sub_f32 v163, 0, v240\n"
"v_mov_b32 v246, v241\n"
"s_nop 1\n"
"v_permlane16_swap_b32 v241, v246\n"
"v_max_f32 v241, v241, v246\n"
"v_mov_b32 v246, v241\n"
"s_nop 1\n"
"v_permlane32_swap_b32 v241, v246\n"
"v_max_f32 v241, v241, v246\n"
"v_sub_f32 v164, 0, v241\n"
"v_sub_f32 v165, 0, v241\n"
"v_sub_f32 v166, 0, v241\n"
"v_sub_f32 v167, 0, v241\n"
"v_mov_b32 v246, v242\n"
"s_nop 1\n"
"v_permlane16_swap_b32 v242, v246\n"
"v_max_f32 v242, v242, v246\n"
"v_mov_b32 v246, v242\n"
"s_nop 1\n"
"v_permlane32_swap_b32 v242, v246\n"
"v_max_f32 v242, v242, v246\n"
"v_sub_f32 v168, 0, v242\n"
"v_sub_f32 v169, 0, v242\n"
"v_sub_f32 v170, 0, v242\n"
"v_sub_f32 v171, 0, v242\n"
"v_mov_b32 v246, v243\n"
"s_nop 1\n"
"v_permlane16_swap_b32 v243, v246\n"
"v_max_f32 v243, v243, v246\n"
"v_mov_b32 v246, v243\n"
"s_nop 1\n"
"v_permlane32_swap_b32 v243, v246\n"
"v_max_f32 v243, v243, v246\n"
"v_sub_f32 v172, 0, v243\n"
"v_sub_f32 v173, 0, v243\n"
"v_sub_f32 v174, 0, v243\n"
"v_sub_f32 v175, 0, v243\n"
"v_accvgpr_write_b32 a0, 0\n"
"v_accvgpr_write_b32 a1, 0\n"
"v_accvgpr_write_b32 a2, 0\n"
"v_accvgpr_write_b32 a3, 0\n"
"v_accvgpr_write_b32 a4, 0\n"
"v_accvgpr_write_b32 a5, 0\n"
"v_accvgpr_write_b32 a6, 0\n"
"v_accvgpr_write_b32 a7, 0\n"
"v_accvgpr_write_b32 a8, 0\n"
"v_accvgpr_write_b32 a9, 0\n"
"v_accvgpr_write_b32 a10, 0\n"
"v_accvgpr_write_b32 a11, 0\n"
"v_accvgpr_write_b32 a12, 0\n"
"v_accvgpr_write_b32 a13, 0\n"
"v_accvgpr_write_b32 a14, 0\n"
"v_accvgpr_write_b32 a15, 0\n"
"v_accvgpr_write_b32 a16, 0\n"
"v_accvgpr_write_b32 a17, 0\n"
"v_accvgpr_write_b32 a18, 0\n"
"v_accvgpr_write_b32 a19, 0\n"
"v_accvgpr_write_b32 a20, 0\n"
"v_accvgpr_write_b32 a21, 0\n"
"v_accvgpr_write_b32 a22, 0\n"
"v_accvgpr_write_b32 a23, 0\n"
"v_accvgpr_write_b32 a24, 0\n"
"v_accvgpr_write_b32 a25, 0\n"
"v_accvgpr_write_b32 a26, 0\n"
"v_accvgpr_write_b32 a27, 0\n"
"v_accvgpr_write_b32 a28, 0\n"
"v_accvgpr_write_b32 a29, 0\n"
"v_accvgpr_write_b32 a30, 0\n"
"v_accvgpr_write_b32 a31, 0\n"
"v_accvgpr_write_b32 a32, 0\n"
"v_accvgpr_write_b32 a33, 0\n"
"v_accvgpr_write_b32 a34, 0\n"
"v_accvgpr_write_b32 a35, 0\n"
"v_accvgpr_write_b32 a36, 0\n"
"v_accvgpr_write_b32 a37, 0\n"
"v_accvgpr_write_b32 a38, 0\n"
"v_accvgpr_write_b32 a39, 0\n"
"v_accvgpr_write_b32 a40, 0\n"
"v_accvgpr_write_b32 a41, 0\n"
"v_accvgpr_write_b32 a42, 0\n"
"v_accvgpr_write_b32 a43, 0\n"
"v_accvgpr_write_b32 a44, 0\n"
"v_accvgpr_write_b32 a45, 0\n"
"v_accvgpr_write_b32 a46, 0\n"
"v_accvgpr_write_b32 a47, 0\n"
"v_accvgpr_write_b32 a48, 0\n"
"v_accvgpr_write_b32 a49, 0\n"
"v_accvgpr_write_b32 a50, 0\n"
"v_accvgpr_write_b32 a51, 0\n"
"v_accvgpr_write_b32 a52, 0\n"
"v_accvgpr_write_b32 a53, 0\n"
"v_accvgpr_write_b32 a54, 0\n"
"v_accvgpr_write_b32 a55, 0\n"
"v_accvgpr_write_b32 a56, 0\n"
"v_accvgpr_write_b32 a57, 0\n"
"v_accvgpr_write_b32 a58, 0\n"
"v_accvgpr_write_b32 a59, 0\n"
"v_accvgpr_write_b32 a60, 0\n"
"v_accvgpr_write_b32 a61, 0\n"
"v_accvgpr_write_b32 a62, 0\n"
"v_accvgpr_write_b32 a63, 0\n"
"v_accvgpr_write_b32 a64, 0\n"
"v_accvgpr_write_b32 a65, 0\n"
"v_accvgpr_write_b32 a66, 0\n"
"v_accvgpr_write_b32 a67, 0\n"
"v_accvgpr_write_b32 a68, 0\n"
"v_accvgpr_write_b32 a69, 0\n"
"v_accvgpr_write_b32 a70, 0\n"
"v_accvgpr_write_b32 a71, 0\n"
"v_accvgpr_write_b32 a72, 0\n"
"v_accvgpr_write_b32 a73, 0\n"
"v_accvgpr_write_b32 a74, 0\n"
"v_accvgpr_write_b32 a75, 0\n"
"v_accvgpr_write_b32 a76, 0\n"
"v_accvgpr_write_b32 a77, 0\n"
"v_accvgpr_write_b32 a78, 0\n"
"v_accvgpr_write_b32 a79, 0\n"
"v_accvgpr_write_b32 a80, 0\n"
"v_accvgpr_write_b32 a81, 0\n"
"v_accvgpr_write_b32 a82, 0\n"
"v_accvgpr_write_b32 a83, 0\n"
"v_accvgpr_write_b32 a84, 0\n"
"v_accvgpr_write_b32 a85, 0\n"
"v_accvgpr_write_b32 a86, 0\n"
"v_accvgpr_write_b32 a87, 0\n"
"v_accvgpr_write_b32 a88, 0\n"
"v_accvgpr_write_b32 a89, 0\n"
"v_accvgpr_write_b32 a90, 0\n"
"v_accvgpr_write_b32 a91, 0\n"
"v_accvgpr_write_b32 a92, 0\n"
"v_accvgpr_write_b32 a93, 0\n"
"v_accvgpr_write_b32 a94, 0\n"
"v_accvgpr_write_b32 a95, 0\n"
"v_accvgpr_write_b32 a96, 0\n"
"v_accvgpr_write_b32 a97, 0\n"
"v_accvgpr_write_b32 a98, 0\n"
"v_accvgpr_write_b32 a99, 0\n"
"v_accvgpr_write_b32 a100, 0\n"
"v_accvgpr_write_b32 a101, 0\n"
"v_accvgpr_write_b32 a102, 0\n"
"v_accvgpr_write_b32 a103, 0\n"
"v_accvgpr_write_b32 a104, 0\n"
"v_accvgpr_write_b32 a105, 0\n"
"v_accvgpr_write_b32 a106, 0\n"
"v_accvgpr_write_b32 a107, 0\n"
"v_accvgpr_write_b32 a108, 0\n"
"v_accvgpr_write_b32 a109, 0\n"
"v_accvgpr_write_b32 a110, 0\n"
"v_accvgpr_write_b32 a111, 0\n"
"v_accvgpr_write_b32 a112, 0\n"
"v_accvgpr_write_b32 a113, 0\n"
"v_accvgpr_write_b32 a114, 0\n"
"v_accvgpr_write_b32 a115, 0\n"
"v_accvgpr_write_b32 a116, 0\n"
"v_accvgpr_write_b32 a117, 0\n"
"v_accvgpr_write_b32 a118, 0\n"
"v_accvgpr_write_b32 a119, 0\n"
"v_accvgpr_write_b32 a120, 0\n"
"v_accvgpr_write_b32 a121, 0\n"
"v_accvgpr_write_b32 a122, 0\n"
"v_accvgpr_write_b32 a123, 0\n"
"v_accvgpr_write_b32 a124, 0\n"
"v_accvgpr_write_b32 a125, 0\n"
"v_accvgpr_write_b32 a126, 0\n"
"v_accvgpr_write_b32 a127, 0\n"
"v_accvgpr_write_b32 a128, 0\n"
"v_accvgpr_write_b32 a129, 0\n"
"v_accvgpr_write_b32 a130, 0\n"
"v_accvgpr_write_b32 a131, 0\n"
"v_accvgpr_write_b32 a132, 0\n"
"v_accvgpr_write_b32 a133, 0\n"
"v_accvgpr_write_b32 a134, 0\n"
"v_accvgpr_write_b32 a135, 0\n"
"v_accvgpr_write_b32 a136, 0\n"
"v_accvgpr_write_b32 a137, 0\n"
"v_accvgpr_write_b32 a138, 0\n"
"v_accvgpr_write_b32 a139, 0\n"
"v_accvgpr_write_b32 a140, 0\n"
"v_accvgpr_write_b32 a141, 0\n"
"v_accvgpr_write_b32 a142, 0\n"
"v_accvgpr_write_b32 a143, 0\n"
"s_mov_b32 s3, 0\n"
"s_waitcnt vmcnt(0) lgkmcnt(0)\n"
"s_nop 15\n"
"L_sl2_%=:\n"
"s_nop 7\n"
"buffer_load_dwordx4 v[68:71], v244, s[44:47], s3 offen\n"
"buffer_load_dwordx4 v[76:79], v244, s[44:47], s3 offen offset:64\n"
"buffer_load_dwordx4 v[84:87], v244, s[44:47], s3 offen offset:128\n"
"buffer_load_dwordx4 v[92:95], v244, s[44:47], s3 offen offset:192\n"
"buffer_load_dwordx4 v[100:103], v244, s[44:47], s3 offen offset:1024\n"
"buffer_load_dwordx4 v[108:111], v244, s[44:47], s3 offen offset:1088\n"
"buffer_load_dwordx4 v[116:119], v244, s[44:47], s3 offen offset:1152\n"
"buffer_load_dwordx4 v[124:127], v244, s[44:47], s3 offen offset:1216\n"
"s_waitcnt vmcnt(7)\n"
"v_mfma_f32_16x16x32_bf16 v[4:7], v[68:71], v[0:3], v[160:163]\n"
"s_waitcnt vmcnt(6)\n"
"v_mfma_f32_16x16x32_bf16 v[4:7], v[76:79], v[32:35], v[4:7]\n"
"s_waitcnt vmcnt(5)\n"
"v_mfma_f32_16x16x32_bf16 v[4:7], v[84:87], v[64:67], v[4:7]\n"
"s_waitcnt vmcnt(4)\n"
"v_mfma_f32_16x16x32_bf16 v[4:7], v[92:95], v[96:99], v[4:7]\n"
"s_waitcnt vmcnt(3)\n"
"v_mfma_f32_16x16x32_bf16 v[36:39], v[100:103], v[0:3], v[160:163]\n"
"s_waitcnt vmcnt(2)\n"
"v_mfma_f32_16x16x32_bf16 v[36:39], v[108:111], v[32:35], v[36:39]\n"
"s_waitcnt vmcnt(1)\n"
"v_mfma_f32_16x16x32_bf16 v[36:39], v[116:119], v[64:67], v[36:39]\n"
"s_waitcnt vmcnt(0)\n"
"v_mfma_f32_16x16x32_bf16 v[36:39], v[124:127], v[96:99], v[36:39]\n"
"v_mfma_f32_16x16x32_bf16 v[12:15], v[68:71], v[8:11], v[164:167]\n"
"v_mfma_f32_16x16x32_bf16 v[12:15], v[76:79], v[40:43], v[12:15]\n"
"v_mfma_f32_16x16x32_bf16 v[12:15], v[84:87], v[72:75], v[12:15]\n"
"v_mfma_f32_16x16x32_bf16 v[12:15], v[92:95], v[104:107], v[12:15]\n"
"v_mfma_f32_16x16x32_bf16 v[44:47], v[100:103], v[8:11], v[164:167]\n"
"v_mfma_f32_16x16x32_bf16 v[44:47], v[108:111], v[40:43], v[44:47]\n"
"v_mfma_f32_16x16x32_bf16 v[44:47], v[116:119], v[72:75], v[44:47]\n"
"v_mfma_f32_16x16x32_bf16 v[44:47], v[124:127], v[104:107], v[44:47]\n"
"v_mfma_f32_16x16x32_bf16 v[20:23], v[68:71], v[16:19], v[168:171]\n"
"v_mfma_f32_16x16x32_bf16 v[20:23], v[76:79], v[48:51], v[20:23]\n"
"v_mfma_f32_16x16x32_bf16 v[20:23], v[84:87], v[80:83], v[20:23]\n"
"v_mfma_f32_16x16x32_bf16 v[20:23], v[92:95], v[112:115], v[20:23]\n"
"v_mfma_f32_16x16x32_bf16 v[52:55], v[100:103], v[16:19], v[168:171]\n"
"v_mfma_f32_16x16x32_bf16 v[52:55], v[108:111], v[48:51], v[52:55]\n"
"v_mfma_f32_16x16x32_bf16 v[52:55], v[116:119], v[80:83], v[52:55]\n"
"v_mfma_f32_16x16x32_bf16 v[52:55], v[124:127], v[112:115], v[52:55]\n"
"v_mfma_f32_16x16x32_bf16 v[28:31], v[68:71], v[24:27], v[172:175]\n"
"v_mfma_f32_16x16x32_bf16 v[28:31], v[76:79], v[56:59], v[28:31]\n"
"v_mfma_f32_16x16x32_bf16 v[28:31], v[84:87], v[88:91], v[28:31]\n"
"v_mfma_f32_16x16x32_bf16 v[28:31], v[92:95], v[120:123], v[28:31]\n"
"v_mfma_f32_16x16x32_bf16 v[60:63], v[100:103], v[24:27], v[172:175]\n"
"v_mfma_f32_16x16x32_bf16 v[60:63], v[108:111], v[56:59], v[60:63]\n"
"v_mfma_f32_16x16x32_bf16 v[60:63], v[116:119], v[88:91], v[60:63]\n"
"v_mfma_f32_16x16x32_bf16 v[60:63], v[124:127], v[120:123], v[60:63]\n"
"v_exp_f32 v4, v4\n"
"v_exp_f32 v5, v5\n"
"v_exp_f32 v6, v6\n"
"v_exp_f32 v7, v7\n"
"v_cvt_pk_bf16_f32 v4, v4, v5\n"
"v_cvt_pk_bf16_f32 v5, v6, v7\n"
"v_exp_f32 v36, v36\n"
"v_exp_f32 v37, v37\n"
"v_exp_f32 v38, v38\n"
"v_exp_f32 v39, v39\n"
"v_cvt_pk_bf16_f32 v6, v36, v37\n"
"v_cvt_pk_bf16_f32 v7, v38, v39\n"
"v_exp_f32 v12, v12\n"
"v_exp_f32 v13, v13\n"
"v_exp_f32 v14, v14\n"
"v_exp_f32 v15, v15\n"
"v_cvt_pk_bf16_f32 v12, v12, v13\n"
"v_cvt_pk_bf16_f32 v13, v14, v15\n"
"v_exp_f32 v44, v44\n"
"v_exp_f32 v45, v45\n"
"v_exp_f32 v46, v46\n"
"v_exp_f32 v47, v47\n"
"v_cvt_pk_bf16_f32 v14, v44, v45\n"
"v_cvt_pk_bf16_f32 v15, v46, v47\n"
"v_exp_f32 v20, v20\n"
"v_exp_f32 v21, v21\n"
"v_exp_f32 v22, v22\n"
"v_exp_f32 v23, v23\n"
"v_cvt_pk_bf16_f32 v20, v20, v21\n"
"v_cvt_pk_bf16_f32 v21, v22, v23\n"
"v_exp_f32 v52, v52\n"
"v_exp_f32 v53, v53\n"
"v_exp_f32 v54, v54\n"
"v_exp_f32 v55, v55\n"
"v_cvt_pk_bf16_f32 v22, v52, v53\n"
"v_cvt_pk_bf16_f32 v23, v54, v55\n"
"v_exp_f32 v28, v28\n"
"v_exp_f32 v29, v29\n"
"v_exp_f32 v30, v30\n"
"v_exp_f32 v31, v31\n"
"v_cvt_pk_bf16_f32 v28, v28, v29\n"
"v_cvt_pk_bf16_f32 v29, v30, v31\n"
"v_exp_f32 v60, v60\n"
"v_exp_f32 v61, v61\n"
"v_exp_f32 v62, v62\n"
"v_exp_f32 v63, v63\n"
"v_cvt_pk_bf16_f32 v30, v60, v61\n"
"v_cvt_pk_bf16_f32 v31, v62, v63\n"
"buffer_load_ushort v36, v245, s[48:51], s3 offen offset:0\n"
"buffer_load_ushort v37, v245, s[48:51], s3 offen offset:256\n"
"buffer_load_ushort v38, v245, s[48:51], s3 offen offset:512\n"
"buffer_load_ushort v39, v245, s[48:51], s3 offen offset:768\n"
"buffer_load_ushort v44, v245, s[48:51], s3 offen offset:1024\n"
"buffer_load_ushort v45, v245, s[48:51], s3 offen offset:1280\n"
"buffer_load_ushort v46, v245, s[48:51], s3 offen offset:1536\n"
"buffer_load_ushort v47, v245, s[48:51], s3 offen offset:1792\n"
"buffer_load_ushort v52, v245, s[48:51], s3 offen offset:8\n"
"buffer_load_ushort v53, v245, s[48:51], s3 offen offset:264\n"
"buffer_load_ushort v54, v245, s[48:51], s3 offen offset:520\n"
"buffer_load_ushort v55, v245, s[48:51], s3 offen offset:776\n"
"buffer_load_ushort v60, v245, s[48:51], s3 offen offset:1032\n"
"buffer_load_ushort v61, v245, s[48:51], s3 offen offset:1288\n"
"buffer_load_ushort v62, v245, s[48:51], s3 offen offset:1544\n"
"buffer_load_ushort v63, v245, s[48:51], s3 offen offset:1800\n"
"s_waitcnt vmcnt(14)\n"
"v_lshl_or_b32 v68, v37, 16, v36\n"
"s_waitcnt vmcnt(12)\n"
"v_lshl_or_b32 v69, v39, 16, v38\n"
"s_waitcnt vmcnt(10)\n"
"v_lshl_or_b32 v70, v45, 16, v44\n"
"s_waitcnt vmcnt(8)\n"
"v_lshl_or_b32 v71, v47, 16, v46\n"
"s_waitcnt vmcnt(6)\n"
"v_lshl_or_b32 v76, v53, 16, v52\n"
"s_waitcnt vmcnt(4)\n"
"v_lshl_or_b32 v77, v55, 16, v54\n"
"s_waitcnt vmcnt(2)\n"
"v_lshl_or_b32 v78, v61, 16, v60\n"
"s_waitcnt vmcnt(0)\n"
"v_lshl_or_b32 v79, v63, 16, v62\n"
"buffer_load_ushort v36, v245, s[48:51], s3 offen offset:64\n"
"buffer_load_ushort v37, v245, s[48:51], s3 offen offset:320\n"
"buffer_load_ushort v38, v245, s[48:51], s3 offen offset:576\n"
"buffer_load_ushort v39, v245, s[48:51], s3 offen offset:832\n"
"buffer_load_ushort v44, v245, s[48:51], s3 offen offset:1088\n"
"buffer_load_ushort v45, v245, s[48:51], s3 offen offset:1344\n"
"buffer_load_ushort v46, v245, s[48:51], s3 offen offset:1600\n"
"buffer_load_ushort v47, v245, s[48:51], s3 offen offset:1856\n"
"buffer_load_ushort v52, v245, s[48:51], s3 offen offset:72\n"
"buffer_load_ushort v53, v245, s[48:51], s3 offen offset:328\n"
"buffer_load_ushort v54, v245, s[48:51], s3 offen offset:584\n"
"buffer_load_ushort v55, v245, s[48:51], s3 offen offset:840\n"
"buffer_load_ushort v60, v245, s[48:51], s3 offen offset:1096\n"
"buffer_load_ushort v61, v245, s[48:51], s3 offen offset:1352\n"
"buffer_load_ushort v62, v245, s[48:51], s3 offen offset:1608\n"
"buffer_load_ushort v63, v245, s[48:51], s3 offen offset:1864\n"
"s_waitcnt vmcnt(14)\n"
"v_lshl_or_b32 v84, v37, 16, v36\n"
"s_waitcnt vmcnt(12)\n"
"v_lshl_or_b32 v85, v39, 16, v38\n"
"s_waitcnt vmcnt(10)\n"
"v_lshl_or_b32 v86, v45, 16, v44\n"
"s_waitcnt vmcnt(8)\n"
"v_lshl_or_b32 v87, v47, 16, v46\n"
"s_waitcnt vmcnt(6)\n"
"v_lshl_or_b32 v92, v53, 16, v52\n"
"s_waitcnt vmcnt(4)\n"
"v_lshl_or_b32 v93, v55, 16, v54\n"
"s_waitcnt vmcnt(2)\n"
"v_lshl_or_b32 v94, v61, 16, v60\n"
"s_waitcnt vmcnt(0)\n"
"v_lshl_or_b32 v95, v63, 16, v62\n"
"buffer_load_ushort v36, v245, s[48:51], s3 offen offset:128\n"
"buffer_load_ushort v37, v245, s[48:51], s3 offen offset:384\n"
"buffer_load_ushort v38, v245, s[48:51], s3 offen offset:640\n"
"buffer_load_ushort v39, v245, s[48:51], s3 offen offset:896\n"
"buffer_load_ushort v44, v245, s[48:51], s3 offen offset:1152\n"
"buffer_load_ushort v45, v245, s[48:51], s3 offen offset:1408\n"
"buffer_load_ushort v46, v245, s[48:51], s3 offen offset:1664\n"
"buffer_load_ushort v47, v245, s[48:51], s3 offen offset:1920\n"
"buffer_load_ushort v52, v245, s[48:51], s3 offen offset:136\n"
"buffer_load_ushort v53, v245, s[48:51], s3 offen offset:392\n"
"buffer_load_ushort v54, v245, s[48:51], s3 offen offset:648\n"
"buffer_load_ushort v55, v245, s[48:51], s3 offen offset:904\n"
"buffer_load_ushort v60, v245, s[48:51], s3 offen offset:1160\n"
"buffer_load_ushort v61, v245, s[48:51], s3 offen offset:1416\n"
"buffer_load_ushort v62, v245, s[48:51], s3 offen offset:1672\n"
"buffer_load_ushort v63, v245, s[48:51], s3 offen offset:1928\n"
"s_waitcnt vmcnt(14)\n"
"v_lshl_or_b32 v100, v37, 16, v36\n"
"s_waitcnt vmcnt(12)\n"
"v_lshl_or_b32 v101, v39, 16, v38\n"
"s_waitcnt vmcnt(10)\n"
"v_lshl_or_b32 v102, v45, 16, v44\n"
"s_waitcnt vmcnt(8)\n"
"v_lshl_or_b32 v103, v47, 16, v46\n"
"s_waitcnt vmcnt(6)\n"
"v_lshl_or_b32 v108, v53, 16, v52\n"
"s_waitcnt vmcnt(4)\n"
"v_lshl_or_b32 v109, v55, 16, v54\n"
"s_waitcnt vmcnt(2)\n"
"v_lshl_or_b32 v110, v61, 16, v60\n"
"s_waitcnt vmcnt(0)\n"
"v_lshl_or_b32 v111, v63, 16, v62\n"
"buffer_load_ushort v36, v245, s[48:51], s3 offen offset:192\n"
"buffer_load_ushort v37, v245, s[48:51], s3 offen offset:448\n"
"buffer_load_ushort v38, v245, s[48:51], s3 offen offset:704\n"
"buffer_load_ushort v39, v245, s[48:51], s3 offen offset:960\n"
"buffer_load_ushort v44, v245, s[48:51], s3 offen offset:1216\n"
"buffer_load_ushort v45, v245, s[48:51], s3 offen offset:1472\n"
"buffer_load_ushort v46, v245, s[48:51], s3 offen offset:1728\n"
"buffer_load_ushort v47, v245, s[48:51], s3 offen offset:1984\n"
"buffer_load_ushort v52, v245, s[48:51], s3 offen offset:200\n"
"buffer_load_ushort v53, v245, s[48:51], s3 offen offset:456\n"
"buffer_load_ushort v54, v245, s[48:51], s3 offen offset:712\n"
"buffer_load_ushort v55, v245, s[48:51], s3 offen offset:968\n"
"buffer_load_ushort v60, v245, s[48:51], s3 offen offset:1224\n"
"buffer_load_ushort v61, v245, s[48:51], s3 offen offset:1480\n"
"buffer_load_ushort v62, v245, s[48:51], s3 offen offset:1736\n"
"buffer_load_ushort v63, v245, s[48:51], s3 offen offset:1992\n"
"s_waitcnt vmcnt(14)\n"
"v_lshl_or_b32 v116, v37, 16, v36\n"
"s_waitcnt vmcnt(12)\n"
"v_lshl_or_b32 v117, v39, 16, v38\n"
"s_waitcnt vmcnt(10)\n"
"v_lshl_or_b32 v118, v45, 16, v44\n"
"s_waitcnt vmcnt(8)\n"
"v_lshl_or_b32 v119, v47, 16, v46\n"
"s_waitcnt vmcnt(6)\n"
"v_lshl_or_b32 v124, v53, 16, v52\n"
"s_waitcnt vmcnt(4)\n"
"v_lshl_or_b32 v125, v55, 16, v54\n"
"s_waitcnt vmcnt(2)\n"
"v_lshl_or_b32 v126, v61, 16, v60\n"
"s_waitcnt vmcnt(0)\n"
"v_lshl_or_b32 v127, v63, 16, v62\n"
"v_mfma_f32_16x16x32_bf16 a[128:131], a[144:147], v[4:7], a[128:131]\n"
"v_mfma_f32_16x16x32_bf16 a[0:3], v[68:71], v[4:7], a[0:3]\n"
"v_mfma_f32_16x16x32_bf16 a[16:19], v[76:79], v[4:7], a[16:19]\n"
"v_mfma_f32_16x16x32_bf16 a[32:35], v[84:87], v[4:7], a[32:35]\n"
"v_mfma_f32_16x16x32_bf16 a[48:51], v[92:95], v[4:7], a[48:51]\n"
"v_mfma_f32_16x16x32_bf16 a[64:67], v[100:103], v[4:7], a[64:67]\n"
"v_mfma_f32_16x16x32_bf16 a[80:83], v[108:111], v[4:7], a[80:83]\n"
"v_mfma_f32_16x16x32_bf16 a[96:99], v[116:119], v[4:7], a[96:99]\n"
"v_mfma_f32_16x16x32_bf16 a[112:115], v[124:127], v[4:7], a[112:115]\n"
"v_mfma_f32_16x16x32_bf16 a[132:135], a[144:147], v[12:15], a[132:135]\n"
"v_mfma_f32_16x16x32_bf16 a[4:7], v[68:71], v[12:15], a[4:7]\n"
"v_mfma_f32_16x16x32_bf16 a[20:23], v[76:79], v[12:15], a[20:23]\n"
"v_mfma_f32_16x16x32_bf16 a[36:39], v[84:87], v[12:15], a[36:39]\n"
"v_mfma_f32_16x16x32_bf16 a[52:55], v[92:95], v[12:15], a[52:55]\n"
"v_mfma_f32_16x16x32_bf16 a[68:71], v[100:103], v[12:15], a[68:71]\n"
"v_mfma_f32_16x16x32_bf16 a[84:87], v[108:111], v[12:15], a[84:87]\n"
"v_mfma_f32_16x16x32_bf16 a[100:103], v[116:119], v[12:15], a[100:103]\n"
"v_mfma_f32_16x16x32_bf16 a[116:119], v[124:127], v[12:15], a[116:119]\n"
"v_mfma_f32_16x16x32_bf16 a[136:139], a[144:147], v[20:23], a[136:139]\n"
"v_mfma_f32_16x16x32_bf16 a[8:11], v[68:71], v[20:23], a[8:11]\n"
"v_mfma_f32_16x16x32_bf16 a[24:27], v[76:79], v[20:23], a[24:27]\n"
"v_mfma_f32_16x16x32_bf16 a[40:43], v[84:87], v[20:23], a[40:43]\n"
"v_mfma_f32_16x16x32_bf16 a[56:59], v[92:95], v[20:23], a[56:59]\n"
"v_mfma_f32_16x16x32_bf16 a[72:75], v[100:103], v[20:23], a[72:75]\n"
"v_mfma_f32_16x16x32_bf16 a[88:91], v[108:111], v[20:23], a[88:91]\n"
"v_mfma_f32_16x16x32_bf16 a[104:107], v[116:119], v[20:23], a[104:107]\n"
"v_mfma_f32_16x16x32_bf16 a[120:123], v[124:127], v[20:23], a[120:123]\n"
"v_mfma_f32_16x16x32_bf16 a[140:143], a[144:147], v[28:31], a[140:143]\n"
"v_mfma_f32_16x16x32_bf16 a[12:15], v[68:71], v[28:31], a[12:15]\n"
"v_mfma_f32_16x16x32_bf16 a[28:31], v[76:79], v[28:31], a[28:31]\n"
"v_mfma_f32_16x16x32_bf16 a[44:47], v[84:87], v[28:31], a[44:47]\n"
"v_mfma_f32_16x16x32_bf16 a[60:63], v[92:95], v[28:31], a[60:63]\n"
"v_mfma_f32_16x16x32_bf16 a[76:79], v[100:103], v[28:31], a[76:79]\n"
"v_mfma_f32_16x16x32_bf16 a[92:95], v[108:111], v[28:31], a[92:95]\n"
"v_mfma_f32_16x16x32_bf16 a[108:111], v[116:119], v[28:31], a[108:111]\n"
"v_mfma_f32_16x16x32_bf16 a[124:127], v[124:127], v[28:31], a[124:127]\n"
"s_waitcnt vmcnt(0) lgkmcnt(0)\n"
"s_nop 15\n"
"s_add_u32 s3, s3, 8192\n"
"s_cmp_lt_u32 s3, 131072\n"
"s_cbranch_scc1 L_sl2_%=\n"
"s_nop 7\n"
"v_accvgpr_read_b32 v234, a128\n"
"v_accvgpr_read_b32 v235, a132\n"
"v_accvgpr_read_b32 v236, a136\n"
"v_accvgpr_read_b32 v237, a140\n"
"s_waitcnt vmcnt(0) lgkmcnt(0)\n"
"s_nop 15\n"
"s_branch L_slowret_%=\n"
"L_spinR_%=:\n"
"s_mov_b32 s2, 400000\n"
"L_spinlR_%=:\n"
"s_sleep 4\n"
"buffer_load_dwordx4 v[240:243], v216, s[60:63], s3 offen sc1\n"
"buffer_load_dwordx4 v[244:247], v216, s[60:63], s3 offen offset:1024 sc1\n"
"s_waitcnt vmcnt(0)\n"
"v_cmp_eq_u64_e64 s[4:5], v[240:241], s[80:81]\n"
"v_cmp_eq_u64_e64 s[6:7], v[242:243], s[80:81]\n"
"s_nop 1\n"
"s_and_b64 s[4:5], s[4:5], s[6:7]\n"
"v_cmp_eq_u64_e64 s[6:7], v[244:245], s[80:81]\n"
"s_nop 1\n"
"s_and_b64 s[4:5], s[4:5], s[6:7]\n"
"v_cmp_eq_u64_e64 s[6:7], v[246:247], s[80:81]\n"
"s_nop 1\n"
"s_and_b64 s[4:5], s[4:5], s[6:7]\n"
"s_cmp_eq_u64 s[4:5], -1\n"
"s_cbranch_scc1 L_retR_%=\n"
"s_sub_u32 s2, s2, 1\n"
"s_cmp_lg_u32 s2, 0\n"
"s_cbranch_scc1 L_spinlR_%=\n"
"s_branch L_retR_%=\n"
"L_spinO_%=:\n"
"s_mov_b32 s2, 400000\n"
"L_spinlO_%=:\n"
"s_sleep 4\n"
"buffer_load_dwordx2 v[240:241], v239, s[60:63], s3 offen sc1\n"
"s_waitcnt vmcnt(0)\n"
"v_cmp_eq_u64_e64 s[4:5], v[240:241], s[80:81]\n"
"s_nop 1\n"
"s_cmp_eq_u64 s[4:5], -1\n"
"s_cbranch_scc1 L_retO_%=\n"
"s_sub_u32 s2, s2, 1\n"
"s_cmp_lg_u32 s2, 0\n"
"s_cbranch_scc1 L_spinlO_%=\n"
"s_branch L_retO_%=\n"
"L_spinH_%=:\n"
"s_mov_b32 s2, 400000\n"
"L_spinlH_%=:\n"
"s_sleep 4\n"
"buffer_load_dwordx2 v[240:241], v239, s[60:63], s3 offen sc1\n"
"s_waitcnt vmcnt(0)\n"
"v_cmp_eq_u64_e64 s[4:5], v[240:241], s[80:81]\n"
"s_nop 1\n"
"s_cmp_eq_u64 s[4:5], -1\n"
"s_cbranch_scc1 L_retH_%=\n"
"s_sub_u32 s2, s2, 1\n"
"s_cmp_lg_u32 s2, 0\n"
"s_cbranch_scc1 L_spinlH_%=\n"
"s_branch L_retH_%=\n"
"L_spinS_%=:\n"
"s_mov_b32 s2, 400000\n"
"L_spinlS_%=:\n"
"s_sleep 4\n"
"buffer_load_dwordx2 v[240:241], v239, s[60:63], s3 offen sc1\n"
"s_waitcnt vmcnt(0)\n"
"v_cmp_eq_u64_e64 s[4:5], v[240:241], s[80:81]\n"
"s_nop 1\n"
"s_cmp_eq_u64 s[4:5], -1\n"
"s_cbranch_scc1 L_retS_%=\n"
"s_sub_u32 s2, s2, 1\n"
"s_cmp_lg_u32 s2, 0\n"
"s_cbranch_scc1 L_spinlS_%=\n"
"s_branch L_retS_%=\n"
"L_end_%=:\n"
  :: "s"(__builtin_amdgcn_kernarg_segment_ptr()), "s"(blockIdx.x), "v"(threadIdx.x), "s"((unsigned)(size_t)smem)
  : "v1","v2","v3","v4","v5","v6","v7","v8","v9","v10","v11","v12","v13","v14","v15","v16","v17","v18","v19","v20","v21","v22","v23","v24","v25","v26","v27","v28","v29","v30","v31","v32","v33","v34","v35","v36","v37","v38","v39","v40","v41","v42","v43","v44","v45","v46","v47","v48","v49","v50","v51","v52","v53","v54","v55","v56","v57","v58","v59","v60","v61","v62","v63","v64","v65","v66","v67","v68","v69","v70","v71","v72","v73","v74","v75","v76","v77","v78","v79","v80","v81","v82","v83","v84","v85","v86","v87","v88","v89","v90","v91","v92","v93","v94","v95","v96","v97","v98","v99","v100","v101","v102","v103","v104","v105","v106","v107","v108","v109","v110","v111","v112","v113","v114","v115","v116","v117","v118","v119","v120","v121","v122","v123","v124","v125","v126","v127","v128","v129","v130","v131","v132","v133","v134","v135","v136","v137","v138","v139","v140","v141","v142","v143","v144","v145","v146","v147","v148","v149","v150","v151","v152","v153","v154","v155","v156","v157","v158","v159","v160","v161","v162","v163","v164","v165","v166","v167","v168","v169","v170","v171","v172","v173","v174","v175","v176","v177","v178","v179","v180","v181","v182","v183","v184","v185","v186","v187","v188","v189","v190","v191","v192","v193","v194","v195","v196","v197","v198","v199","v200","v201","v202","v203","v204","v205","v206","v207","v208","v209","v210","v211","v212","v213","v214","v215","v216","v217","v218","v219","v220","v221","v222","v223","v224","v225","v226","v227","v228","v229","v230","v231","v232","v233","v234","v235","v236","v237","v238","v239","v240","v241","v242","v243","v244","v245","v246","v247","v248","v249","v250","v251","v252","v253","v254","v255","a0","a1","a2","a3","a4","a5","a6","a7","a8","a9","a10","a11","a12","a13","a14","a15","a16","a17","a18","a19","a20","a21","a22","a23","a24","a25","a26","a27","a28","a29","a30","a31","a32","a33","a34","a35","a36","a37","a38","a39","a40","a41","a42","a43","a44","a45","a46","a47","a48","a49","a50","a51","a52","a53","a54","a55","a56","a57","a58","a59","a60","a61","a62","a63","a64","a65","a66","a67","a68","a69","a70","a71","a72","a73","a74","a75","a76","a77","a78","a79","a80","a81","a82","a83","a84","a85","a86","a87","a88","a89","a90","a91","a92","a93","a94","a95","a96","a97","a98","a99","a100","a101","a102","a103","a104","a105","a106","a107","a108","a109","a110","a111","a112","a113","a114","a115","a116","a117","a118","a119","a120","a121","a122","a123","a124","a125","a126","a127","a128","a129","a130","a131","a132","a133","a134","a135","a136","a137","a138","a139","a140","a141","a142","a143","a144","a145","a146","a147","a148","a149","a150","a151","a152","a153","a154","a155","a156","a157","a158","a159","a160","a161","a162","a163","a164","a165","a166","a167","a168","a169","a170","a171","a172","a173","a174","a175","a176","a177","a178","a179","a180","a181","a182","a183","a184","a185","a186","a187","a188","a189","a190","a191","a192","a193","a194","a195","a196","a197","a198","a199","a200","a201","a202","a203","a204","a205","a206","a207","a208","a209","a210","a211","a212","a213","a214","a215","a216","a217","a218","a219","a220","a221","a222","a223","a224","a225","a226","a227","a228","a229","a230","a231","a232","a233","a234","a235","a236","a237","a238","a239","a240","a241","a242","a243","a244","a245","a246","a247","a248","a249","a250","a251","a252","a253","a254","a255","s28","s29","s30","s31","s32","s33","s34","s35","s36","s37","s38","s39","s40","s41","s42","s43","s44","s45","s46","s47","s48","s49","s50","s51","s52","s53","s54","s55","s56","s57","s58","s59","s60","s61","s62","s63","s64","s65","s66","s67","s68","s69","s70","s71","s72","s73","s74","s75","s76","s77","s78","s79","s80","s81","s82","s83","s84","s85","s86","s87","s88","s89","s90","s91","s92","s93","s94","s95","s96","s97","s98","s99","vcc","scc","memory");
}

extern "C" void kernel_launch(void* const* d_in, const int* in_sizes, int n_in, void* d_out, int out_size, void* d_ws, size_t ws_size,
                              hipStream_t stream) {
  if (ws_size < (size_t)43909120) return;
  fused_kernel<<<256, 256, 0, stream>>>(d_in[0], d_in[1], d_in[2], d_in[3], d_in[4], d_in[6], d_in[7], d_in[8], d_ws, d_out);
}
```
